```python
import torch

def _softmax(x, axis=-1):
    return torch.softmax(x, dim=axis)
def _gelu(x):
    return torch.nn.functional.gelu(x, approximate="tanh")
def _silu(x):
    return torch.nn.functional.silu(x)
def _sigmoid(x):
    return torch.sigmoid(x)
def _layernorm(x, axis=-1, eps=1e-5):
    m = x.mean(dim=axis, keepdim=True)
    v = x.var(dim=axis, keepdim=True, unbiased=False)
    return (x - m) / torch.sqrt(v + eps)
def _scatter_add(tgt, idx, src, axis=0):
    return tgt.clone().index_add_(axis, idx, src)
def _segment_sum(src, seg, n):
    out = torch.zeros((n,) + src.shape[1:], dtype=src.dtype)
    return out.index_add_(0, seg, src)
def _pad(x, pw):
    flat = tuple(v for pair in reversed(pw) for v in pair)
    return torch.nn.functional.pad(x, flat)

kernel_name = 'gated_mix__grouped_kv_mqa__ssm_conv_h3__prompt'

def setup_inputs():

    _w = (lambda a: (lambda t: t.to(torch.bfloat16) if t.is_floating_point() else t)(torch.as_tensor(a)))
    def _A_setup():
        B, S, D, H = 1, 2048, 576, 32
        Dh = D // H
        return {
            "x": _w((lambda *s: torch.randn(*s, dtype=torch.bfloat16) * 0.05)(B, S, D)),
            "wq": _w((lambda *s: torch.randn(*s, dtype=torch.bfloat16) * 0.8)(D, H * Dh)),
            "wk": _w((lambda *s: torch.randn(*s, dtype=torch.bfloat16) * 0.8)(D, Dh)),
            "wv": _w((lambda *s: torch.randn(*s, dtype=torch.bfloat16) * 0.05)(D, Dh)),
            "wo": _w((lambda *s: torch.randn(*s, dtype=torch.bfloat16) * 0.05)(H * Dh, D)),
        }
    _A_in = _A_setup()

    def _B_setup():
        B, S, D = 1, 2048, 576
        ks = 4
        return {
            "q": _w((lambda *s: torch.randn(*s, dtype=torch.bfloat16) * 0.05)(B, S, D)),
            "k": _w((lambda *s: torch.randn(*s, dtype=torch.bfloat16) * 0.4)(B, S, D)),
            "v": _w((lambda *s: torch.randn(*s, dtype=torch.bfloat16) * 0.2)(B, S, D)),
            "shift_k": _w((lambda *s: torch.randn(*s, dtype=torch.bfloat16) * 0.4)(D, ks) * 0.5),
            "ssm_k": _w((lambda *s: torch.randn(*s, dtype=torch.bfloat16) * 0.4)(D, S) * (1.0 / S ** 0.5)),
            "ssm_D": _w((lambda *s: torch.randn(*s, dtype=torch.bfloat16) * 0.2)(D)),
        }
    _B_in = _B_setup()

    return {
        "A_x": _A_in["x"],
        "A_wq": _A_in["wq"],
        "A_wk": _A_in["wk"],
        "A_wv": _A_in["wv"],
        "A_wo": _A_in["wo"],
        "B_q": _B_in["q"],
        "B_k": _B_in["k"],
        "B_v": _B_in["v"],
        "B_shift_k": _B_in["shift_k"],
        "B_ssm_k": _B_in["ssm_k"],
        "B_ssm_D": _B_in["ssm_D"],
        "gate_w": _w((lambda *s: torch.randn(*s, dtype=torch.bfloat16) * 0.05)(576) * 16.0),
    }

def reference(A_x, A_wq, A_wk, A_wv, A_wo, B_q, B_k, B_v, B_shift_k, B_ssm_k, B_ssm_D, gate_w):
    def _A_ref(x, wq, wk, wv, wo):
        B, S, D = x.shape
        H = 32
        Dh = D // H
        q = (lambda x, *d: x.permute(*d))((x @ wq).reshape(B, S, H, Dh), 0, 2, 1, 3)
        k = x @ wk
        v = x @ wv
        scores = torch.einsum("bhqd,bkd->bhqk", q, k) * (Dh ** -0.5)
        p = _softmax(scores, axis=-1)
        o = torch.einsum("bhqk,bkd->bhqd", p, v)
        return (lambda x, *d: x.permute(*d))(o, 0, 2, 1, 3).reshape(B, S, H * Dh) @ wo

    def _B_ref(q, k, v, shift_k, ssm_k, ssm_D):
        B, S, D = q.shape
        ks = shift_k.shape[-1]
        k_pad = _pad(k, ((0, 0), (ks - 1, 0), (0, 0)))
        k_shift = torch.zeros_like(k)
        for t in range(ks):
            k_shift = k_shift + k_pad[:, t:t + S, :] * shift_k[:, ks - 1 - t][None, None, :]
        kv = k_shift * v
        L = 2 * S
        kv_pad = _pad(kv, ((0, 0), (0, S), (0, 0)))
        kf_pad = _pad(ssm_k, ((0, 0), (0, S)))
        kv_f = (lambda x, axes=(-2,-1): torch.fft.rfft2(x.float(), dim=axes))(kv_pad[:, None, :, :], axes=(1, 2))
        kf_f = (lambda x, axes=(-2,-1): torch.fft.rfft2(x.float(), dim=axes))(kf_pad[:, None, :], axes=(1, 2))
        kf_f = (lambda x, *d: x.permute(*d))(kf_f, 1, 2, 0)
        y_f = kv_f * kf_f[None, :, :, :]
        y = (lambda x, s, axes=(-2,-1): torch.fft.irfft2(x, s=s, dim=axes).to(torch.bfloat16))(y_f, (1, L), axes=(1, 2))[:, 0, :S, :]
        y = y + kv * ssm_D[None, None, :]
        return q * y

    x = A_x
    g = _sigmoid((x * gate_w).sum(-1, keepdims=True))
    a = _A_ref(x, A_wq, A_wk, A_wv, A_wo)
    b = _B_ref(x, B_k, B_v, B_shift_k, B_ssm_k, B_ssm_D)
    return g * a + (1 - g) * b
```

```cpp
#include <hip/hip_runtime.h>
#include <hip/hip_bf16.h>
#include <stdint.h>

typedef __attribute__((ext_vector_type(4))) __bf16 bf16x4;
typedef __attribute__((ext_vector_type(8))) __bf16 bf16x8;
typedef __attribute__((ext_vector_type(16))) __bf16 bf16x16;
typedef __attribute__((ext_vector_type(4))) short s16x4;
typedef __attribute__((ext_vector_type(4))) float f32x4;
typedef __attribute__((ext_vector_type(8))) float f32x8;
typedef __attribute__((ext_vector_type(16))) float f32x16;
typedef __attribute__((ext_vector_type(2))) unsigned u32x2;
typedef __attribute__((ext_vector_type(4))) unsigned u32x4;
typedef unsigned short u16;
typedef __attribute__((ext_vector_type(4), aligned(4))) unsigned u32x4_a4;
typedef __attribute__((ext_vector_type(4), aligned(2))) unsigned u32x4_a2;
typedef __attribute__((ext_vector_type(2), aligned(4))) unsigned u32x2_a4;

#define LDS_AS __attribute__((address_space(3)))
#define GLB_AS __attribute__((address_space(1)))

constexpr int S_LEN = 2048, DM = 576, NH = 32, DH = 18;
constexpr int UR_STRIDE = 2080;
constexpr int SG_K1 = 0, SG_K2 = 4096, SG_V = 4624, SG_BYTES = 9744;
constexpr int IMG_PIECES = 156;
constexpr int IMG_BYTES = IMG_PIECES * 1024;

constexpr size_t WS_Q = 0;
constexpr size_t WS_IMG = WS_Q + (size_t)S_LEN * DM * 2;
constexpr size_t WS_UR = WS_IMG + IMG_BYTES;
constexpr size_t UR_COPY = (size_t)DM * UR_STRIDE;
constexpr size_t WS_G = WS_UR + 2 * UR_COPY * 2;
constexpr size_t WS_O = WS_G + S_LEN * 4;
constexpr size_t WS_Z = WS_O + (size_t)S_LEN * DM * 2;

#define STAMP_DECL do {} while (0)
#define STAMP(i) do {} while (0)
#define STAMP_FLUSH(kern) do {} while (0)
#define PRIO_X 1
#define PRIO_A 2
#define PRIO_B 0
#define RAW_BARRIER() do { asm volatile("s_waitcnt lgkmcnt(0)" ::: "memory"); __builtin_amdgcn_s_barrier(); asm volatile("" ::: "memory"); } while (0)
template <int N> __device__ __forceinline__ void wait_vm() { asm volatile("s_waitcnt vmcnt(%0)" :: "i"(N) : "memory"); }

__device__ __forceinline__ float bf2f(unsigned h) { return __uint_as_float(h << 16); }
__device__ __forceinline__ float bflo(unsigned w) { return __uint_as_float(w << 16); }
__device__ __forceinline__ float bfhi(unsigned w) { return __uint_as_float(w & 0xffff0000u); }
__device__ __forceinline__ unsigned pack2(float lo, float hi) {
  typedef __attribute__((ext_vector_type(2))) __bf16 bf16x2;
  bf16x2 v; v[0] = (__bf16)lo; v[1] = (__bf16)hi;
  return __builtin_bit_cast(unsigned, v);
}
__device__ __forceinline__ float rbf(float x) { return (float)(__bf16)x; }

__device__ __forceinline__ void glds16(const void* g, void* lds) {
  __builtin_amdgcn_global_load_lds((const GLB_AS unsigned*)g, (LDS_AS unsigned*)lds, 16, 0, 0);
}
__device__ __forceinline__ bf16x4 tr_read(const char* p) {
  return __builtin_amdgcn_ds_read_tr16_b64_v4bf16((LDS_AS bf16x4*)p);
}
__device__ __forceinline__ int vpos(int d) {
  return d <= 10 ? d : (d == 11 ? 19 : (d <= 15 ? d - 1 : (d <= 18 ? d : 15)));
}

__device__ __forceinline__ int wsw(int k) { return ((k >> 1) & 1) | ((k >> 2) & 2); }

template <int NLW, int APC, int WPC, int NSLOT>
__device__ __forceinline__ void gemm_loader(const u16* __restrict__ A, int row0, const u16* __restrict__ W, int n0,
                                            char* ring, int lw, int lane) {
  constexpr int PPC = APC + WPC, PL = PPC / NLW, SLOT = PPC * 1024;
  static_assert(PPC % NLW == 0, "pieces must split evenly");
  unsigned off[PL];
  const char* base[PL];
  int step[PL];
#pragma unroll
  for (int i = 0; i < PL; ++i) {
    const int p = lw * PL + i;
    const int r8 = lane >> 3, c = lane & 7;
    if (p < APC) {
      const int row = p * 8 + r8;
      int gr = row0 + row; gr = gr > S_LEN - 1 ? S_LEN - 1 : gr;
      off[i] = (unsigned)gr * (DM * 2) + ((c ^ ((row >> 1) & 7)) << 4);
      base[i] = (const char*)A; step[i] = 128;
    } else {
      const int k = (p - APC) * 8 + r8;
      off[i] = (unsigned)k * (DM * 2) + ((c ^ (wsw(k) << 1)) << 4);
      base[i] = (const char*)(W + n0); step[i] = 64 * DM * 2;
    }
  }
  auto issue = [&](int ch) {
#pragma unroll
    for (int i = 0; i < PL; ++i) glds16(base[i] + ch * step[i] + off[i], ring + (ch % NSLOT) * SLOT + (lw * PL + i) * 1024);
  };
#pragma unroll
  for (int ch = 0; ch < NSLOT; ++ch) issue(ch);
#define GL_STEP(c) do { constexpr int iss = (NSLOT + ((c) > 0 ? (c) - 1 : 0)) < 9 ? (NSLOT + ((c) > 0 ? (c) - 1 : 0)) : 9; \
    wait_vm<PL * (iss - (c) - 1)>(); __builtin_amdgcn_s_barrier(); asm volatile("" ::: "memory"); \
    if ((c) >= 1 && (c) - 1 + NSLOT < 9) issue((c) - 1 + NSLOT); } while (0)
  GL_STEP(0); GL_STEP(1); GL_STEP(2); GL_STEP(3); GL_STEP(4); GL_STEP(5); GL_STEP(6); GL_STEP(7); GL_STEP(8);
#undef GL_STEP
}

__device__ __forceinline__ bf16x8 a_frag(const char* sA, int lane, int cw, int s) {
  const int row = cw * 16 + (lane & 15);
  return *(const bf16x8*)(sA + row * 128 + (((s * 4 + (lane >> 4)) ^ ((row >> 1) & 7)) << 4));
}
__device__ __forceinline__ bf16x8 w_frag(const char* sW, int lane, int s, int nb) {
  const int g = lane >> 4, q = (lane & 15) >> 2, p = lane & 3;
  const int k = s * 32 + g * 8 + q;
  const char* b = sW + k * 128 + ((nb ^ wsw(k)) << 5) + p * 8;
  bf16x4 lo = tr_read(b);
  bf16x4 hi = tr_read(b + 512);
  return __builtin_shufflevector(lo, hi, 0, 1, 2, 3, 4, 5, 6, 7);
}
#define MFMA16(a, b, c) __builtin_amdgcn_mfma_f32_16x16x32_bf16(a, b, c, 0, 0, 0)

constexpr int K1_LDS = 156 * 1024;
__global__ void __launch_bounds__(640) k1_proj(const u16* __restrict__ x, const u16* __restrict__ wq,
                                               const u16* __restrict__ wk, const u16* __restrict__ wv,
                                               const u16* __restrict__ gw, const u16* __restrict__ bk,
                                               const u16* __restrict__ bv, const u16* __restrict__ shk,
                                               char* __restrict__ ws) {
  __shared__ __attribute__((aligned(16))) char sm[K1_LDS];
  const int tid = threadIdx.x, lane = tid & 63;
  const int wave = __builtin_amdgcn_readfirstlane(tid >> 6);
  const int id = (blockIdx.x & 7) * 32 + (blockIdx.x >> 3);
  const int g = lane >> 4, tl = lane & 15;
  STAMP_DECL;
  STAMP(0);
  const int grp = id >> 5, loc = id & 31;
  const int nkv = grp < 6 ? 3 : 2;
  if (loc < 32 - nkv) {
    const int qid = grp * 29 + (grp > 6 ? grp - 6 : 0) + loc;
    const int mt = qid / 9, nt = qid - mt * 9;
    const int t0 = mt * 80, n0 = nt * 64;
    constexpr int SLOT = 18 * 1024, NSLOT = 7;
    char* sU = sm + NSLOT * SLOT;
    constexpr int UP = 164, UT = 64 * UP;
    if (wave < 3) {
      gemm_loader<3, 10, 8, NSLOT>(x, t0, wq, n0, sm, wave, lane);
    } else if (wave < 8) {
      const int cw = wave - 3;
      const int kid = tid - 192, cg = kid & 7, st = kid >> 3;
      const int c0 = n0 + cg * 8, ts = t0 + st * 2;
      u32x4 kr[6], vr[3], sk[4];
#pragma unroll
      for (int i = 0; i < 6; ++i) {
        int t = ts - 4 + i; t = t < 0 ? 0 : (t > S_LEN - 1 ? S_LEN - 1 : t);
        kr[i] = *(const u32x4*)(bk + (size_t)t * DM + c0);
      }
#pragma unroll
      for (int i = 0; i < 3; ++i) {
        int t = ts - 1 + i; t = t < 0 ? 0 : (t > S_LEN - 1 ? S_LEN - 1 : t);
        vr[i] = *(const u32x4*)(bv + (size_t)t * DM + c0);
      }
#pragma unroll
      for (int i = 0; i < 4; ++i) sk[i] = *(const u32x4*)(shk + c0 * 4 + i * 8);
      f32x4 acc[4];
#pragma unroll
      for (int nb = 0; nb < 4; ++nb) acc[nb] = f32x4{0.f, 0.f, 0.f, 0.f};
#pragma unroll 1
      for (int ch = 0; ch < 9; ++ch) {
        RAW_BARRIER();
        if (ch == 0) STAMP(1);
        if (ch == 1) {
          if (ts < 4) {
#pragma unroll
            for (int i = 0; i < 4; ++i) if (ts - 4 + i < 0) kr[i] = u32x4{0u, 0u, 0u, 0u};
          }
#pragma unroll
          for (int c = 0; c < 8; ++c) {
            const unsigned w01 = sk[c >> 1][(c & 1) * 2], w23 = sk[c >> 1][(c & 1) * 2 + 1];
            const float w0 = bflo(w01), w1 = bfhi(w01), w2 = bflo(w23), w3 = bfhi(w23);
            float kk[6], o[3];
#pragma unroll
            for (int i = 0; i < 6; ++i) kk[i] = (c & 1) ? bfhi(kr[i][c >> 1]) : bflo(kr[i][c >> 1]);
#pragma unroll
            for (int i = 0; i < 3; ++i) {
              const float a = kk[i] * w3 + kk[i + 1] * w2 + kk[i + 2] * w1 + kk[i + 3] * w0;
              o[i] = a * ((c & 1) ? bfhi(vr[i][c >> 1]) : bflo(vr[i][c >> 1]));
            }
            char* d = sU + (cg * 8 + c) * UP + (78 - st * 2) * 2;
            *(unsigned*)d = pack2(o[2], o[1]);
            *(unsigned*)(d + UT) = pack2(o[1], o[0]);
          }
          STAMP(2);
        }
        const char* sl = sm + (ch >= NSLOT ? ch - NSLOT : ch) * SLOT;
#pragma unroll
        for (int s = 0; s < 2; ++s) {
          const bf16x8 af = a_frag(sl, lane, cw, s);
#pragma unroll
          for (int nb = 0; nb < 4; ++nb) acc[nb] = MFMA16(w_frag(sl + 10240, lane, s, nb), af, acc[nb]);
        }
      }
      STAMP(3);
      const float qscale = 0.23570226039551584f * 1.4426950408889634f;
      char* sE = sm + 2 * SLOT + cw * 2304;
#pragma unroll
      for (int nb = 0; nb < 4; ++nb)
        *(u32x2*)(sE + tl * 144 + nb * 32 + g * 8) =
            u32x2{pack2(acc[nb][0] * qscale, acc[nb][1] * qscale), pack2(acc[nb][2] * qscale, acc[nb][3] * qscale)};
      u16* Q = (u16*)(ws + WS_Q);
      if (t0 + cw * 16 < S_LEN) {
#pragma unroll
        for (int i = 0; i < 2; ++i) {
          const int row = i * 8 + (lane >> 3);
          *(u32x4*)(Q + (size_t)(t0 + cw * 16 + row) * DM + n0 + (lane & 7) * 8) = *(const u32x4*)(sE + row * 144 + (lane & 7) * 16);
        }
      }
    } else {
      const int cp = wave - 8;
      u16* ur = (u16*)(ws + WS_UR) + cp * UR_COPY;
      if (mt == 0) {
        u16* d = ur + (size_t)(n0 + lane) * UR_STRIDE + 2048;
#pragma unroll
        for (int i = 0; i < 4; ++i) *(u32x4*)(d + i * 8) = u32x4{0u, 0u, 0u, 0u};
      }
      RAW_BARRIER(); RAW_BARRIER(); RAW_BARRIER();
      const int p0 = 1968 - t0;
#pragma unroll
      for (int i = 0; i < 10; ++i) {
        const int ci = i * 64 + lane, c = ci / 10, k = ci - c * 10;
        const char* sp = sU + cp * UT + c * UP + k * 16;
        const u32x4 v = {*(const unsigned*)sp, *(const unsigned*)(sp + 4), *(const unsigned*)(sp + 8), *(const unsigned*)(sp + 12)};
        if (p0 + k * 8 >= 0) *(u32x4*)(ur + (size_t)(n0 + c) * UR_STRIDE + p0 + k * 8) = v;
      }
    }
  } else {
    const int mt = grp * 3 - (grp > 6 ? grp - 6 : 0) + (loc - (32 - nkv));
    const int t0 = mt * 96;
    constexpr int SLOT = 12 * 1024, NSLOT = 7;
    char* sW = sm + NSLOT * SLOT;
    if (wave < 4) {
      gemm_loader<4, 12, 0, NSLOT>(x, t0, wq, 0, sm, wave, lane);
    } else {
      const int cw = wave - 4;
      for (int k = tid - 256; k < 576; k += 384) {
        const u32x4 k0 = *(const u32x4_a4*)(wk + k * DH), k1 = *(const u32x4_a4*)(wk + k * DH + 8);
        const u32x4 v0 = *(const u32x4_a4*)(wv + k * DH), v1 = *(const u32x4_a4*)(wv + k * DH + 8);
        const unsigned k2 = *(const unsigned*)(wk + k * DH + 16), v2 = *(const unsigned*)(wv + k * DH + 16);
        const unsigned gk = gw[k];
        char* d = sW + k * 128;
        const int sw = wsw(k);
        *(u32x4*)(d + ((0 ^ sw) << 5)) = k0; *(u32x4*)(d + ((0 ^ sw) << 5) + 16) = k1;
        *(u32x4*)(d + ((1 ^ sw) << 5)) = v0; *(u32x4*)(d + ((1 ^ sw) << 5) + 16) = v1;
        *(u32x4*)(d + ((2 ^ sw) << 5)) = u32x4{k2, gk, v2, 0u}; *(u32x4*)(d + ((2 ^ sw) << 5) + 16) = u32x4{0u, 0u, 0u, 0u};
      }
      f32x4 aK = {0.f, 0.f, 0.f, 0.f}, aV = aK, aX = aK;
#pragma unroll 1
      for (int ch = 0; ch < 9; ++ch) {
        RAW_BARRIER();
        if (ch == 0) STAMP(1);
        const char* sl = sm + (ch >= NSLOT ? ch - NSLOT : ch) * SLOT;
        const char* wl = sW + ch * 8192;
#pragma unroll
        for (int s = 0; s < 2; ++s) {
          const bf16x8 af = a_frag(sl, lane, cw, s);
          aK = MFMA16(w_frag(wl, lane, s, 0), af, aK);
          aV = MFMA16(af, w_frag(wl, lane, s, 1), aV);
          aX = MFMA16(w_frag(wl, lane, s, 2), af, aX);
        }
      }
      STAMP(3);
      const int tw = t0 + cw * 16;
      if (tw < S_LEN) {
        const int t = tw + tl;
        char* img = ws + WS_IMG + (tw >> 7) * SG_BYTES;
        const int tile = (tw >> 5) & 3, r = t & 31;
        *(u32x2*)(img + SG_K1 + tile * 1024 + (r + 32 * (g >> 1)) * 16 + (g & 1) * 8) = u32x2{pack2(aK[0], aK[1]), pack2(aK[2], aK[3])};
        char* vimg = img + SG_V + ((tw >> 4) & 7) * 640;
        *(u32x2*)(vimg + (g >> 1) * 320 + (g & 1) * 8 + vpos(tl) * 16) = u32x2{pack2(aV[0], aV[1]), pack2(aV[2], aV[3])};
        if (g == 0) {
          *(unsigned*)(img + SG_K2 + r * 16 + tile * 4) = pack2(aX[0], aX[1]);
          const float lg = rbf(aX[2]);
          ((float*)(ws + WS_G))[t] = rbf(1.0f / (1.0f + __expf(-lg)));
        } else if (g == 1) {
          const unsigned pv = pack2(aX[0], aX[1]);
          char* d = vimg + (tl >> 3) * 320 + (tl & 7) * 2;
          *(u16*)(d + 16 * 16) = (u16)pv;
          *(u16*)(d + 17 * 16) = (u16)(pv >> 16);
        } else if (g == 2) {
          if (tl < 4) {
            const unsigned val = (tl & 1) ? 0x3f803f80u : 0u;
            *(u32x4*)(vimg + (tl >> 1) * 320 + ((tl & 1) ? 18 : 15) * 16) = u32x4{val, val, val, val};
          } else if (tl == 4 && (tw & 127) == 0) {
            *(u32x4*)(img + SG_K2 + 512) = u32x4{0x3f80u, 0u, 0x3f80u, 0u};
          }
        }
      }
    }
  }
  STAMP(4);
  STAMP_FLUSH(0);
}

template <int N0>
__device__ __forceinline__ void ssm_unit(int c, const u16* __restrict__ hk, const u16* __restrict__ dk,
                                         const char* __restrict__ ws_c, char* __restrict__ ws, int lane) {
  constexpr int NM = N0 + 4;
  const int i = lane & 15, g = lane >> 4;
  const u16* ur = (const u16*)(ws_c + WS_UR) + (size_t)c * UR_STRIDE;
  const u16* ap = ur + ((i & 8) ? 0 : UR_COPY) + 2030 - 2 * (i & 7) + 8 * g;
  const __amdgpu_buffer_rsrc_t hr = __builtin_amdgcn_make_buffer_rsrc((void*)(hk + (size_t)c * S_LEN), 0, S_LEN * 2, 0x00020000);
  const int bo = (16 * i - 16 + 8 * g) * 2;
  f32x4 acc[4];
#pragma unroll
  for (int n = 0; n < 4; ++n) acc[n] = f32x4{0.f, 0.f, 0.f, 0.f};
#pragma unroll 1
  for (int rho = 0; rho < 8; ++rho) {
    u32x4 A[NM], B[NM];
#pragma unroll
    for (int m = 0; m < NM; ++m) A[m] = *(const u32x4_a4*)(ap - 32 * rho - 256 * m);
    B[0] = __builtin_amdgcn_raw_buffer_load_b128(hr, bo - 64 * rho, 0, 0);
#pragma unroll
    for (int j = 1; j < NM; ++j) B[j] = __builtin_amdgcn_raw_buffer_load_b128(hr, bo - 64 * rho + 512, 512 * (j - 1), 0);
#pragma unroll
    for (int m = 0; m < NM; ++m)
#pragma unroll
      for (int n = 0; n < 4; ++n)
        if (N0 + n - m >= 0)
          acc[n] = MFMA16(__builtin_bit_cast(bf16x8, A[m]), __builtin_bit_cast(bf16x8, B[N0 + n - m]), acc[n]);
  }
  const float dd = bf2f(dk[c]);
  u16* z = (u16*)(ws + WS_Z) + (size_t)c * S_LEN;
#pragma unroll
  for (int n = 0; n < 4; ++n) {
    const int tq = 256 * (N0 + n) + 16 * i + 8 * (g & 1) + 4 * (g >> 1);
    const u32x2 u = *(const u32x2*)(ur + 2044 - tq);
    const auto s02 = __builtin_amdgcn_permlane32_swap(__float_as_uint(acc[n][0]), __float_as_uint(acc[n][2]), false, false);
    const auto s13 = __builtin_amdgcn_permlane32_swap(__float_as_uint(acc[n][1]), __float_as_uint(acc[n][3]), false, false);
    const float z0 = rbf(__uint_as_float(s02[0])) + rbf(bfhi(u[1]) * dd);
    const float z1 = rbf(__uint_as_float(s02[1])) + rbf(bflo(u[1]) * dd);
    const float z2 = rbf(__uint_as_float(s13[0])) + rbf(bfhi(u[0]) * dd);
    const float z3 = rbf(__uint_as_float(s13[1])) + rbf(bflo(u[0]) * dd);
    *(u32x2*)(z + tq) = u32x2{pack2(z0, z1), pack2(z2, z3)};
  }
}

__device__ __forceinline__ f32x16 qk(bf16x8 k1, s16x4 k2, bf16x8 q1, s16x4 q2) {
  f32x16 z;
#pragma unroll
  for (int i = 0; i < 16; ++i) z[i] = 0.f;
  f32x16 s = __builtin_amdgcn_mfma_f32_32x32x16_bf16(k1, q1, z, 0, 0, 0);
  return __builtin_amdgcn_mfma_f32_32x32x8bf16_1k(k2, q2, s, 0, 0, 0);
}
__device__ __forceinline__ bf16x16 lo8(f32x16 s) {
  return __builtin_bit_cast(bf16x16, __builtin_shufflevector(s, s, 0, 1, 2, 3, 4, 5, 6, 7));
}
__device__ __forceinline__ bf16x16 hi8(f32x16 s) {
  return __builtin_bit_cast(bf16x16, __builtin_shufflevector(s, s, 8, 9, 10, 11, 12, 13, 14, 15));
}
__device__ __forceinline__ float max16(f32x16 s) {
  float m = fmaxf(s[0], s[1]);
#pragma unroll
  for (int i = 2; i < 16; ++i) m = fmaxf(m, s[i]);
  return m;
}
__device__ __forceinline__ s16x4 mk2(unsigned a, unsigned b) { return __builtin_bit_cast(s16x4, u32x2{a, b}); }

#define ATTN_CLOB "v96","v97","v98","v99","v100","v101","v102","v103","v104","v105","v106","v107","v108","v109","v110","v111", \
  "v112","v113","v114","v115","v116","v117","v118","v119","v120","v121","v122","v123","v124","v125","v126","v127", \
  "v128","v129","v130","v131","v132","v133","v134","v135","v136","v137","v138","v139","v140","v141","v142","v143"
#define EX4(a, b, c, d) "v_exp_f32 v" #a ", v" #a "\n v_exp_f32 v" #b ", v" #b "\n v_exp_f32 v" #c ", v" #c "\n v_exp_f32 v" #d ", v" #d "\n"
#define ATILE(OVHI, OK1N, K2C, QB2, K2N, QA2, OVLON, EXTRA3, W4, W5, W6, EXTRA7) \
  "v_smfmac_f32_32x32x32_bf16 %[ob], v[140:143], v[120:127], %[idx]\n" \
  "ds_read_b128 v[140:143], %[av] offset:" #OVHI "\n" \
  EX4(96, 97, 98, 99) \
  "v_mfma_f32_32x32x16_bf16 v[112:127], v[128:131], %[qb1], 0\n" \
  "ds_read_b128 v[128:131], %[ak1] offset:" #OK1N "\n" \
  EX4(100, 101, 102, 103) \
  "v_mfma_f32_32x32x8_bf16 v[112:127], " K2C ", %[" QB2 "], v[112:127]\n" \
  EXTRA3 \
  EX4(104, 105, 106, 107) \
  "s_waitcnt lgkmcnt(" #W4 ")\n" \
  "v_smfmac_f32_32x32x32_bf16 %[oa], v[136:139], v[96:103], %[idx]\n" \
  EX4(108, 109, 110, 111) \
  "s_waitcnt lgkmcnt(" #W5 ")\n" \
  "s_nop 0\n" \
  "v_smfmac_f32_32x32x32_bf16 %[oa], v[140:143], v[104:111], %[idx]\n" \
  EX4(112, 113, 114, 115) \
  "s_waitcnt lgkmcnt(" #W6 ")\n" \
  "v_mfma_f32_32x32x16_bf16 v[96:111], v[128:131], %[qa1], 0\n" \
  EX4(116, 117, 118, 119) \
  EXTRA7 \
  "v_mfma_f32_32x32x8_bf16 v[96:111], " K2N ", %[" QA2 "], v[96:111]\n" \
  EX4(120, 121, 122, 123) \
  "v_smfmac_f32_32x32x32_bf16 %[ob], v[136:139], v[112:119], %[idx]\n" \
  "ds_read_b128 v[136:139], %[av] offset:" #OVLON "\n" \
  EX4(124, 125, 126, 127) \
  "s_nop 1\n"
#define ATTN_LOOP_ASM \
  "ds_read_b128 v[128:131], %[ak1]\n" \
  "ds_read_b128 v[132:135], %[ak2]\n" \
  "ds_read_b128 v[136:139], %[av]\n" \
  "v_mov_b32 v140, 0\n v_mov_b32 v141, 0\n v_mov_b32 v142, 0\n v_mov_b32 v143, 0\n" \
  "v_mov_b32 v120, 0\n v_mov_b32 v121, 0\n v_mov_b32 v122, 0\n v_mov_b32 v123, 0\n" \
  "v_mov_b32 v124, 0\n v_mov_b32 v125, 0\n v_mov_b32 v126, 0\n v_mov_b32 v127, 0\n" \
  "s_waitcnt lgkmcnt(1)\n" \
  "v_mfma_f32_32x32x16_bf16 v[96:111], v[128:131], %[qa1], 0\n" \
  "v_mfma_f32_32x32x8_bf16 v[96:111], v[132:133], %[qa2e], v[96:111]\n" \
  "s_nop 11\n" \
  "L_attn_loop_%=:\n" \
  ATILE(640, 1024, "v[132:133]", "qb2e", "v[132:133]", "qa2o", 1280, "", 2, 1, 0, "") \
  ATILE(1920, 2048, "v[132:133]", "qb2o", "v[134:135]", "qa2e", 2560, "", 2, 1, 0, "") \
  ATILE(3200, 3072, "v[134:135]", "qb2e", "v[134:135]", "qa2o", 3840, "", 2, 1, 0, "") \
  "s_cmp_lt_i32 %[it], %[nbar]\n" \
  "s_cbranch_scc0 L_attn_nobar_%=\n" \
  "s_barrier\n" \
  "L_attn_nobar_%=:\n" \
  ATILE(4480, 19488, "v[134:135]", "qb2o", "v[132:133]", "qa2e", 19488, \
        "ds_read_b128 v[132:135], %[ak2] offset:19488\n", 3, 2, 1, "s_waitcnt lgkmcnt(0)\n") \
  "v_add_u32 %[ak1], 19488, %[ak1]\n" \
  "v_add_u32 %[ak2], 19488, %[ak2]\n" \
  "v_add_u32 %[av], 19488, %[av]\n" \
  "s_add_i32 %[it], %[it], 1\n" \
  "s_cmp_lt_i32 %[it], 8\n" \
  "s_cbranch_scc1 L_attn_loop_%=\n" \
  "v_smfmac_f32_32x32x32_bf16 %[ob], v[140:143], v[120:127], %[idx]\n" \
  "s_waitcnt lgkmcnt(0)\n" \
  "s_nop 15\n"

__global__ void __launch_bounds__(768) k2_attn(const u16* __restrict__ hk, const u16* __restrict__ dk, char* __restrict__ ws) {
  __shared__ __attribute__((aligned(16))) char sm[IMG_BYTES + 2560];
  const int tid = threadIdx.x, lane = tid & 63;
  const int wave = __builtin_amdgcn_readfirstlane(tid >> 6);
  const int b = blockIdx.x;
  STAMP_DECL;
  STAMP(0);

  if (wave < 4) {
    const int a = wave;
    const char* src = ws + WS_IMG + a * 1024 + lane * 16;
    __builtin_amdgcn_s_setprio(3);
#pragma unroll
    for (int p = 0; p < 10; ++p) glds16(src + p * 4096, sm + a * 1024 + p * 4096);
    asm volatile("s_waitcnt vmcnt(5)" ::: "memory");
    __builtin_amdgcn_s_barrier();
#pragma unroll
    for (int p = 10; p < 39; ++p) glds16(src + p * 4096, sm + a * 1024 + p * 4096);
    asm volatile("s_waitcnt vmcnt(29)" ::: "memory");
    __builtin_amdgcn_s_barrier();
    asm volatile("s_waitcnt vmcnt(0)" ::: "memory");
    __builtin_amdgcn_s_barrier();
    asm volatile("" ::: "memory");
    STAMP(1);
    __builtin_amdgcn_s_setprio(PRIO_X);
    const int W = b * 4 + a;
    if (W < 576) {
      ssm_unit<4>(W, hk, dk, ws, ws, lane);
    } else {
      for (int c = W - 576; c < 576; c += 448) ssm_unit<0>(c, hk, dk, ws, ws, lane);
    }
    STAMP(2);
    STAMP_FLUSH(1);
    return;
  }

  const int pair = wave & 3, half = (wave >> 2) - 1;
  if (half) __builtin_amdgcn_s_setprio(PRIO_B); else __builtin_amdgcn_s_setprio(PRIO_A);
  const int r = lane & 31, hh = lane >> 5;
  const int tq = b * 8 + pair * 2;
  const u16* Q = (const u16*)(ws + WS_Q) + (size_t)tq * DM + r * DH;
  const bf16x8 qa1 = __builtin_bit_cast(bf16x8, (u32x4)*(const u32x4_a4*)(Q + hh * 8));
  const bf16x8 qb1 = __builtin_bit_cast(bf16x8, (u32x4)*(const u32x4_a4*)(Q + DM + hh * 8));
  const unsigned qa2 = *(const unsigned*)(Q + 16), qb2 = *(const unsigned*)(Q + DM + 16);
  const int offK1 = SG_K1 + lane * 16;
  const int offK2 = SG_K2 + (hh ? 512 : r * 16);
  const int offV = SG_V + hh * 320 + vpos(r) * 16;
  const int idx = 0xDDDDDDDD;
  const unsigned ldsBase = (unsigned)(uintptr_t)(LDS_AS char*)sm;
  float* sRed = (float*)(sm + IMG_BYTES);
  volatile int* sFlag = (volatile int*)(sm + IMG_BYTES + 2048);

  RAW_BARRIER();
  STAMP(1);

  f32x16 Oa, Ob;
  for (int attempt = 0; attempt < 2; ++attempt) {
    float ma, mb;
    if (attempt == 0) {
      const bf16x8 k1 = *(const bf16x8*)(sm + offK1);
      const u32x4 k2 = *(const u32x4*)(sm + offK2);
      ma = max16(qk(k1, mk2(k2[0], k2[1]), qa1, mk2(hh ? 0u : qa2, 0u)));
      mb = max16(qk(k1, mk2(k2[0], k2[1]), qb1, mk2(hh ? 0u : qb2, 0u)));
    } else {
      ma = -INFINITY; mb = -INFINITY;
      for (int sg = 0; sg < 16; ++sg) {
        const char* base = sm + sg * SG_BYTES;
        const u32x4 k2 = *(const u32x4*)(base + offK2);
#pragma unroll
        for (int tl = 0; tl < 4; ++tl) {
          const bf16x8 k1 = *(const bf16x8*)(base + offK1 + tl * 1024);
          const s16x4 kk = mk2(k2[tl & 2], k2[(tl & 2) + 1]);
          const unsigned xa = hh ? 0u : qa2, xb = hh ? 0u : qb2;
          ma = fmaxf(ma, max16(qk(k1, kk, qa1, (tl & 1) ? mk2(0u, xa) : mk2(xa, 0u))));
          mb = fmaxf(mb, max16(qk(k1, kk, qb1, (tl & 1) ? mk2(0u, xb) : mk2(xb, 0u))));
        }
      }
    }
    ma = fmaxf(ma, __shfl_xor(ma, 32));
    mb = fmaxf(mb, __shfl_xor(mb, 32));
    const unsigned nma = pack2(-ma, 0.f), nmb = pack2(-mb, 0.f);
    const s16x4 qa2e = mk2(hh ? nma : qa2, 0u), qa2o = mk2(hh ? nma : 0u, hh ? 0u : qa2);
    const s16x4 qb2e = mk2(hh ? nmb : qb2, 0u), qb2o = mk2(hh ? nmb : 0u, hh ? 0u : qb2);

#pragma unroll
    for (int i = 0; i < 16; ++i) { Oa[i] = 0.f; Ob[i] = 0.f; }
    {
      unsigned aK1 = ldsBase + half * SG_BYTES + offK1;
      unsigned aK2 = ldsBase + half * SG_BYTES + offK2;
      unsigned aV = ldsBase + half * SG_BYTES + offV;
      int it = 0;
      const int nbar = attempt == 0 ? 2 : 0;
      asm volatile(ATTN_LOOP_ASM
                   : [oa] "+v"(Oa), [ob] "+v"(Ob), [ak1] "+v"(aK1), [ak2] "+v"(aK2), [av] "+v"(aV), [it] "+s"(it)
                   : [qa1] "v"(qa1), [qb1] "v"(qb1), [qa2e] "v"(qa2e), [qa2o] "v"(qa2o), [qb2e] "v"(qb2e), [qb2o] "v"(qb2o),
                     [idx] "v"(idx), [nbar] "s"(nbar)
                   : "memory", "scc", ATTN_CLOB);
    }
    STAMP(2);
    if (attempt == 1) break;
    bool bad = false;
#pragma unroll
    for (int i = 0; i < 11; ++i) bad = bad || !(fabsf(Oa[i]) < 1e30f) || !(fabsf(Ob[i]) < 1e30f);
    const bool anybad = __any(bad);
    if (lane == 0) sFlag[wave - 4] = anybad ? 1 : 0;
    RAW_BARRIER();
    int tot = 0;
#pragma unroll
    for (int w = 0; w < 8; ++w) tot |= sFlag[w];
    if (__builtin_amdgcn_readfirstlane(tot) == 0) break;
  }
  (void)sRed;
  STAMP(3);
  RAW_BARRIER();
  float* xch = (float*)sm + (pair * 2 + half) * (11 * 64);
  float* xin = (float*)sm + (pair * 2 + (half ^ 1)) * (11 * 64);
#pragma unroll
  for (int i = 0; i < 11; ++i) xch[i * 64 + lane] = half ? Oa[i] : Ob[i];
  RAW_BARRIER();
  float o[11];
#pragma unroll
  for (int i = 0; i < 11; ++i) o[i] = (half ? Ob[i] : Oa[i]) + xin[i * 64 + lane];
  const float l = __shfl(o[10], r);
  const float inv = 1.0f / l;
  u16* O = (u16*)(ws + WS_O) + (size_t)(tq + half) * DM + r * DH + hh * 4;
  *(u32x2_a4*)(O) = u32x2{pack2(o[0] * inv, o[1] * inv), pack2(o[2] * inv, o[3] * inv)};
  *(u32x2_a4*)(O + 8) = u32x2{pack2(o[4] * inv, o[5] * inv), pack2(o[6] * inv, o[7] * inv)};
  if (!hh) *(unsigned*)(O + 16) = pack2(o[8] * inv, o[9] * inv);
  STAMP(4);
  STAMP_FLUSH(1);
}

__global__ void __launch_bounds__(576) k3_out(const u16* __restrict__ x, const u16* __restrict__ wo,
                                              char* __restrict__ ws, u16* __restrict__ out) {
  constexpr int SLOT = 18 * 1024, NSLOT = 8;
  __shared__ __attribute__((aligned(16))) char sm[NSLOT * SLOT + 10240];
  const int tid = threadIdx.x, lane = tid & 63;
  const int wave = __builtin_amdgcn_readfirstlane(tid >> 6);
  const int grp = blockIdx.x & 7, loc = blockIdx.x >> 3;
  const int qid = grp * 29 + (grp < 2 ? grp : 2) + loc;
  const int mt = qid / 9, nt = qid - mt * 9;
  const int t0 = mt * 80, n0 = nt * 64;
  char* sZ = sm + NSLOT * SLOT;
  STAMP_DECL;
  STAMP(0);
  if (wave < 3) {
    gemm_loader<3, 10, 8, NSLOT>((const u16*)(ws + WS_O), t0, wo, n0, sm, wave, lane);
  } else if (wave == 8) {
    const u16* zT = (const u16*)(ws + WS_Z);
#pragma unroll
    for (int i = 0; i < 10; ++i) {
      const int ci = i * 64 + lane, c = ci / 10, k = ci - c * 10;
      glds16(zT + (size_t)(n0 + c) * S_LEN + t0 + k * 8, sZ + i * 1024);
    }
    wait_vm<0>();
  } else {
    const int cw = wave - 3;
    const int g = lane >> 4, tl = lane & 15;
    u32x4 xv[2]; float gt[2];
#pragma unroll
    for (int i = 0; i < 2; ++i) {
      int t = t0 + cw * 16 + i * 8 + (lane >> 3); t = t > S_LEN - 1 ? S_LEN - 1 : t;
      xv[i] = *(const u32x4*)(x + (size_t)t * DM + n0 + (lane & 7) * 8);
      gt[i] = ((const float*)(ws + WS_G))[t];
    }
    f32x4 acc[4];
#pragma unroll
    for (int nb = 0; nb < 4; ++nb) acc[nb] = f32x4{0.f, 0.f, 0.f, 0.f};
#pragma unroll 1
    for (int ch = 0; ch < 9; ++ch) {
      RAW_BARRIER();
      if (ch == 0) STAMP(1);
      const char* sl = sm + (ch >= NSLOT ? ch - NSLOT : ch) * SLOT;
#pragma unroll
      for (int s = 0; s < 2; ++s) {
        const bf16x8 af = a_frag(sl, lane, cw, s);
#pragma unroll
        for (int nb = 0; nb < 4; ++nb) acc[nb] = MFMA16(w_frag(sl + 10240, lane, s, nb), af, acc[nb]);
      }
    }
    STAMP(3);
    char* sE = sm + 2 * SLOT + cw * 4608;
#pragma unroll
    for (int nb = 0; nb < 4; ++nb) {
      const bf16x4 z = tr_read(sZ + (nb * 16 + g * 4 + (tl >> 2)) * 160 + (cw * 16 + (tl & 3) * 4) * 2);
      *(u32x2*)(sE + tl * 144 + nb * 32 + g * 8) = u32x2{pack2(acc[nb][0], acc[nb][1]), pack2(acc[nb][2], acc[nb][3])};
      *(u32x2*)(sE + 2304 + tl * 144 + nb * 32 + g * 8) = __builtin_bit_cast(u32x2, z);
    }
    if (t0 + cw * 16 < S_LEN) {
#pragma unroll
      for (int i = 0; i < 2; ++i) {
        const int row = i * 8 + (lane >> 3);
        const u32x4 av = *(const u32x4*)(sE + row * 144 + (lane & 7) * 16);
        const u32x4 zv = *(const u32x4*)(sE + 2304 + row * 144 + (lane & 7) * 16);
        const float gg = gt[i], omg = rbf(1.0f - gg);
        u32x4 o;
#pragma unroll
        for (int j = 0; j < 4; ++j) {
          const float lo = rbf(gg * bflo(av[j])) + rbf(omg * rbf(bflo(xv[i][j]) * bflo(zv[j])));
          const float hi = rbf(gg * bfhi(av[j])) + rbf(omg * rbf(bfhi(xv[i][j]) * bfhi(zv[j])));
          o[j] = pack2(lo, hi);
        }
        *(u32x4*)(out + (size_t)(t0 + cw * 16 + row) * DM + n0 + (lane & 7) * 8) = o;
      }
    }
  }
  STAMP(4);
  STAMP_FLUSH(2);
}

extern "C" void kernel_launch(void* const* d_in, const int* in_sizes, int n_in, void* d_out, int out_size,
                              void* d_ws, size_t ws_size, hipStream_t stream) {
  const u16* x = (const u16*)d_in[0];
  const u16* wq = (const u16*)d_in[1];
  const u16* wk = (const u16*)d_in[2];
  const u16* wv = (const u16*)d_in[3];
  const u16* wo = (const u16*)d_in[4];
  const u16* bk = (const u16*)d_in[6];
  const u16* bv = (const u16*)d_in[7];
  const u16* shk = (const u16*)d_in[8];
  const u16* hk = (const u16*)d_in[9];
  const u16* dk = (const u16*)d_in[10];
  const u16* gw = (const u16*)d_in[11];
  char* ws = (char*)d_ws;
  k1_proj<<<256, 640, 0, stream>>>(x, wq, wk, wv, gw, bk, bv, shk, ws);
  k2_attn<<<256, 768, 0, stream>>>(hk, dk, ws);
  k3_out<<<234, 576, 0, stream>>>(x, wo, ws, (u16*)d_out);
}
```

```cpp
#include <hip/hip_runtime.h>
#include <hip/hip_bf16.h>
#include <stdint.h>

typedef __attribute__((ext_vector_type(4))) __bf16 bf16x4;
typedef __attribute__((ext_vector_type(8))) __bf16 bf16x8;
typedef __attribute__((ext_vector_type(4))) short s16x4;
typedef __attribute__((ext_vector_type(4))) float f32x4;
typedef __attribute__((ext_vector_type(16))) float f32x16;
typedef __attribute__((ext_vector_type(2))) unsigned u32x2;
typedef __attribute__((ext_vector_type(4))) unsigned u32x4;
typedef unsigned short u16;
typedef __attribute__((ext_vector_type(4), aligned(4))) unsigned u32x4_a4;

#define LDS_AS __attribute__((address_space(3)))
#define GLB_AS __attribute__((address_space(1)))

constexpr int S_LEN = 2048, DM = 576, DH = 18;
constexpr int UR_STRIDE = 2112;
constexpr int SG_K1 = 0, SG_K2 = 4096, SG_V = 4624, SG_BYTES = 9744;
constexpr int IMG_PIECES = 156;
constexpr int IMG_BYTES = IMG_PIECES * 1024;

constexpr size_t WS_CNT = 0;
constexpr size_t WS_Q = 8192;
constexpr size_t WS_IMG = WS_Q + (size_t)S_LEN * DM * 2;
constexpr size_t WS_UR = WS_IMG + IMG_BYTES;
constexpr size_t UR_COPY = (size_t)DM * UR_STRIDE;
constexpr size_t WS_G = WS_UR + 2 * UR_COPY * 2;
constexpr size_t WS_O = WS_G + S_LEN * 4;
constexpr size_t O_BYTES = (size_t)S_LEN * DM * 2;
constexpr size_t WS_Z = WS_O + O_BYTES;
constexpr size_t Z_BYTES = (size_t)S_LEN * DM * 2;
constexpr int CNT_U = 0, CNT_Z = 36;
constexpr unsigned O_EMPTY = 0xffffffffu;

#define STAMP_DECL do {} while (0)
#define STAMP(i) do {} while (0)
#define STAMP_FLUSH(kern) do {} while (0)
#define RAW_BARRIER() do { asm volatile("s_waitcnt lgkmcnt(0)" ::: "memory"); __builtin_amdgcn_s_barrier(); asm volatile("" ::: "memory"); } while (0)
template <int N> __device__ __forceinline__ void wait_vm() { asm volatile("s_waitcnt vmcnt(%0)" :: "i"(N) : "memory"); }

__device__ __forceinline__ float bflo(unsigned w) { return __uint_as_float(w << 16); }
__device__ __forceinline__ float bfhi(unsigned w) { return __uint_as_float(w & 0xffff0000u); }
__device__ __forceinline__ unsigned pack2(float lo, float hi) {
  typedef __attribute__((ext_vector_type(2))) __bf16 bf16x2;
  bf16x2 v; v[0] = (__bf16)lo; v[1] = (__bf16)hi;
  return __builtin_bit_cast(unsigned, v);
}
__device__ __forceinline__ float rbf(float x) { return (float)(__bf16)x; }
__device__ __forceinline__ void glds16(const void* g, void* lds) {
  __builtin_amdgcn_global_load_lds((const GLB_AS unsigned*)g, (LDS_AS unsigned*)lds, 16, 0, 0);
}
__device__ __forceinline__ bf16x4 tr_read(const char* p) {
  return __builtin_amdgcn_ds_read_tr16_b64_v4bf16((LDS_AS bf16x4*)p);
}
__device__ __forceinline__ int vpos(int d) {
  const int a = d - (d > 11 && d < 16);
  return d > 18 ? 15 : (d == 11 ? 19 : a);
}
#define MFMA16(a, b, c) __builtin_amdgcn_mfma_f32_16x16x32_bf16(a, b, c, 0, 0, 0)

__device__ __forceinline__ int wsw(int k) { return ((k >> 1) & 1) | ((k >> 2) & 2); }
__device__ __forceinline__ bf16x8 a_frag(const char* sA, int lane, int rg, int s) {
  const int row = rg * 16 + (lane & 15);
  return *(const bf16x8*)(sA + row * 128 + (((s * 4 + (lane >> 4)) ^ ((row >> 1) & 7)) << 4));
}
__device__ __forceinline__ bf16x8 w_frag(const char* sW, int lane, int s, int nb) {
  const int k = s * 32 + (lane >> 4) * 8 + ((lane & 15) >> 2);
  const char* b = sW + k * 128 + ((nb ^ wsw(k)) << 5) + (lane & 3) * 8;
  const bf16x4 lo = tr_read(b), hi = tr_read(b + 512);
  return __builtin_shufflevector(lo, hi, 0, 1, 2, 3, 4, 5, 6, 7);
}

template <int NP, int NSLOT, int SLOT, int DEPTH, int PSTRIDE>
__device__ __forceinline__ void ring_loader(const char* const (&base)[NP], const int (&step)[NP], char* dst, int rot) {
  auto issue = [&](int j) {
    int ch = j + rot; ch = ch >= 9 ? ch - 9 : ch;
#pragma unroll
    for (int i = 0; i < NP; ++i) glds16(base[i] + ch * step[i], dst + (j % NSLOT) * SLOT + i * PSTRIDE);
  };
#pragma unroll
  for (int j = 0; j < DEPTH; ++j) issue(j);
#define RL_STEP(j) do { wait_vm<NP * ((8 - (j)) < (DEPTH - 1) ? (8 - (j)) : (DEPTH - 1))>(); __builtin_amdgcn_s_barrier(); \
    asm volatile("" ::: "memory"); if ((j) + DEPTH < 9) issue((j) + DEPTH); } while (0)
  RL_STEP(0); RL_STEP(1); RL_STEP(2); RL_STEP(3); RL_STEP(4); RL_STEP(5); RL_STEP(6); RL_STEP(7); RL_STEP(8);
#undef RL_STEP
}

#define K1_DEPTH 3
constexpr int K1_NQ = 198, K1_NS = 43, K1_NF = 15;
__global__ void __launch_bounds__(640) k1_proj(const u16* __restrict__ x, const u16* __restrict__ wq,
                                               const u16* __restrict__ wk, const u16* __restrict__ wv,
                                               const u16* __restrict__ gw, char* __restrict__ ws) {
  __shared__ __attribute__((aligned(16))) char sm[6 * 20480];
  const int tid = threadIdx.x, lane = tid & 63;
  const int wave = __builtin_amdgcn_readfirstlane(tid >> 6);
  const int b = blockIdx.x;
  const int g = lane >> 4, tl = lane & 15;
  STAMP_DECL;
  STAMP(0);
  if (b < K1_NQ) {
    const int grp = b & 7;
    const int qid = grp * 24 + (grp < 6 ? grp : 6) + (b >> 3);
    const int mt = qid / 9, nt = qid - mt * 9;
    const int t0 = mt * 96, n0 = nt * 64;
    const int rot = (nt + 3 * mt) % 9;
    constexpr int SLOT = 20480, NSLOT = 6;
    if (wave < 5) {
      const char* base[4]; int step[4];
#pragma unroll
      for (int i = 0; i < 4; ++i) {
        const int p = wave + 5 * i;
        const bool isA = p < 12;
        const int row = (isA ? p : p - 12) * 8 + (lane >> 3);
        const int sw = isA ? (row >> 1) & 7 : wsw(row) << 1;
        const int gr = isA ? min(t0 + row, S_LEN - 1) : row;
        base[i] = (isA ? (const char*)x : (const char*)(wq + n0)) + (unsigned)gr * (DM * 2) + (((lane & 7) ^ sw) << 4);
        step[i] = isA ? 128 : 64 * DM * 2;
      }
      ring_loader<4, NSLOT, SLOT, K1_DEPTH, 5120>(base, step, sm + wave * 1024, rot);
    } else if (wave < 9) {
      const int s = (wave - 5) & 1, rh = (wave - 5) >> 1;
      f32x4 acc[3][4];
#pragma unroll
      for (int i = 0; i < 3; ++i)
#pragma unroll
        for (int nb = 0; nb < 4; ++nb) acc[i][nb] = f32x4{0.f, 0.f, 0.f, 0.f};
      bf16x8 af[2][3], wf[2][4];
      auto rd = [&](int j, bf16x8 (&a)[3], bf16x8 (&w)[4]) {
        const char* sl = sm + (j >= NSLOT ? j - NSLOT : j) * SLOT;
#pragma unroll
        for (int nb = 0; nb < 4; ++nb) w[nb] = w_frag(sl + 12288, lane, s, nb);
#pragma unroll
        for (int i = 0; i < 3; ++i) a[i] = a_frag(sl, lane, rh * 3 + i, s);
        __builtin_amdgcn_sched_barrier(0);
      };
      auto mm = [&](bf16x8 (&a)[3], bf16x8 (&w)[4]) {
#pragma unroll
        for (int i = 0; i < 3; ++i)
#pragma unroll
          for (int nb = 0; nb < 4; ++nb) acc[i][nb] = MFMA16(w[nb], a[i], acc[i][nb]);
        __builtin_amdgcn_sched_barrier(0);
      };
      RAW_BARRIER();
      STAMP(1);
      rd(0, af[0], wf[0]);
#pragma unroll 1
      for (int j = 1; j < 9; j += 2) {
        RAW_BARRIER(); rd(j, af[1], wf[1]); mm(af[0], wf[0]);
        RAW_BARRIER(); rd(j + 1, af[0], wf[0]); mm(af[1], wf[1]);
      }
      mm(af[0], wf[0]);
      STAMP(2);
      f32x4* pd = (f32x4*)(sm + 3 * SLOT + (wave - 5) * 6144) + lane;
      const f32x4* ps = (const f32x4*)(sm + 3 * SLOT + ((wave - 5) ^ 1) * 6144) + lane;
      if (s) {
#pragma unroll
        for (int i = 0; i < 3; ++i) { pd[i * 128] = acc[i][0]; pd[i * 128 + 64] = acc[i][1]; acc[i][0] = acc[i][2]; acc[i][1] = acc[i][3]; }
      } else {
#pragma unroll
        for (int i = 0; i < 3; ++i) { pd[i * 128] = acc[i][2]; pd[i * 128 + 64] = acc[i][3]; }
      }
      RAW_BARRIER();
      const float qscale = 0.23570226039551584f * 1.4426950408889634f;
      char* sE = sm + (wave - 5) * 3840;
      u16* Q = (u16*)(ws + WS_Q);
#pragma unroll
      for (int i = 0; i < 3; ++i)
#pragma unroll
        for (int h = 0; h < 2; ++h) {
          const f32x4 a = (acc[i][h] + ps[i * 128 + h * 64]) * qscale;
          *(u32x2*)(sE + (i * 16 + tl) * 80 + h * 32 + g * 8) = u32x2{pack2(a[0], a[1]), pack2(a[2], a[3])};
        }
#pragma unroll
      for (int i = 0; i < 3; ++i) {
        const int t = t0 + rh * 48 + i * 16 + (lane >> 2);
        const u32x4 v = *(const u32x4*)(sE + (i * 16 + (lane >> 2)) * 80 + (lane & 3) * 16);
        if (t < S_LEN) *(u32x4*)(Q + (size_t)t * DM + n0 + s * 32 + (lane & 3) * 8) = v;
      }
    }
  } else if (b >= K1_NQ + K1_NS) {
    const int fb = b - (K1_NQ + K1_NS);
    if (fb == 0 && tid < 64) *(unsigned*)(ws + WS_CNT + tid * 128) = 0u;
    u32x4* od = (u32x4*)(ws + WS_O);
    for (int i = fb * 640 + tid; i < (int)(O_BYTES / 16); i += K1_NF * 640) od[i] = u32x4{O_EMPTY, O_EMPTY, O_EMPTY, O_EMPTY};
  } else {
    const int si = b - K1_NQ, sg8 = si & 7;
    const int gx = (sg8 + 6) & 7;
    const int mt = gx * 5 + (gx > 0 ? 1 : 0) + (gx > 6 ? 1 : 0) + (si >> 3), t0 = mt * 48;
    char* sW = sm + 55296;
    for (int p = wave; p < 54; p += 10) {
      const int ch = p / 6, row = (p - ch * 6) * 8 + (lane >> 3);
      glds16((const char*)x + (unsigned)min(t0 + row, S_LEN - 1) * (DM * 2) + ch * 128 + (((lane & 7) ^ ((row >> 1) & 7)) << 4), sm + p * 1024);
    }
    if (tid < 576) {
      const int k = tid;
      const u32x4 k0 = *(const u32x4_a4*)(wk + k * DH), k1 = *(const u32x4_a4*)(wk + k * DH + 8);
      const u32x4 v0 = *(const u32x4_a4*)(wv + k * DH), v1 = *(const u32x4_a4*)(wv + k * DH + 8);
      const unsigned k2 = *(const unsigned*)(wk + k * DH + 16), v2 = *(const unsigned*)(wv + k * DH + 16);
      const unsigned gk = gw[k];
      char* d = sW + k * 96;
      *(u32x4*)(d) = k0; *(u32x4*)(d + 16) = k1;
      *(u32x4*)(d + 32) = v0; *(u32x4*)(d + 48) = v1;
      *(u32x4*)(d + 64) = u32x4{k2, gk, v2, 0u}; *(u32x4*)(d + 80) = u32x4{0u, 0u, 0u, 0u};
    }
    wait_vm<0>();
    RAW_BARRIER();
    STAMP(1);
    const int rg = wave / 3, sub = wave - rg * 3;
    const int tw = t0 + rg * 16;
    if (wave < 9 && tw < S_LEN) {
      f32x4 acc = {0.f, 0.f, 0.f, 0.f}, acc2 = acc;
      const char* wb = sW + (g * 8 + (tl >> 2)) * 96 + sub * 32 + (lane & 3) * 8;
#pragma unroll 3
      for (int ch = 0; ch < 9; ++ch) {
        bf16x8 af[2], wf[2];
#pragma unroll
        for (int s = 0; s < 2; ++s) {
          af[s] = a_frag(sm + ch * 6144, lane, rg, s);
          const char* wp = wb + (ch * 64 + s * 32) * 96;
          wf[s] = __builtin_shufflevector(tr_read(wp), tr_read(wp + 384), 0, 1, 2, 3, 4, 5, 6, 7);
        }
        if (sub == 1) { acc = MFMA16(af[0], wf[0], acc); acc2 = MFMA16(af[1], wf[1], acc2); }
        else          { acc = MFMA16(wf[0], af[0], acc); acc2 = MFMA16(wf[1], af[1], acc2); }
      }
      acc += acc2;
      STAMP(2);
      const int t = tw + tl;
      char* img = ws + WS_IMG + (tw >> 7) * SG_BYTES;
      char* vimg = img + SG_V + ((tw >> 4) & 7) * 640;
      const int tile = (tw >> 5) & 3, r = t & 31;
      const unsigned p01 = pack2(acc[0], acc[1]), p23 = pack2(acc[2], acc[3]);
      if (sub == 0) {
        *(u32x2*)(img + SG_K1 + tile * 1024 + (r + 32 * (g >> 1)) * 16 + (g & 1) * 8) = u32x2{p01, p23};
      } else if (sub == 1) {
        *(u32x2*)(vimg + (g >> 1) * 320 + (g & 1) * 8 + vpos(tl) * 16) = u32x2{p01, p23};
      } else if (g == 0) {
        *(unsigned*)(img + SG_K2 + r * 16 + tile * 4) = p01;
        const float lg = rbf(acc[2]);
        ((float*)(ws + WS_G))[t] = rbf(1.0f / (1.0f + __expf(-lg)));
      } else if (g == 1) {
        char* d = vimg + (tl >> 3) * 320 + (tl & 7) * 2;
        *(u16*)(d + 16 * 16) = (u16)p01;
        *(u16*)(d + 17 * 16) = (u16)(p01 >> 16);
      } else if (g == 2) {
        if (tl < 4) {
          const unsigned val = (tl & 1) ? 0x3f803f80u : 0u;
          *(u32x4*)(vimg + (tl >> 1) * 320 + ((tl & 1) ? 18 : 15) * 16) = u32x4{val, val, val, val};
        } else if (tl == 4 && (tw & 127) == 0) {
          *(u32x4*)(img + SG_K2 + 512) = u32x4{0x3f80u, 0u, 0x3f80u, 0u};
        }
      }
    }
  }
  STAMP(3);
  STAMP_FLUSH(0);
}

__device__ __forceinline__ void cnt_add(char* ws, int idx) {
  __hip_atomic_fetch_add((GLB_AS unsigned*)(ws + WS_CNT + idx * 128), 1u, __ATOMIC_RELAXED, __HIP_MEMORY_SCOPE_AGENT);
}
template <int SLP>
__device__ __forceinline__ void cnt_wait(char* ws, int i0, int i1, unsigned need, int lane) {
  GLB_AS unsigned* cp = (GLB_AS unsigned*)(ws + WS_CNT + ((lane & 1) ? i1 : i0) * 128);
#pragma unroll 1
  for (int spin = 0; spin < (1 << 22); ++spin) {
    const unsigned v = __hip_atomic_load(cp, __ATOMIC_RELAXED, __HIP_MEMORY_SCOPE_AGENT);
    if (__all(v >= need)) break;
    __builtin_amdgcn_s_sleep(SLP);
  }
  asm volatile("" ::: "memory");
}

struct KvRegs { u32x2 kr[12], vr[9]; u32x4 s0, s1; };
__device__ __forceinline__ void kv_load(KvRegs& R, int task, const u16* __restrict__ bk, const u16* __restrict__ bv,
                                        const u16* __restrict__ shk, int lane) {
  const int tb = task / 18, cgp = task - tb * 18;
  const int c0 = cgp * 32 + (lane >> 3) * 4, ts = tb * 64 + (7 - (lane & 7)) * 8;
#pragma unroll
  for (int i = 0; i < 12; ++i) R.kr[i] = *(const u32x2*)(bk + (size_t)max(ts - 4 + i, 0) * DM + c0);
#pragma unroll
  for (int i = 0; i < 9; ++i) R.vr[i] = *(const u32x2*)(bv + (size_t)max(ts - 1 + i, 0) * DM + c0);
  R.s0 = *(const u32x4*)(shk + c0 * 4); R.s1 = *(const u32x4*)(shk + c0 * 4 + 8);
}
__device__ __forceinline__ void kv_finish(KvRegs& R, int task, char* ws, int lane) {
  const int tb = task / 18, cgp = task - tb * 18;
  const int c0 = cgp * 32 + (lane >> 3) * 4, ts = tb * 64 + (7 - (lane & 7)) * 8;
  u32x2 (&kr)[12] = R.kr; u32x2 (&vr)[9] = R.vr; const u32x4 s0 = R.s0, s1 = R.s1;
  const __amdgpu_buffer_rsrc_t ur = __builtin_amdgcn_make_buffer_rsrc((void*)(ws + WS_UR), 0, (int)(4 * UR_COPY), 0x00020000);
  if (tb == 0) {
    if (ts == 0) {
#pragma unroll
      for (int i = 0; i < 4; ++i) kr[i] = u32x2{0u, 0u};
    }
#pragma unroll
    for (int i = 0; i < 4; ++i) {
      const int row = cgp * 32 + i * 8 + (lane >> 3);
      __builtin_amdgcn_raw_buffer_store_b128(u32x4{0u, 0u, 0u, 0u}, ur,
          (int)(((lane >> 2) & 1) * UR_COPY + row * UR_STRIDE + 2048 + (lane & 3) * 8) * 2, 0, 16);
    }
  }
  int off0 = (c0 * UR_STRIDE + 2040 - ts) * 2;
  asm volatile("" : "+v"(off0));
#pragma unroll
  for (int c = 0; c < 4; ++c) {
    const unsigned w01 = (c < 2 ? s0 : s1)[(c & 1) * 2], w23 = (c < 2 ? s0 : s1)[(c & 1) * 2 + 1];
    const float w0 = bflo(w01), w1 = bfhi(w01), w2 = bflo(w23), w3 = bfhi(w23);
    float kk[12], o[9];
#pragma unroll
    for (int i = 0; i < 12; ++i) kk[i] = (c & 1) ? bfhi(kr[i][c >> 1]) : bflo(kr[i][c >> 1]);
#pragma unroll
    for (int i = 0; i < 9; ++i) {
      const float a = kk[i] * w3 + kk[i + 1] * w2 + kk[i + 2] * w1 + kk[i + 3] * w0;
      o[i] = a * ((c & 1) ? bfhi(vr[i][c >> 1]) : bflo(vr[i][c >> 1]));
    }
    const int off = off0 + c * UR_STRIDE * 2;
    __builtin_amdgcn_raw_buffer_store_b128(u32x4{pack2(o[8], o[7]), pack2(o[6], o[5]), pack2(o[4], o[3]), pack2(o[2], o[1])}, ur, off, 0, 16);
    __builtin_amdgcn_raw_buffer_store_b128(u32x4{pack2(o[7], o[6]), pack2(o[5], o[4]), pack2(o[3], o[2]), pack2(o[1], o[0])}, ur,
                                           off, (int)(2 * UR_COPY), 16);
  }
  wait_vm<0>();
  if (lane == 0) cnt_add(ws, CNT_U + cgp * 2 + (tb >> 4));
}

template <int N0>
__device__ __forceinline__ void ssm_unit(int c, const u16* __restrict__ hk, const u16* __restrict__ dk,
                                         char* ws, int lane) {
  constexpr int NM = N0 + 4;
  const int i = lane & 15, g = lane >> 4;
  const __amdgpu_buffer_rsrc_t ur = __builtin_amdgcn_make_buffer_rsrc((void*)(ws + WS_UR + (size_t)c * (UR_STRIDE * 2)), 0,
                                                                      (int)(UR_COPY + UR_STRIDE) * 2, 0x00020000);
  int ao = (((i & 8) ? 0 : (int)UR_COPY) + 2030 - 256 * (NM - 1) - 2 * (i & 7) + 8 * g) * 2;
  const __amdgpu_buffer_rsrc_t hr = __builtin_amdgcn_make_buffer_rsrc((void*)(hk + (size_t)c * S_LEN), 0, S_LEN * 2, 0x00020000);
  int bo = (16 * i - 16 + 8 * g) * 2, bo1 = bo + 512;
  asm volatile("" : "+v"(bo1));
  f32x4 acc[4];
#pragma unroll
  for (int n = 0; n < 4; ++n) acc[n] = f32x4{0.f, 0.f, 0.f, 0.f};
  u32x4 A[NM], B[NM];
  auto ldA = [&](int m) { A[m] = __builtin_amdgcn_raw_buffer_load_b128(ur, ao, 512 * (NM - 1 - m), 16); };
  auto ldB = [&](int j) {
    if (j == 0) B[0] = __builtin_amdgcn_raw_buffer_load_b128(hr, bo, 0, 0);
    else B[j] = __builtin_amdgcn_raw_buffer_load_b128(hr, bo1, 512 * (j - 1), 0);
  };
#pragma unroll
  for (int j = 0; j < NM; ++j) ldB(j);
  const float dd = bflo(dk[c]);
  cnt_wait<8>(ws, CNT_U + (c >> 5) * 2, CNT_U + (c >> 5) * 2 + (N0 ? 1 : 0), 16u, lane);
  const unsigned tch = __builtin_amdgcn_raw_buffer_load_b32(ur, (lane >> 5) * (int)UR_COPY * 2 + (N0 ? (lane & 31) * 128 : 2048 + (lane & 31) * 64), 0, 16);
#pragma unroll
  for (int m = 0; m < NM; ++m) ldA(m);
  u32x2 u[4];
#pragma unroll
  for (int n = 0; n < 4; ++n) {
    const int uo = (2044 - (256 * (N0 + n) + 16 * i + 8 * (g & 1) + 4 * (g >> 1))) * 2;
    u[n] = u32x2{__builtin_amdgcn_raw_buffer_load_b32(ur, uo, 0, 16), __builtin_amdgcn_raw_buffer_load_b32(ur, uo, 4, 16)};
  }
  asm volatile("" :: "v"(tch));
  if (N0) {
#pragma unroll 1
    for (int rho = 0; rho < 8; ++rho) {
      if (rho < 7) { ao -= 64; bo -= 64; bo1 -= 64; asm volatile("" : "+v"(ao), "+v"(bo), "+v"(bo1)); }
#pragma unroll
      for (int m = 0; m < NM; ++m) {
#pragma unroll
        for (int n = 3; n >= 0; --n)
          if (N0 + n - m >= 0)
            acc[n] = MFMA16(__builtin_bit_cast(bf16x8, A[m]), __builtin_bit_cast(bf16x8, B[N0 + n - m]), acc[n]);
        __builtin_amdgcn_sched_barrier(0);
        ldA(m); ldB(NM - 1 - m);
        __builtin_amdgcn_sched_barrier(0);
      }
    }
  } else {
    u32x4 A2[NM], B2[NM];
    auto round = [&](u32x4 (&Ac)[NM], u32x4 (&Bc)[NM], u32x4 (&An)[NM], u32x4 (&Bn)[NM]) {
#pragma unroll
      for (int m = 0; m < NM; ++m) {
        An[m] = __builtin_amdgcn_raw_buffer_load_b128(ur, ao, 512 * (NM - 1 - m), 16);
        Bn[m] = m == 0 ? __builtin_amdgcn_raw_buffer_load_b128(hr, bo, 0, 0) : __builtin_amdgcn_raw_buffer_load_b128(hr, bo1, 512 * (m - 1), 0);
      }
      __builtin_amdgcn_sched_barrier(0);
#pragma unroll
      for (int m = 0; m < NM; ++m)
#pragma unroll
        for (int n = 3; n >= m; --n)
          acc[n] = MFMA16(__builtin_bit_cast(bf16x8, Ac[m]), __builtin_bit_cast(bf16x8, Bc[n - m]), acc[n]);
      __builtin_amdgcn_sched_barrier(0);
    };
#pragma unroll 1
    for (int rho = 0; rho < 8; rho += 2) {
      ao -= 64; bo -= 64; bo1 -= 64; asm volatile("" : "+v"(ao), "+v"(bo), "+v"(bo1));
      round(A, B, A2, B2);
      if (rho < 6) { ao -= 64; bo -= 64; bo1 -= 64; asm volatile("" : "+v"(ao), "+v"(bo), "+v"(bo1)); }
      round(A2, B2, A, B);
    }
  }
  const __amdgpu_buffer_rsrc_t zr = __builtin_amdgcn_make_buffer_rsrc((void*)(ws + WS_Z + (size_t)c * (S_LEN * 2)), 0, S_LEN * 2, 0x00020000);
#pragma unroll
  for (int n = 0; n < 4; ++n) {
    const int tq = 256 * (N0 + n) + 16 * i + 8 * (g & 1) + 4 * (g >> 1);
    const auto s02 = __builtin_amdgcn_permlane32_swap(__float_as_uint(acc[n][0]), __float_as_uint(acc[n][2]), false, false);
    const auto s13 = __builtin_amdgcn_permlane32_swap(__float_as_uint(acc[n][1]), __float_as_uint(acc[n][3]), false, false);
    const float z0 = __uint_as_float(s02[0]) + bfhi(u[n][1]) * dd;
    const float z1 = __uint_as_float(s02[1]) + bflo(u[n][1]) * dd;
    const float z2 = __uint_as_float(s13[0]) + bfhi(u[n][0]) * dd;
    const float z3 = __uint_as_float(s13[1]) + bflo(u[n][0]) * dd;
    __builtin_amdgcn_raw_buffer_store_b64(u32x2{pack2(z0, z1), pack2(z2, z3)}, zr, tq * 2, 0, 16);
  }
  wait_vm<0>();
  if (lane == 0) cnt_add(ws, CNT_Z + (c >> 6) * 2 + (N0 ? 1 : 0));
}

__device__ __forceinline__ f32x16 qk(bf16x8 k1, s16x4 k2, bf16x8 q1, s16x4 q2) {
  f32x16 z;
#pragma unroll
  for (int i = 0; i < 16; ++i) z[i] = 0.f;
  f32x16 s = __builtin_amdgcn_mfma_f32_32x32x16_bf16(k1, q1, z, 0, 0, 0);
  return __builtin_amdgcn_mfma_f32_32x32x8bf16_1k(k2, q2, s, 0, 0, 0);
}
__device__ __forceinline__ float max16(f32x16 s) {
  float m = fmaxf(s[0], s[1]);
#pragma unroll
  for (int i = 2; i < 16; ++i) m = fmaxf(m, s[i]);
  return m;
}
__device__ __forceinline__ s16x4 mk2(unsigned a, unsigned b) { return __builtin_bit_cast(s16x4, u32x2{a, b}); }
__device__ __forceinline__ float swap32_max(float m) {
  const auto s = __builtin_amdgcn_permlane32_swap(__float_as_uint(m), __float_as_uint(m), false, false);
  return fmaxf(__uint_as_float(s[0]), __uint_as_float(s[1]));
}

#define ATTN_CLOB "v96","v97","v98","v99","v100","v101","v102","v103","v104","v105","v106","v107","v108","v109","v110","v111", \
  "v112","v113","v114","v115","v116","v117","v118","v119","v120","v121","v122","v123","v124","v125","v126","v127", \
  "v128","v129","v130","v131","v132","v133","v134","v135","v136","v137","v138","v139","v140","v141","v142","v143"
#define EX1(a) "v_exp_f32 v" #a ", v" #a "\n"
#define EX2(a, b) EX1(a) EX1(b)
#define EX3(a, b, c) EX1(a) EX1(b) EX1(c)
#define EX4(a, b, c, d) EX1(a) EX1(b) EX1(c) EX1(d)
#define ATILE(OVHI, OK1N, K2C, QB2, K2N, QA2, OVLON, X3, X4, X6, X7) \
  EX2(96, 97) \
  "v_smfmac_f32_32x32x32_bf16 %[ob], v[140:143], v[120:127], %[idx]\n" \
  "ds_read_b128 v[140:143], %[av] offset:" #OVHI "\n" \
  EX3(98, 99, 100) \
  "v_mfma_f32_32x32x16_bf16 v[112:127], v[128:131], %[qb1], 0\n" \
  "ds_read_b128 v[128:131], %[ak1] offset:" #OK1N "\n" \
  EX3(101, 102, 103) \
  "v_mfma_f32_32x32x8_bf16 v[112:127], " K2C ", %[" QB2 "], v[112:127]\n" \
  X3 \
  EX4(104, 105, 106, 107) \
  "v_smfmac_f32_32x32x32_bf16 %[oa], v[136:139], v[96:103], %[idx]\n" \
  X4 \
  EX4(108, 109, 110, 111) \
  EX2(112, 113) \
  "v_smfmac_f32_32x32x32_bf16 %[oa], v[140:143], v[104:111], %[idx]\n" \
  "s_waitcnt lgkmcnt(0)\n" \
  EX3(114, 115, 116) \
  "v_mfma_f32_32x32x16_bf16 v[96:111], v[128:131], %[qa1], 0\n" \
  X6 \
  EX3(117, 118, 119) \
  "v_mfma_f32_32x32x8_bf16 v[96:111], " K2N ", %[" QA2 "], v[96:111]\n" \
  X7 \
  EX4(120, 121, 122, 123) \
  "v_smfmac_f32_32x32x32_bf16 %[ob], v[136:139], v[112:119], %[idx]\n" \
  "ds_read_b128 v[136:139], %[av] offset:" #OVLON "\n" \
  EX4(124, 125, 126, 127)
#define WL2 "s_waitcnt lgkmcnt(2)\n"
#define WL1 "s_waitcnt lgkmcnt(1)\n"
#define NOP0 "s_nop 0\n"
#define ATTN_LOOP_ASM \
  "ds_read_b128 v[128:131], %[ak1]\n" \
  "ds_read_b128 v[132:135], %[ak2]\n" \
  "ds_read_b128 v[136:139], %[av]\n" \
  "v_mov_b32 v140, 0\n v_mov_b32 v141, 0\n v_mov_b32 v142, 0\n v_mov_b32 v143, 0\n" \
  "v_mov_b32 v120, 0\n v_mov_b32 v121, 0\n v_mov_b32 v122, 0\n v_mov_b32 v123, 0\n" \
  "v_mov_b32 v124, 0\n v_mov_b32 v125, 0\n v_mov_b32 v126, 0\n v_mov_b32 v127, 0\n" \
  "s_waitcnt lgkmcnt(1)\n" \
  "v_mfma_f32_32x32x16_bf16 v[96:111], v[128:131], %[qa1], 0\n" \
  "v_mfma_f32_32x32x8_bf16 v[96:111], v[132:133], %[qa2e], v[96:111]\n" \
  "s_nop 11\n" \
  "L_attn_loop_%=:\n" \
  ATILE(640, 1024, "v[132:133]", "qb2e", "v[132:133]", "qa2o", 1280, WL2, WL1, "", NOP0) \
  ATILE(1920, 2048, "v[132:133]", "qb2o", "v[134:135]", "qa2e", 2560, WL2, WL1, "", NOP0) \
  ATILE(3200, 3072, "v[134:135]", "qb2e", "v[134:135]", "qa2o", 3840, WL2, WL1, "s_cmp_lt_i32 %[it], %[nbar]\n", \
        "s_cbranch_scc0 L_attn_nobar_%=\n s_barrier\n L_attn_nobar_%=:\n") \
  ATILE(4480, 19488, "v[134:135]", "qb2o", "v[132:133]", "qa2e", 19488, \
        "ds_read_b128 v[132:135], %[ak2] offset:19488\n s_waitcnt lgkmcnt(3)\n", WL2, "s_add_i32 %[it], %[it], 1\n", "s_cmp_lt_i32 %[it], 8\n") \
  "v_add_u32 %[ak1], 19488, %[ak1]\n" \
  "v_add_u32 %[ak2], 19488, %[ak2]\n" \
  "v_add_u32 %[av], 19488, %[av]\n" \
  "s_cbranch_scc1 L_attn_loop_%=\n" \
  "v_smfmac_f32_32x32x32_bf16 %[ob], v[140:143], v[120:127], %[idx]\n" \
  "s_waitcnt lgkmcnt(0)\n" \
  "s_nop 15\n"

constexpr int T_XCH = 0;
constexpr int T_OST = 22528;
constexpr int T_Z = 32768;
constexpr int T_PART = 43008;
constexpr int T_EPI = 63488;
constexpr int T_WO = 89088;
static_assert(T_WO + 576 * 128 <= IMG_BYTES + 4096, "lds");

__global__ void __launch_bounds__(768) k2_attn(const u16* __restrict__ hk, const u16* __restrict__ dk,
                                               const u16* __restrict__ x, const u16* __restrict__ wo,
                                               const u16* __restrict__ bk, const u16* __restrict__ bv,
                                               const u16* __restrict__ shk, char* ws, u16* __restrict__ out) {
  __shared__ __attribute__((aligned(16))) char sm[IMG_BYTES + 4096];
  const int tid = threadIdx.x, lane = tid & 63;
  const int wave = __builtin_amdgcn_readfirstlane(tid >> 6);
  const int b = blockIdx.x;
  unsigned* sFlag = (unsigned*)(sm + IMG_BYTES + 4032);
  const int grp = b & 7, loc = b >> 3;
  const bool has_tile = loc < (grp < 2 ? 30 : 29);
  const int qid = has_tile ? grp * 29 + (grp < 2 ? grp : 2) + loc : 0;
  const int mt = qid / 9, nt = qid - mt * 9;
  const int t0 = mt * 80, n0 = nt * 64;
  STAMP_DECL;
  STAMP(0);

  if (wave < 4) {
    const char* src = ws + WS_IMG + wave * 1024 + lane * 16;
    __builtin_amdgcn_s_setprio(3);
#pragma unroll
    for (int p = 0; p < 10; ++p) glds16(src + p * 4096, sm + wave * 1024 + p * 4096);
    wait_vm<5>();
    __builtin_amdgcn_s_barrier();
    const int W = b + 256 * wave;
    KvRegs R;
#pragma unroll
    for (int p = 10; p < 39; ++p) glds16(src + p * 4096, sm + wave * 1024 + p * 4096);
    asm volatile("" ::: "memory");
    if (W >= 448) { kv_load(R, W - 448, bk, bv, shk, lane); wait_vm<21>(); }
    else wait_vm<0>();
    __builtin_amdgcn_s_barrier();
    asm volatile("" ::: "memory");
    STAMP(1);
    if (W >= 448) kv_finish(R, W - 448, ws, lane);
    STAMP(9);
    if (W < 576) {
      ssm_unit<4>(W, hk, dk, ws, lane);
    } else {
      for (int c = W - 576; c < 576; c += 448) ssm_unit<0>(c, hk, dk, ws, lane);
    }
    STAMP(2);
    const int k0 = wave * 144 + (lane >> 3);
    const char* wsrc = (const char*)(wo + n0) + (size_t)k0 * (DM * 2);
    {
      unsigned tv[3];
#pragma unroll
      for (int i = 0; i < 3; ++i) {
        const int ln = wave * 192 + i * 64 + lane;
        const char* pp = ln < 576 ? (const char*)(wo + n0) + (size_t)ln * (DM * 2) : (const char*)(x + n0) + (size_t)min(t0 + ((ln - 576) >> 1), S_LEN - 1) * (DM * 2);
        tv[i] = *(const unsigned*)pp;
      }
#pragma unroll
      for (int i = 0; i < 3; ++i) asm volatile("" :: "v"(tv[i]));
    }
    STAMP(3);
    RAW_BARRIER();
    if (__any(sFlag[lane & 7] != 0u)) RAW_BARRIER();
    const int c0 = ((lane & 7) ^ (wsw(lane >> 3) << 1)) << 4;
    char* wdst = sm + T_WO + wave * 18432;
#pragma unroll
    for (int i = 0; i < 9; ++i) glds16(wsrc + i * 8 * DM * 2 + (c0 ^ ((i & 1) << 6)), wdst + i * 1024);
    RAW_BARRIER();
    __builtin_amdgcn_s_barrier();
#pragma unroll
    for (int i = 9; i < 18; ++i) glds16(wsrc + i * 8 * DM * 2 + (c0 ^ ((i & 1) << 6)), wdst + i * 1024);
  } else {
    const int pair = wave & 3, half = (wave >> 2) - 1;
    const int r = lane & 31, hh = lane >> 5;
    const int tq = b * 8 + pair * 2;
    const u16* Q = (const u16*)(ws + WS_Q) + (size_t)tq * DM + r * DH;
    const bf16x8 qa1 = __builtin_bit_cast(bf16x8, (u32x4)*(const u32x4_a4*)(Q + hh * 8));
    const bf16x8 qb1 = __builtin_bit_cast(bf16x8, (u32x4)*(const u32x4_a4*)(Q + DM + hh * 8));
    const unsigned qa2 = *(const unsigned*)(Q + 16), qb2 = *(const unsigned*)(Q + DM + 16);
    const int offK1 = SG_K1 + lane * 16;
    const int offK2 = SG_K2 + (hh ? 512 : r * 16);
    const int offV = SG_V + hh * 320 + vpos(r) * 16;
    const int idx = 0xDDDDDDDD;
    const unsigned ldsBase = (unsigned)(uintptr_t)(LDS_AS char*)sm;

    RAW_BARRIER();
    STAMP(1);

    f32x16 Oa, Ob;
    for (int attempt = 0; attempt < 2; ++attempt) {
      float ma, mb;
      const unsigned xa = hh ? 0u : qa2, xb = hh ? 0u : qb2;
      if (attempt == 0) {
        const bf16x8 k1 = *(const bf16x8*)(sm + offK1);
        const u32x2 k2 = *(const u32x2*)(sm + offK2);
        ma = max16(qk(k1, mk2(k2[0], k2[1]), qa1, mk2(xa, 0u)));
        mb = max16(qk(k1, mk2(k2[0], k2[1]), qb1, mk2(xb, 0u)));
      } else {
        ma = -INFINITY; mb = -INFINITY;
#pragma unroll 1
        for (int tile = 0; tile < 64; tile += 2) {
          const char* base = sm + (tile >> 2) * SG_BYTES;
          const u32x2 k2 = *(const u32x2*)(base + offK2 + (tile & 2) * 4);
          const s16x4 kk = mk2(k2[0], k2[1]);
          const bf16x8 k1 = *(const bf16x8*)(base + offK1 + (tile & 3) * 1024);
          ma = fmaxf(ma, max16(qk(k1, kk, qa1, mk2(xa, 0u))));
          mb = fmaxf(mb, max16(qk(k1, kk, qb1, mk2(xb, 0u))));
          const bf16x8 k1o = *(const bf16x8*)(base + offK1 + (tile & 3) * 1024 + 1024);
          ma = fmaxf(ma, max16(qk(k1o, kk, qa1, mk2(0u, xa))));
          mb = fmaxf(mb, max16(qk(k1o, kk, qb1, mk2(0u, xb))));
        }
      }
      ma = swap32_max(ma);
      mb = swap32_max(mb);
      const unsigned nma = pack2(-ma, 0.f), nmb = pack2(-mb, 0.f);
      const s16x4 qa2e = mk2(hh ? nma : qa2, 0u), qa2o = mk2(hh ? nma : 0u, xa);
      const s16x4 qb2e = mk2(hh ? nmb : qb2, 0u), qb2o = mk2(hh ? nmb : 0u, xb);
#pragma unroll
      for (int i = 0; i < 16; ++i) { Oa[i] = 0.f; Ob[i] = 0.f; }
      {
        unsigned aK1 = ldsBase + half * SG_BYTES + offK1;
        unsigned aK2 = ldsBase + half * SG_BYTES + offK2;
        unsigned aV = ldsBase + half * SG_BYTES + offV;
        int it = 0;
        const int nbar = attempt == 0 ? 1 : 0;
        asm volatile(ATTN_LOOP_ASM
                     : [oa] "+v"(Oa), [ob] "+v"(Ob), [ak1] "+v"(aK1), [ak2] "+v"(aK2), [av] "+v"(aV), [it] "+s"(it)
                     : [qa1] "v"(qa1), [qb1] "v"(qb1), [qa2e] "v"(qa2e), [qa2o] "v"(qa2o), [qb2e] "v"(qb2e), [qb2o] "v"(qb2o),
                       [idx] "v"(idx), [nbar] "s"(nbar)
                     : "memory", "scc", ATTN_CLOB);
      }
      STAMP(2);
      if (attempt == 1) { RAW_BARRIER(); break; }
      unsigned big = 0;
#pragma unroll
      for (int i = 0; i < 11; ++i) big = max(big, max(__float_as_uint(Oa[i]) & 0x7fffffffu, __float_as_uint(Ob[i]) & 0x7fffffffu));
      const bool anybad = __any(big > 0x71000000u);
      if (lane == 0) sFlag[wave - 4] = anybad ? 1u : 0u;
      RAW_BARRIER();
      if (!__any(sFlag[lane & 7] != 0u)) break;
    }
    STAMP(3);
    float* xch = (float*)(sm + T_XCH) + (pair * 2 + half) * (11 * 64);
    const float* xin = (const float*)(sm + T_XCH) + (pair * 2 + (half ^ 1)) * (11 * 64);
#pragma unroll
    for (int i = 0; i < 11; ++i) xch[i * 64 + lane] = half ? Oa[i] : Ob[i];
    RAW_BARRIER();
    float o[11];
#pragma unroll
    for (int i = 0; i < 11; ++i) o[i] = (half ? Ob[i] : Oa[i]) + xin[i * 64 + lane];
    const auto ls = __builtin_amdgcn_permlane32_swap(__float_as_uint(o[10]), __float_as_uint(o[10]), false, false);
    const float inv = 1.0f / __uint_as_float(ls[0]);
    char* od = sm + T_OST + (pair * 2 + half) * 1168 + r * 36 + hh * 8;
    *(unsigned*)(od) = pack2(o[0] * inv, o[1] * inv);
    *(unsigned*)(od + 4) = pack2(o[2] * inv, o[3] * inv);
    *(unsigned*)(od + 16) = pack2(o[4] * inv, o[5] * inv);
    *(unsigned*)(od + 20) = pack2(o[6] * inv, o[7] * inv);
    if (!hh) *(unsigned*)(od + 32) = pack2(o[8] * inv, o[9] * inv);
    RAW_BARRIER();
  }
  const __amdgpu_buffer_rsrc_t orsrc = __builtin_amdgcn_make_buffer_rsrc((void*)(ws + WS_O), 0, (int)O_BYTES, 0x00020000);
  if (wave >= 4) {
    for (int j8 = wave - 4; j8 < 9; j8 += 8) {
      const int row = lane & 7, j = j8 * 8 + (lane >> 3);
      u32x4 v = *(const u32x4*)(sm + T_OST + row * 1168 + j * 16);
#pragma unroll
      for (int i = 0; i < 4; ++i) v[i] = min(v[i], O_EMPTY - 1u);
      __builtin_amdgcn_raw_buffer_store_b128(v, orsrc, (((b >> 1) * 72 + j) * 16 + (b & 1) * 8 + row) * 16, 0, 16);
    }
  }
  STAMP(4);
  if (!has_tile) { wait_vm<0>(); STAMP_FLUSH(1); return; }

  const int cwi = wave - 2;
  const int kh = cwi >= 5 ? 1 : 0, cw = cwi - kh * 5;
  const int g = lane >> 4, tl = lane & 15;
  u32x2 xv[2]; float gt;
  if (wave >= 2) {
    const int te = min(t0 + cw * 16 + tl, S_LEN - 1);
#pragma unroll
    for (int h = 0; h < 2; ++h) xv[h] = *(const u32x2*)(x + (size_t)te * DM + n0 + kh * 32 + h * 16 + g * 4);
    gt = ((const float*)(ws + WS_G))[te];
  }
  if (wave == 0) cnt_wait<1>(ws, CNT_Z + nt * 2 + (t0 < 1024 ? 0 : 1), CNT_Z + nt * 2 + (t0 + 79 < 1024 ? 0 : 1), 64u, lane);
  if (wave < 4) wait_vm<0>();
  RAW_BARRIER();
  STAMP(5);
  if (wave < 2) {
    const __amdgpu_buffer_rsrc_t zrsrc = __builtin_amdgcn_make_buffer_rsrc((void*)(ws + WS_Z), 0, (int)Z_BYTES, 0x00020000);
    u32x4 zv[5];
#pragma unroll
    for (int i = 0; i < 5; ++i) {
      const int ci = (wave * 5 + i) * 64 + lane, c = ci / 10, k = ci - c * 10;
      zv[i] = __builtin_amdgcn_raw_buffer_load_b128(zrsrc, ((n0 + c) * S_LEN + t0 + k * 8) * 2, 0, 16);
    }
#pragma unroll
    for (int i = 0; i < 5; ++i) *(u32x4*)(sm + T_Z + ((wave * 5 + i) * 64 + lane) * 16) = zv[i];
    asm volatile("s_waitcnt lgkmcnt(0)" ::: "memory");
  } else {
    float bx[2][4];
    const float omg = 1.0f - gt;
#pragma unroll
    for (int h = 0; h < 2; ++h) {
      bx[h][0] = omg * bflo(xv[h][0]); bx[h][1] = omg * bfhi(xv[h][0]);
      bx[h][2] = omg * bflo(xv[h][1]); bx[h][3] = omg * bfhi(xv[h][1]);
    }
    const int rgp = mt * 5 + cw;
    const int obase = ((rgp * 18 + kh * 9) * 64 + lane) * 16;
    const char* wl = sm + T_WO + kh * 9 * 4096;
#pragma unroll 1
    for (int spin = 0; spin < (1 << 22); ++spin) {
      asm volatile("" ::: "memory");
      const unsigned v = __builtin_amdgcn_raw_buffer_load_b32(orsrc, obase, nt * 1024, 16);
      if (__all(v != O_EMPTY)) break;
      __builtin_amdgcn_s_sleep(1);
    }
    STAMP(6);
    f32x4 acc[4];
#pragma unroll 1
    for (int spin = 0; spin < (1 << 20); ++spin) {
      asm volatile("" ::: "memory");
      u32x4 of[9];
#pragma unroll
      for (int s = 0; s < 9; ++s) {
        const int sr = s + nt >= 9 ? s + nt - 9 : s + nt;
        of[s] = __builtin_amdgcn_raw_buffer_load_b128(orsrc, obase, sr * 1024, 16);
      }
      __builtin_amdgcn_sched_barrier(0);
#pragma unroll
      for (int nb = 0; nb < 4; ++nb) acc[nb] = f32x4{0.f, 0.f, 0.f, 0.f};
      unsigned mx = 0;
#pragma unroll
      for (int s = 0; s < 9; ++s) {
        const int sr = s + nt >= 9 ? s + nt - 9 : s + nt;
#pragma unroll
        for (int nb = 0; nb < 4; ++nb) acc[nb] = MFMA16(w_frag(wl + sr * 4096, lane, 0, nb), __builtin_bit_cast(bf16x8, of[s]), acc[nb]);
        mx = max(max(mx, max(of[s][0], of[s][1])), max(of[s][2], of[s][3]));
      }
      if (__all(mx != O_EMPTY)) break;
    }
    f32x4* pd = (f32x4*)(sm + T_PART + cwi * 2048) + lane;
    pd[0] = kh ? acc[0] : acc[2];
    pd[64] = kh ? acc[1] : acc[3];
    STAMP(7);
    RAW_BARRIER();
    const f32x4* ps = (const f32x4*)(sm + T_PART + (kh ? cw : cw + 5) * 2048) + lane;
    char* sE = sm + T_EPI + cwi * 1280;
#pragma unroll
    for (int h = 0; h < 2; ++h) {
      const f32x4 a = (kh ? acc[2 + h] : acc[h]) + ps[h * 64];
      const u32x2 z = __builtin_bit_cast(u32x2, tr_read(sm + T_Z + ((kh * 2 + h) * 16 + g * 4 + (tl >> 2)) * 160 + (cw * 16 + (tl & 3) * 4) * 2));
      *(u32x2*)(sE + tl * 80 + h * 32 + g * 8) =
          u32x2{pack2(gt * a[0] + bx[h][0] * bflo(z[0]), gt * a[1] + bx[h][1] * bfhi(z[0])),
                pack2(gt * a[2] + bx[h][2] * bflo(z[1]), gt * a[3] + bx[h][3] * bfhi(z[1]))};
    }
    const u32x4 o = *(const u32x4*)(sE + (lane >> 2) * 80 + (lane & 3) * 16);
    if (t0 + cw * 16 < S_LEN) *(u32x4*)(out + (size_t)(t0 + cw * 16 + (lane >> 2)) * DM + n0 + kh * 32 + (lane & 3) * 8) = o;
  }
  STAMP(8);
  STAMP_FLUSH(1);
}

extern "C" void kernel_launch(void* const* d_in, const int* in_sizes, int n_in, void* d_out, int out_size,
                              void* d_ws, size_t ws_size, hipStream_t stream) {
  const u16* x = (const u16*)d_in[0];
  const u16* wq = (const u16*)d_in[1];
  const u16* wk = (const u16*)d_in[2];
  const u16* wv = (const u16*)d_in[3];
  const u16* wo = (const u16*)d_in[4];
  const u16* bk = (const u16*)d_in[6];
  const u16* bv = (const u16*)d_in[7];
  const u16* shk = (const u16*)d_in[8];
  const u16* hk = (const u16*)d_in[9];
  const u16* dk = (const u16*)d_in[10];
  const u16* gw = (const u16*)d_in[11];
  char* ws = (char*)d_ws;
  k1_proj<<<K1_NQ + K1_NS + K1_NF, 640, 0, stream>>>(x, wq, wk, wv, gw, ws);
  k2_attn<<<256, 768, 0, stream>>>(hk, dk, x, wo, bk, bv, shk, ws, (u16*)d_out);
}
```

```cpp
#include <hip/hip_runtime.h>
#include <hip/hip_bf16.h>
#include <stdint.h>

typedef __attribute__((ext_vector_type(4))) __bf16 bf16x4;
typedef __attribute__((ext_vector_type(8))) __bf16 bf16x8;
typedef __attribute__((ext_vector_type(16))) __bf16 bf16x16;
typedef __attribute__((ext_vector_type(4))) short s16x4;
typedef __attribute__((ext_vector_type(4))) float f32x4;
typedef __attribute__((ext_vector_type(16))) float f32x16;
typedef __attribute__((ext_vector_type(2))) unsigned u32x2;
typedef __attribute__((ext_vector_type(4))) unsigned u32x4;
typedef unsigned short u16;
typedef __attribute__((ext_vector_type(4), aligned(4))) unsigned u32x4_a4;

#define LDS_AS __attribute__((address_space(3)))
#define GLB_AS __attribute__((address_space(1)))

constexpr int S_LEN = 2048, DM = 576, DH = 18;
constexpr int PS_COPY = 2112;
constexpr int SG_K1 = 0, SG_K2 = 4096, SG_V = 4624, SG_BYTES = 9744;
constexpr int IMG_PIECES = 156;
constexpr int IMG_BYTES = IMG_PIECES * 1024;

constexpr size_t WS_CNT = 0;
constexpr size_t WS_Q = 8192;
constexpr size_t WS_IMG = WS_Q + (size_t)S_LEN * DM * 2;
constexpr size_t WS_UR = WS_IMG + IMG_BYTES;
constexpr size_t WS_PS = WS_UR + (size_t)DM * PS_COPY * 2;
constexpr size_t WS_G = WS_PS + (size_t)DM * 2 * PS_COPY * 2;
constexpr size_t WS_O = WS_G + S_LEN * 4;
constexpr size_t O_BYTES = (size_t)S_LEN * DM * 2;
constexpr size_t WS_Z = WS_O + O_BYTES;
constexpr size_t Z_BYTES = (size_t)S_LEN * DM * 2;
constexpr int CNT_Z = 0, CNT_F = 18;
constexpr unsigned O_EMPTY = 0xffffffffu;

#define STAMP_DECL do {} while (0)
#define STAMP(i) do {} while (0)
#define STAMP_FLUSH(kern) do {} while (0)
#define WAIT_LGKM0() do { asm volatile("" ::: "memory"); __builtin_amdgcn_s_waitcnt(0xC07F); asm volatile("" ::: "memory"); } while (0)
#define RAW_BARRIER() do { WAIT_LGKM0(); __builtin_amdgcn_s_barrier(); asm volatile("" ::: "memory"); } while (0)
template <int N> __device__ __forceinline__ void wait_vm() {
  asm volatile("s_waitcnt vmcnt(%0)" :: "i"(N) : "memory");
  if (N == 0) __builtin_amdgcn_s_waitcnt(0x0F70);
}

__device__ __forceinline__ float bflo(unsigned w) { return __uint_as_float(w << 16); }
__device__ __forceinline__ float bfhi(unsigned w) { return __uint_as_float(w & 0xffff0000u); }
__device__ __forceinline__ unsigned pack2(float lo, float hi) {
  typedef __attribute__((ext_vector_type(2))) __bf16 bf16x2;
  bf16x2 v; v[0] = (__bf16)lo; v[1] = (__bf16)hi;
  return __builtin_bit_cast(unsigned, v);
}
__device__ __forceinline__ float rbf(float x) { return (float)(__bf16)x; }
__device__ __forceinline__ void glds16(const void* g, void* lds) {
  __builtin_amdgcn_global_load_lds((const GLB_AS unsigned*)g, (LDS_AS unsigned*)lds, 16, 0, 0);
}
__device__ __forceinline__ bf16x4 tr_read(const char* p) {
  return __builtin_amdgcn_ds_read_tr16_b64_v4bf16((LDS_AS bf16x4*)p);
}
__device__ __forceinline__ int vpos(int d) {
  const int a = d - (d > 11 && d < 16);
  return d > 18 ? 15 : (d == 11 ? 19 : a);
}
#define MFMA16(a, b, c) __builtin_amdgcn_mfma_f32_16x16x32_bf16(a, b, c, 0, 0, 0)

__device__ __forceinline__ int wsw(int k) { return ((k >> 1) & 1) | ((k >> 2) & 2); }
__device__ __forceinline__ bf16x8 a_frag(const char* sA, int lane, int rg, int s) {
  const int row = rg * 16 + (lane & 15);
  return *(const bf16x8*)(sA + row * 128 + (((s * 4 + (lane >> 4)) ^ ((row >> 1) & 7)) << 4));
}
__device__ __forceinline__ bf16x8 w_frag(const char* sW, int lane, int s, int nb) {
  const int k = s * 32 + (lane >> 4) * 8 + ((lane & 15) >> 2);
  const char* b = sW + k * 128 + ((nb ^ wsw(k)) << 5) + (lane & 3) * 8;
  const bf16x4 lo = tr_read(b), hi = tr_read(b + 512);
  return __builtin_shufflevector(lo, hi, 0, 1, 2, 3, 4, 5, 6, 7);
}

template <int NP, int NSLOT, int SLOT, int DEPTH>
__device__ __forceinline__ void ring_loader(const char* const (&base)[NP], const int (&step)[NP], char* const (&dst)[NP], int rot) {
  auto issue = [&](int j) {
    int ch = j + rot; ch = ch >= 9 ? ch - 9 : ch;
#pragma unroll
    for (int i = 0; i < NP; ++i) glds16(base[i] + ch * step[i], dst[i] + (j % NSLOT) * SLOT);
  };
#pragma unroll
  for (int j = 0; j < DEPTH; ++j) issue(j);
#define RL_STEP(j) do { wait_vm<NP * ((8 - (j)) < (DEPTH - 1) ? (8 - (j)) : (DEPTH - 1))>(); __builtin_amdgcn_s_barrier(); \
    asm volatile("" ::: "memory"); if ((j) + DEPTH < 9) issue((j) + DEPTH); } while (0)
  RL_STEP(0); RL_STEP(1); RL_STEP(2); RL_STEP(3); RL_STEP(4); RL_STEP(5); RL_STEP(6); RL_STEP(7); RL_STEP(8);
#undef RL_STEP
}

#define K1_DEPTH 3
#define ST_AUX 16
__device__ __forceinline__ void code_prefetch_exit(bool both, int lane) {
  unsigned long long pc; unsigned d1, d2;
  asm volatile("s_getpc_b64 %0\nL_pf_%=:\n s_mov_b32 %1, k2_attn-L_pf_%=\n s_mov_b32 %2, k9_end-L_pf_%=" : "=s"(pc), "=s"(d1), "=s"(d2));
  const unsigned last = (both ? d2 : d1) - 4u;
  unsigned o[5], t[5];
#pragma unroll
  for (int i = 0; i < 5; ++i) o[i] = min((unsigned)(lane * 128 + i * 8192), last);
  asm volatile("global_load_dword %0, %5, %10\n global_load_dword %1, %6, %10\n global_load_dword %2, %7, %10\n"
               " global_load_dword %3, %8, %10\n global_load_dword %4, %9, %10\n s_endpgm"
               : "=&v"(t[0]), "=&v"(t[1]), "=&v"(t[2]), "=&v"(t[3]), "=&v"(t[4])
               : "v"(o[0]), "v"(o[1]), "v"(o[2]), "v"(o[3]), "v"(o[4]), "s"(pc) : "memory");
}

extern "C" __global__ void __launch_bounds__(832) k1_proj(const u16* __restrict__ x, const u16* __restrict__ wq,
                                               const u16* __restrict__ wk, const u16* __restrict__ wv,
                                               const u16* __restrict__ gw, const u16* __restrict__ bk,
                                               const u16* __restrict__ bv, const u16* __restrict__ shk, char* __restrict__ ws) {
  __shared__ __attribute__((aligned(256))) char sm[6 * 24576];
  const int tid = threadIdx.x, lane = tid & 63;
  const int wave = __builtin_amdgcn_readfirstlane(tid >> 6);
  const int grp = blockIdx.x & 7, loc = blockIdx.x >> 3;
  if (wave == 0) code_prefetch_exit(loc == 30, lane);
  const int g = lane >> 4, tl = lane & 15;
  STAMP_DECL;
  STAMP(0);
  if (loc < 18) {
    const int mh = loc >= 9 ? 1 : 0, nt = loc - mh * 9;
    const int t0 = (grp * 2 + mh) * 128, n0 = nt * 64;
    constexpr int SLOT = 24576, NSLOT = 6;
    if (wave < 9) {
      const int lw = wave - 1;
      int rot = nt + 4 * mh + grp; rot = rot >= 18 ? rot - 18 : (rot >= 9 ? rot - 9 : rot);
      const char* base[3]; const int step[3] = {64 * DM * 2, 128, 128};
      char* dst[3] = {sm + 16384 + lw * 1024, sm + lw * 2048, sm + lw * 2048 + 1024};
      {
        const int row = lw * 8 + (lane >> 3);
        base[0] = (const char*)(wq + n0) + (unsigned)row * (DM * 2) + (((lane & 7) ^ (wsw(row) << 1)) << 4);
      }
#pragma unroll
      for (int i = 0; i < 2; ++i) {
        const int row = (lw * 2 + i) * 8 + (lane >> 3);
        base[1 + i] = (const char*)x + (unsigned)(t0 + row) * (DM * 2) + (((lane & 7) ^ ((row >> 1) & 7)) << 4);
      }
      ring_loader<3, NSLOT, SLOT, K1_DEPTH>(base, step, dst, rot);
    } else {
      const int cwv = wave - 9;
      const int nh = cwv & 1, rh = cwv >> 1;
      f32x4 acc[4][2];
#pragma unroll
      for (int i = 0; i < 4; ++i) { acc[i][0] = f32x4{0.f, 0.f, 0.f, 0.f}; acc[i][1] = acc[i][0]; }
      bf16x8 af[2][4], wf[2][2];
      auto rd = [&](int j, int s, bf16x8 (&a)[4], bf16x8 (&w)[2]) {
        const char* sl = sm + (j >= NSLOT ? j - NSLOT : j) * SLOT;
#pragma unroll
        for (int h = 0; h < 2; ++h) w[h] = w_frag(sl + 16384, lane, s, nh * 2 + h);
#pragma unroll
        for (int i = 0; i < 4; ++i) a[i] = a_frag(sl, lane, rh * 4 + i, s);
      };
      auto mm = [&](bf16x8 (&a)[4], bf16x8 (&w)[2]) {
#pragma unroll
        for (int i = 0; i < 4; ++i)
#pragma unroll
          for (int h = 0; h < 2; ++h) acc[i][h] = MFMA16(w[h], a[i], acc[i][h]);
      };
      auto mix = [&]() {
#pragma unroll
        for (int i = 0; i < 4; ++i) { __builtin_amdgcn_sched_group_barrier(0x100, 2, 0); __builtin_amdgcn_sched_group_barrier(0x8, 1, 0); }
        __builtin_amdgcn_sched_group_barrier(0x8, 4, 0);
        __builtin_amdgcn_sched_barrier(0);
      };
      RAW_BARRIER();
      STAMP(1);
      rd(0, 0, af[0], wf[0]);
      __builtin_amdgcn_sched_barrier(0);
      rd(0, 1, af[1], wf[1]); mm(af[0], wf[0]); mix();
#pragma unroll 1
      for (int j = 1; j < 9; ++j) {
        RAW_BARRIER(); __builtin_amdgcn_sched_barrier(0);
        rd(j, 0, af[0], wf[0]); mm(af[1], wf[1]); mix();
        rd(j, 1, af[1], wf[1]); mm(af[0], wf[0]); mix();
      }
      mm(af[1], wf[1]);
      STAMP(2);
      const float qscale = 0.23570226039551584f * 1.4426950408889634f;
      char* sE = sm + 3 * SLOT + cwv * 5120;
#pragma unroll
      for (int i = 0; i < 4; ++i)
#pragma unroll
        for (int h = 0; h < 2; ++h) {
          const f32x4 a = acc[i][h] * qscale;
          *(u32x2*)(sE + (i * 16 + tl) * 80 + h * 32 + g * 8) = u32x2{pack2(a[0], a[1]), pack2(a[2], a[3])};
        }
      const __amdgpu_buffer_rsrc_t qr = __builtin_amdgcn_make_buffer_rsrc((void*)(ws + WS_Q), 0, S_LEN * DM * 2, 0x00020000);
#pragma unroll
      for (int i = 0; i < 4; ++i) {
        const int row = i * 16 + (lane >> 2);
        const u32x4 v = *(const u32x4*)(sE + row * 80 + (lane & 3) * 16);
        __builtin_amdgcn_raw_buffer_store_b128(v, qr, ((t0 + rh * 64 + row) * DM + n0 + nh * 32 + (lane & 3) * 8) * 2, 0, ST_AUX);
      }
    }
  } else if (loc < 22) {
    const int t0 = (grp * 4 + loc - 18) * 64;
    char* sW = sm + 73728;
    const int k = min(tid - 64, 575);
    const u32x4 k0 = *(const u32x4_a4*)(wk + k * DH), k1 = *(const u32x4_a4*)(wk + k * DH + 8);
    const u32x4 v0 = *(const u32x4_a4*)(wv + k * DH), v1 = *(const u32x4_a4*)(wv + k * DH + 8);
    const unsigned k2 = *(const unsigned*)(wk + k * DH + 16), v2 = *(const unsigned*)(wv + k * DH + 16);
    const unsigned gk = gw[k];
#pragma unroll
    for (int i = 0; i < 6; ++i) {
      const int p = wave - 1 + 12 * i;
      const int ch = p >> 3, row = (p & 7) * 8 + (lane >> 3);
      glds16((const char*)x + (unsigned)(t0 + row) * (DM * 2) + ch * 128 + (((lane & 7) ^ ((row >> 1) & 7)) << 4), sm + p * 1024);
    }
    if (blockIdx.x == 144 && wave == 12) *(unsigned*)(ws + WS_CNT + lane * 128) = 0u;
    {
      char* d = sW + k * 96;
      *(u32x4*)(d) = k0; *(u32x4*)(d + 16) = k1;
      *(u32x4*)(d + 32) = v0; *(u32x4*)(d + 48) = v1;
      *(u32x4*)(d + 64) = u32x4{k2, gk, v2, 0u}; *(u32x4*)(d + 80) = u32x4{0u, 0u, 0u, 0u};
    }
    wait_vm<0>();
    RAW_BARRIER();
    STAMP(1);
    const int rg = (wave - 1) & 3, sub = (wave - 1) >> 2;
    const int tw = t0 + rg * 16;
    f32x4 acc = {0.f, 0.f, 0.f, 0.f}, acc2 = acc;
    const char* wb = sW + (g * 8 + (tl >> 2)) * 96 + sub * 32 + (lane & 3) * 8;
#pragma unroll
    for (int hf = 0; hf < 2; ++hf) {
      bf16x8 fa[9], fw[9];
#pragma unroll
      for (int q = 0; q < 9; ++q) {
        const int ks = hf * 9 + q;
        fa[q] = a_frag(sm + (ks >> 1) * 8192, lane, rg, ks & 1);
        fw[q] = __builtin_shufflevector(tr_read(wb + ks * 3072), tr_read(wb + ks * 3072 + 384), 0, 1, 2, 3, 4, 5, 6, 7);
      }
      __builtin_amdgcn_sched_barrier(0);
      if (sub == 1) {
#pragma unroll
        for (int q = 0; q < 9; ++q) { if (q & 1) acc2 = MFMA16(fa[q], fw[q], acc2); else acc = MFMA16(fa[q], fw[q], acc); }
      } else {
#pragma unroll
        for (int q = 0; q < 9; ++q) { if (q & 1) acc2 = MFMA16(fw[q], fa[q], acc2); else acc = MFMA16(fw[q], fa[q], acc); }
      }
    }
    acc += acc2;
    STAMP(2);
    const int t = tw + tl;
    char* img = ws + WS_IMG + (tw >> 7) * SG_BYTES;
    char* vimg = img + SG_V + ((tw >> 4) & 7) * 640;
    const int tile = (tw >> 5) & 3, r = t & 31;
    const unsigned p01 = pack2(acc[0], acc[1]), p23 = pack2(acc[2], acc[3]);
    if (sub == 0) {
      *(u32x2*)(img + SG_K1 + tile * 1024 + (r + 32 * (g >> 1)) * 16 + (g & 1) * 8) = u32x2{p01, p23};
      if (lane < 4) {
        const unsigned val = (tl & 1) ? 0x3f803f80u : 0u;
        *(u32x4*)(vimg + (tl >> 1) * 320 + ((tl & 1) ? 18 : 15) * 16) = u32x4{val, val, val, val};
      } else if (lane == 4 && (tw & 127) == 0) {
        *(u32x4*)(img + SG_K2 + 512) = u32x4{0x3f80u, 0u, 0x3f80u, 0u};
      }
    } else if (sub == 1) {
      *(u32x2*)(vimg + (g >> 1) * 320 + (g & 1) * 8 + vpos(tl) * 16) = u32x2{p01, p23};
    } else if (g == 0) {
      *(unsigned*)(img + SG_K2 + r * 16 + tile * 4) = p01;
      const float lg = rbf(acc[2]);
      ((float*)(ws + WS_G))[t] = rbf(__builtin_amdgcn_rcpf(1.0f + __expf(-lg)));
    } else if (g == 1) {
      char* d = vimg + (tl >> 3) * 320 + (tl & 7) * 2;
      *(u16*)(d + 16 * 16) = (u16)p01;
      *(u16*)(d + 17 * 16) = (u16)(p01 >> 16);
    }
  } else {
    const int t0 = grp * 256, n0 = (loc - 22) * 64;
    constexpr int UP = 520;
    const __amdgpu_buffer_rsrc_t ur = __builtin_amdgcn_make_buffer_rsrc((void*)(ws + WS_UR), 0, DM * PS_COPY * 2, 0x00020000);
    if (wave < 9) {
      const int kid = tid - 64, cg = kid & 7, st = kid >> 3;
      const int c0 = n0 + cg * 8, ts = t0 + st * 4;
      u32x4 kr[7], vr[4], sk[4];
#pragma unroll
      for (int i = 0; i < 7; ++i) kr[i] = *(const u32x4*)(bk + (size_t)max(ts - 3 + i, 0) * DM + c0);
#pragma unroll
      for (int i = 0; i < 4; ++i) vr[i] = *(const u32x4*)(bv + (size_t)(ts + i) * DM + c0);
#pragma unroll
      for (int i = 0; i < 4; ++i) sk[i] = *(const u32x4*)(shk + c0 * 4 + i * 8);
      if (ts == 0) { kr[0] = u32x4{0u, 0u, 0u, 0u}; kr[1] = kr[0]; kr[2] = kr[0]; }
#pragma unroll
      for (int c = 0; c < 8; ++c) {
        const unsigned w01 = sk[c >> 1][(c & 1) * 2], w23 = sk[c >> 1][(c & 1) * 2 + 1];
        const float w0 = bflo(w01), w1 = bfhi(w01), w2 = bflo(w23), w3 = bfhi(w23);
        float kk[7], o[4];
#pragma unroll
        for (int i = 0; i < 7; ++i) kk[i] = (c & 1) ? bfhi(kr[i][c >> 1]) : bflo(kr[i][c >> 1]);
#pragma unroll
        for (int i = 0; i < 4; ++i) {
          const float a = kk[i] * w3 + kk[i + 1] * w2 + kk[i + 2] * w1 + kk[i + 3] * w0;
          o[i] = a * ((c & 1) ? bfhi(vr[i][c >> 1]) : bflo(vr[i][c >> 1]));
        }
        *(u32x2*)(sm + (cg * 8 + c) * UP + (252 - st * 4) * 2) = u32x2{pack2(o[3], o[2]), pack2(o[1], o[0])};
      }
    } else if (grp == 0 && wave < 11) {
      __builtin_amdgcn_raw_buffer_store_b128(u32x4{0u, 0u, 0u, 0u}, ur, ((n0 + lane) * PS_COPY + 2048 + (wave - 9) * 8) * 2, 0, ST_AUX);
    }
    STAMP(1);
    RAW_BARRIER();
    if (wave < 9) {
      u32x2 lo[4], hi[4];
#pragma unroll
      for (int i = 0; i < 4; ++i) {
        const char* sp = sm + ((wave - 1) * 8 + i * 2 + (lane >> 5)) * UP + (lane & 31) * 16;
        lo[i] = *(const u32x2*)sp; hi[i] = *(const u32x2*)(sp + 8);
      }
#pragma unroll
      for (int i = 0; i < 4; ++i)
        __builtin_amdgcn_raw_buffer_store_b128(u32x4{lo[i][0], lo[i][1], hi[i][0], hi[i][1]}, ur,
            ((n0 + (wave - 1) * 8 + i * 2 + (lane >> 5)) * PS_COPY + (7 - grp) * 256 + (lane & 31) * 8) * 2, 0, ST_AUX);
    }
  }
  STAMP(3);
  STAMP_FLUSH(0);
}

__device__ __forceinline__ void lds_wait(volatile LDS_AS unsigned* p, unsigned need) {
#pragma unroll 1
  for (int spin = 0; spin < (1 << 24); ++spin) {
    if (__builtin_amdgcn_readfirstlane(*p) >= need) break;
    __builtin_amdgcn_s_sleep(2);
  }
  asm volatile("" ::: "memory");
}

__device__ __forceinline__ void cnt_add(char* ws, int byte_off) {
  __hip_atomic_fetch_add((GLB_AS unsigned*)(ws + WS_CNT + byte_off), 1u, __ATOMIC_RELAXED, __HIP_MEMORY_SCOPE_AGENT);
}
template <int SLP>
__device__ __forceinline__ void cnt_wait(char* ws, int byte_off, unsigned need) {
  GLB_AS unsigned* cp = (GLB_AS unsigned*)(ws + WS_CNT + byte_off);
#pragma unroll 1
  for (int spin = 0; spin < (1 << 22); ++spin) {
    const unsigned v = __hip_atomic_load(cp, __ATOMIC_RELAXED, __HIP_MEMORY_SCOPE_AGENT);
    if (__all(v >= need)) break;
    __builtin_amdgcn_s_sleep(SLP);
  }
  asm volatile("" ::: "memory");
}

template <int N0>
__device__ __forceinline__ void ssm_unit(int c, const u16* __restrict__ hk, const u16* __restrict__ dk,
                                         char* ws, int lane) {
  constexpr int NM = N0 + 4;
  const int i = lane & 15, g = lane >> 4;
  const int pso = (int)(WS_PS - WS_UR) + (c + (N0 ? 1 : 0)) * (PS_COPY * 2);
  const __amdgpu_buffer_rsrc_t ur = __builtin_amdgcn_make_buffer_rsrc((void*)(ws + WS_UR + (size_t)c * (PS_COPY * 2)), 0, pso + PS_COPY * 2, 0x00020000);
  int ao = ((i & 8) ? 0 : pso) + (2030 - 256 * (NM - 1) - 2 * (i & 7) + 8 * g) * 2;
  const __amdgpu_buffer_rsrc_t hr = __builtin_amdgcn_make_buffer_rsrc((void*)(hk + (size_t)c * S_LEN), 0, S_LEN * 2, 0x00020000);
  int bo = (16 * i - 16 + 8 * g) * 2, bo1 = bo + 512;
  asm volatile("" : "+v"(bo1), "+v"(ao));
  f32x4 acc[4];
#pragma unroll
  for (int n = 0; n < 4; ++n) acc[n] = f32x4{0.f, 0.f, 0.f, 0.f};
  u32x4 A[NM], B[NM];
  auto ldA = [&](int m) { A[m] = __builtin_amdgcn_raw_buffer_load_b128(ur, ao, 512 * (NM - 1 - m), 0); };
  auto ldB = [&](int j) {
    if (j == 0) B[0] = __builtin_amdgcn_raw_buffer_load_b128(hr, bo, 0, 0);
    else B[j] = __builtin_amdgcn_raw_buffer_load_b128(hr, bo1, 512 * (j - 1), 0);
  };
#pragma unroll
  for (int j = 0; j < NM; ++j) ldB(j);
  const float dd = bflo(dk[c]);
  {
    constexpr int NC = NM / 2;
    u32x4 d[NC]; unsigned nx[NC];
#pragma unroll
    for (int q = 0; q < NC; ++q) {
      d[q] = __builtin_amdgcn_raw_buffer_load_b128(ur, (4 - NC + q) * 1024 + lane * 16, 0, 0);
      nx[q] = __builtin_amdgcn_raw_buffer_load_b32(ur, (4 - NC + q) * 1024 + lane * 16 + 16, 0, 0);
    }
    if (lane < 2) __builtin_amdgcn_raw_buffer_store_b128(u32x4{0u, 0u, 0u, 0u}, ur, pso + 4096 + lane * 16, 0, 0);
#pragma unroll
    for (int q = 0; q < NC; ++q) {
      const u32x4 sh = {__builtin_amdgcn_alignbit(d[q][1], d[q][0], 16), __builtin_amdgcn_alignbit(d[q][2], d[q][1], 16),
                        __builtin_amdgcn_alignbit(d[q][3], d[q][2], 16), __builtin_amdgcn_alignbit(nx[q], d[q][3], 16)};
      __builtin_amdgcn_raw_buffer_store_b128(sh, ur, pso + (4 - NC + q) * 1024 + lane * 16, 0, 0);
    }
    wait_vm<0>();
  }
#pragma unroll
  for (int m = 0; m < NM; ++m) ldA(m);
  u32x2 u[4];
#pragma unroll
  for (int n = 0; n < 4; ++n) {
    int uo = (2044 - (256 * (N0 + n) + 16 * i + 8 * (g & 1) + 4 * (g >> 1))) * 2;
    asm volatile("" : "+v"(uo));
    u[n] = __builtin_amdgcn_raw_buffer_load_b64(ur, uo, 0, 0);
  }
  if (N0) {
#pragma unroll 1
    for (int rho = 0; rho < 8; ++rho) {
      if (rho < 7) { ao -= 64; bo -= 64; bo1 -= 64; asm volatile("" : "+v"(ao), "+v"(bo), "+v"(bo1)); }
#pragma unroll
      for (int m = 0; m < NM; ++m) {
#pragma unroll
        for (int n = 3; n >= 0; --n)
          if (N0 + n - m >= 0)
            acc[n] = MFMA16(__builtin_bit_cast(bf16x8, A[m]), __builtin_bit_cast(bf16x8, B[N0 + n - m]), acc[n]);
        __builtin_amdgcn_sched_barrier(0);
        ldA(m); ldB(NM - 1 - m);
        __builtin_amdgcn_sched_barrier(0);
      }
    }
  } else {
    u32x4 A2[NM], B2[NM];
    auto round = [&](u32x4 (&Ac)[NM], u32x4 (&Bc)[NM], u32x4 (&An)[NM], u32x4 (&Bn)[NM]) {
#pragma unroll
      for (int m = 0; m < NM; ++m) {
        An[m] = __builtin_amdgcn_raw_buffer_load_b128(ur, ao, 512 * (NM - 1 - m), 0);
        Bn[m] = m == 0 ? __builtin_amdgcn_raw_buffer_load_b128(hr, bo, 0, 0) : __builtin_amdgcn_raw_buffer_load_b128(hr, bo1, 512 * (m - 1), 0);
      }
      __builtin_amdgcn_sched_barrier(0);
#pragma unroll
      for (int m = 0; m < NM; ++m)
#pragma unroll
        for (int n = 3; n >= m; --n)
          acc[n] = MFMA16(__builtin_bit_cast(bf16x8, Ac[m]), __builtin_bit_cast(bf16x8, Bc[n - m]), acc[n]);
      __builtin_amdgcn_sched_barrier(0);
    };
#pragma unroll 1
    for (int rho = 0; rho < 8; rho += 2) {
      ao -= 64; bo -= 64; bo1 -= 64; asm volatile("" : "+v"(ao), "+v"(bo), "+v"(bo1));
      round(A, B, A2, B2);
      if (rho < 6) { ao -= 64; bo -= 64; bo1 -= 64; asm volatile("" : "+v"(ao), "+v"(bo), "+v"(bo1)); }
      round(A2, B2, A, B);
    }
  }
  const __amdgpu_buffer_rsrc_t zr = __builtin_amdgcn_make_buffer_rsrc((void*)(ws + WS_Z + (size_t)c * (S_LEN * 2)), 0, S_LEN * 2, 0x00020000);
#pragma unroll
  for (int n = 0; n < 4; ++n) {
    const int tq = 256 * (N0 + n) + 16 * i + 8 * (g & 1) + 4 * (g >> 1);
    const auto s02 = __builtin_amdgcn_permlane32_swap(__float_as_uint(acc[n][0]), __float_as_uint(acc[n][2]), false, false);
    const auto s13 = __builtin_amdgcn_permlane32_swap(__float_as_uint(acc[n][1]), __float_as_uint(acc[n][3]), false, false);
    const float z0 = __uint_as_float(s02[0]) + bfhi(u[n][1]) * dd;
    const float z1 = __uint_as_float(s02[1]) + bflo(u[n][1]) * dd;
    const float z2 = __uint_as_float(s13[0]) + bfhi(u[n][0]) * dd;
    const float z3 = __uint_as_float(s13[1]) + bflo(u[n][0]) * dd;
    __builtin_amdgcn_raw_buffer_store_b64(u32x2{pack2(z0, z1), pack2(z2, z3)}, zr, tq * 2, 0, 16);
  }
  wait_vm<0>();
  if (lane == 0) cnt_add(ws, (CNT_Z + (c >> 6) * 2 + (N0 ? 1 : 0)) * 128);
}

__device__ __forceinline__ f32x16 qk(bf16x8 k1, s16x4 k2, bf16x8 q1, s16x4 q2) {
  f32x16 z;
#pragma unroll
  for (int i = 0; i < 16; ++i) z[i] = 0.f;
  f32x16 s = __builtin_amdgcn_mfma_f32_32x32x16_bf16(k1, q1, z, 0, 0, 0);
  return __builtin_amdgcn_mfma_f32_32x32x8bf16_1k(k2, q2, s, 0, 0, 0);
}
__device__ __forceinline__ float max16(f32x16 s) {
  float m = fmaxf(s[0], s[1]);
#pragma unroll
  for (int i = 2; i < 16; ++i) m = fmaxf(m, s[i]);
  return m;
}
__device__ __forceinline__ s16x4 mk2(unsigned a, unsigned b) { return __builtin_bit_cast(s16x4, u32x2{a, b}); }
__device__ __forceinline__ float swap32_max(float m) {
  const auto s = __builtin_amdgcn_permlane32_swap(__float_as_uint(m), __float_as_uint(m), false, false);
  return fmaxf(__uint_as_float(s[0]), __uint_as_float(s[1]));
}

#define ATTN_CLOB "v96","v97","v98","v99","v100","v101","v102","v103","v104","v105","v106","v107","v108","v109","v110","v111", \
  "v112","v113","v114","v115","v116","v117","v118","v119","v120","v121","v122","v123","v124","v125","v126","v127", \
  "v128","v129","v130","v131","v132","v133","v134","v135","v136","v137","v138","v139","v140","v141","v142","v143"
#define EX1(a) "v_exp_f32 v" #a ", v" #a "\n"
#define EX2(a, b) EX1(a) EX1(b)
#define EX3(a, b, c) EX1(a) EX1(b) EX1(c)
#define EX4(a, b, c, d) EX1(a) EX1(b) EX1(c) EX1(d)
#define ATILE(OVHI, OK1N, K2C, QB2, K2N, QA2, OVLON, X3, X4, X6, X7) \
  EX2(96, 97) \
  "v_smfmac_f32_32x32x32_bf16 %[ob], v[140:143], v[120:127], %[idx]\n" \
  "ds_read_b128 v[140:143], %[av] offset:" #OVHI "\n" \
  EX3(98, 99, 100) \
  "v_mfma_f32_32x32x16_bf16 v[112:127], v[128:131], %[qb1], 0\n" \
  "ds_read_b128 v[128:131], %[ak1] offset:" #OK1N "\n" \
  EX3(101, 102, 103) \
  "v_mfma_f32_32x32x8_bf16 v[112:127], " K2C ", %[" QB2 "], v[112:127]\n" \
  X3 \
  EX4(104, 105, 106, 107) \
  "v_smfmac_f32_32x32x32_bf16 %[oa], v[136:139], v[96:103], %[idx]\n" \
  X4 \
  EX4(108, 109, 110, 111) \
  EX2(112, 113) \
  "v_smfmac_f32_32x32x32_bf16 %[oa], v[140:143], v[104:111], %[idx]\n" \
  "s_waitcnt lgkmcnt(0)\n" \
  EX3(114, 115, 116) \
  "v_mfma_f32_32x32x16_bf16 v[96:111], v[128:131], %[qa1], 0\n" \
  X6 \
  EX3(117, 118, 119) \
  "v_mfma_f32_32x32x8_bf16 v[96:111], " K2N ", %[" QA2 "], v[96:111]\n" \
  X7 \
  EX4(120, 121, 122, 123) \
  "v_smfmac_f32_32x32x32_bf16 %[ob], v[136:139], v[112:119], %[idx]\n" \
  "ds_read_b128 v[136:139], %[av] offset:" #OVLON "\n" \
  EX4(124, 125, 126, 127)
#define WL2 "s_waitcnt lgkmcnt(2)\n"
#define WL1 "s_waitcnt lgkmcnt(1)\n"
#define NOP0 "s_nop 0\n"
#define ATTN_LOOP_ASM \
  "ds_read_b128 v[128:131], %[ak1]\n" \
  "ds_read_b128 v[132:135], %[ak2]\n" \
  "ds_read_b128 v[136:139], %[av]\n" \
  "v_mov_b32 v140, 0\n v_mov_b32 v141, 0\n v_mov_b32 v142, 0\n v_mov_b32 v143, 0\n" \
  "v_mov_b32 v120, 0\n v_mov_b32 v121, 0\n v_mov_b32 v122, 0\n v_mov_b32 v123, 0\n" \
  "v_mov_b32 v124, 0\n v_mov_b32 v125, 0\n v_mov_b32 v126, 0\n v_mov_b32 v127, 0\n" \
  "s_waitcnt lgkmcnt(1)\n" \
  "v_mfma_f32_32x32x16_bf16 v[96:111], v[128:131], %[qa1], 0\n" \
  "v_mfma_f32_32x32x8_bf16 v[96:111], v[132:133], %[qa2e], v[96:111]\n" \
  "s_nop 11\n" \
  "L_attn_loop_%=:\n" \
  ATILE(640, 1024, "v[132:133]", "qb2e", "v[132:133]", "qa2o", 1280, WL2, WL1, "", NOP0) \
  ATILE(1920, 2048, "v[132:133]", "qb2o", "v[134:135]", "qa2e", 2560, WL2, WL1, "", NOP0) \
  ATILE(3200, 3072, "v[134:135]", "qb2e", "v[134:135]", "qa2o", 3840, WL2, WL1, "s_cmp_eq_u32 %[it], %[chk]\n", \
        "s_cbranch_scc0 L_attn_nobar_%=\n L_attn_spin_%=:\n ds_read_b32 %[tmp], %[syn]\n s_add_i32 %[chk], %[chk], 16\n s_waitcnt lgkmcnt(0)\n" \
        " v_readfirstlane_b32 %[stmp], %[tmp]\n s_cmp_gt_u32 %[chk], 0x10000000\n s_cbranch_scc1 L_attn_nobar_%=\n s_cmp_lt_u32 %[stmp], 4\n" \
        " s_cbranch_scc1 L_attn_spin_%=\n L_attn_nobar_%=:\n") \
  ATILE(4480, 9744, "v[134:135]", "qb2o", "v[132:133]", "qa2e", 9744, \
        "ds_read_b128 v[132:135], %[ak2] offset:9744\n s_waitcnt lgkmcnt(3)\n", WL2, "s_add_i32 %[it], %[it], 1\n", "s_cmp_lt_i32 %[it], %[nit]\n") \
  "v_add_u32 %[ak1], 9744, %[ak1]\n" \
  "v_add_u32 %[ak2], 9744, %[ak2]\n" \
  "v_add_u32 %[av], 9744, %[av]\n" \
  "s_cbranch_scc1 L_attn_loop_%=\n" \
  "v_smfmac_f32_32x32x32_bf16 %[ob], v[140:143], v[120:127], %[idx]\n" \
  "s_waitcnt lgkmcnt(0)\n" \
  "s_nop 15\n"

__device__ __forceinline__ void attn_exact(const char* img, int offK1, int offK2, int offV, int hh, bf16x8 qa1, bf16x8 qb1,
                                           unsigned qa2, unsigned qb2, f32x16& Oa, f32x16& Ob) {
  const unsigned xa = hh ? 0u : qa2, xb = hh ? 0u : qb2;
  float ma = -INFINITY, mb = -INFINITY;
#pragma unroll 1
  for (int tile = 0; tile < 64; ++tile) {
    const char* base = img + (tile >> 2) * SG_BYTES;
    const s16x4 kk = mk2(*(const unsigned*)(base + offK2 + (hh ? 0 : (tile & 3) * 4)), 0u);
    const bf16x8 k1 = *(const bf16x8*)(base + offK1 + (tile & 3) * 1024);
    ma = fmaxf(ma, max16(qk(k1, kk, qa1, mk2(xa, 0u))));
    mb = fmaxf(mb, max16(qk(k1, kk, qb1, mk2(xb, 0u))));
  }
  ma = swap32_max(ma); mb = swap32_max(mb);
  const s16x4 ea = mk2(hh ? pack2(-ma, 0.f) : qa2, 0u), eb = mk2(hh ? pack2(-mb, 0.f) : qb2, 0u);
#pragma unroll
  for (int i = 0; i < 16; ++i) { Oa[i] = 0.f; Ob[i] = 0.f; }
#pragma unroll 1
  for (int tile = 0; tile < 64; ++tile) {
    const char* base = img + (tile >> 2) * SG_BYTES;
    const s16x4 kk = mk2(*(const unsigned*)(base + offK2 + (hh ? 0 : (tile & 3) * 4)), 0u);
    const bf16x8 k1 = *(const bf16x8*)(base + offK1 + (tile & 3) * 1024);
    const bf16x8 vlo = *(const bf16x8*)(base + offV + (tile & 3) * 1280), vhi = *(const bf16x8*)(base + offV + (tile & 3) * 1280 + 640);
    f32x16 S = qk(k1, kk, qa1, ea);
#pragma unroll
    for (int i = 0; i < 16; ++i) S[i] = __builtin_amdgcn_exp2f(S[i]);
    Oa = __builtin_amdgcn_smfmac_f32_32x32x32_bf16(vlo, __builtin_bit_cast(bf16x16, __builtin_shufflevector(S, S, 0, 1, 2, 3, 4, 5, 6, 7)), Oa, 0xDDDDDDDD, 0, 0);
    Oa = __builtin_amdgcn_smfmac_f32_32x32x32_bf16(vhi, __builtin_bit_cast(bf16x16, __builtin_shufflevector(S, S, 8, 9, 10, 11, 12, 13, 14, 15)), Oa, 0xDDDDDDDD, 0, 0);
    S = qk(k1, kk, qb1, eb);
#pragma unroll
    for (int i = 0; i < 16; ++i) S[i] = __builtin_amdgcn_exp2f(S[i]);
    Ob = __builtin_amdgcn_smfmac_f32_32x32x32_bf16(vlo, __builtin_bit_cast(bf16x16, __builtin_shufflevector(S, S, 0, 1, 2, 3, 4, 5, 6, 7)), Ob, 0xDDDDDDDD, 0, 0);
    Ob = __builtin_amdgcn_smfmac_f32_32x32x32_bf16(vhi, __builtin_bit_cast(bf16x16, __builtin_shufflevector(S, S, 8, 9, 10, 11, 12, 13, 14, 15)), Ob, 0xDDDDDDDD, 0, 0);
  }
}

#define NLATE 6
constexpr int T_WO = 58624;
constexpr int T_XCH = T_WO + 73728;
constexpr int T_Z = 0;
constexpr int T_PART = 10240;
constexpr int T_EPI = 30720;
constexpr int T_SYNC = 163840 - 64;
static_assert(T_WO >= NLATE * SG_BYTES && T_XCH + 24576 <= T_SYNC && T_EPI + 12800 <= T_WO, "lds");
constexpr int P_EARLY = NLATE * SG_BYTES / 1024;

extern "C" __global__ void __launch_bounds__(768) k2_attn(const u16* __restrict__ hk, const u16* __restrict__ dk,
                                               const u16* __restrict__ x, const u16* __restrict__ wo,
                                               char* ws, u16* __restrict__ out) {
  __shared__ __attribute__((aligned(256))) char sm[163840];
  const int tid = threadIdx.x, lane = tid & 63;
  const int wave = __builtin_amdgcn_readfirstlane(tid >> 6);
  const int b = blockIdx.x;
  const int grp = b & 7, loc = b >> 3;
  const bool has_tile = loc < (grp < 2 ? 30 : 29);
  const int qid = has_tile ? grp * 29 + (grp < 2 ? grp : 2) + loc : 0;
  const int mt = qid / 9, nt = qid - mt * 9;
  const int t0 = mt * 80, n0 = nt * 64;
  const __amdgpu_buffer_rsrc_t orsrc = __builtin_amdgcn_make_buffer_rsrc((void*)(ws + WS_O), 0, (int)O_BYTES, 0x00020000);
  STAMP_DECL;
  STAMP(0);

  volatile LDS_AS unsigned* sync = (volatile LDS_AS unsigned*)(sm + T_SYNC);
  if (wave < 4) {
    __builtin_amdgcn_s_setprio(3);
    const int W = b + 256 * wave;
    if (wave == 0 && lane < 8) sync[lane] = 0u;
    RAW_BARRIER();
    lds_wait(sync + 5, 4u);
    STAMP(1);
    if (wave == 3) {
#pragma unroll
      for (int i = 0; i < 9; ++i)
        __builtin_amdgcn_raw_buffer_store_b128(u32x4{O_EMPTY, O_EMPTY, O_EMPTY, O_EMPTY}, orsrc,
                                               ((b >> 1) * 72 + i * 8 + (lane >> 3)) * 256 + (b & 1) * 128 + (lane & 7) * 16, 0, 16);
      wait_vm<0>();
      if (lane == 0) { cnt_add(ws, (CNT_F + b / 10) * 128); sync[6] = 1u; }
    }
    STAMP(9);
    if (W < 576) {
      ssm_unit<4>(W, hk, dk, ws, lane);
    } else {
      for (int c = W - 576; c < 576; c += 448) ssm_unit<0>(c, hk, dk, ws, lane);
    }
    STAMP(2);
  } else {
    const int pair = wave & 3;
    const bool early = wave < 8;
    __builtin_amdgcn_s_setprio(2);
    const char* src = ws + WS_IMG + lane * 16;
#pragma unroll
    for (int i = 0; i < 3; ++i) { const int p = P_EARLY + (wave - 4) + 8 * i; glds16(src + p * 1024, sm + p * 1024); }
    const int r = lane & 31, hh = lane >> 5;
    const int tq = b * 8 + pair * 2;
    const u16* Q = (const u16*)(ws + WS_Q) + (size_t)tq * DM + r * DH;
    const bf16x8 qa1 = __builtin_bit_cast(bf16x8, (u32x4)*(const u32x4_a4*)(Q + hh * 8));
    const bf16x8 qb1 = __builtin_bit_cast(bf16x8, (u32x4)*(const u32x4_a4*)(Q + DM + hh * 8));
    const unsigned qa2 = *(const unsigned*)(Q + 16), qb2 = *(const unsigned*)(Q + DM + 16);
    const int offK1 = SG_K1 + lane * 16;
    const int offK2 = SG_K2 + (hh ? 512 : r * 16);
    const int offV = SG_V + hh * 320 + vpos(r) * 16;
    const int idx = 0xDDDDDDDD;
    const unsigned ldsBase = (unsigned)(uintptr_t)(LDS_AS char*)sm + (early ? NLATE * SG_BYTES : 0);

    wait_vm<0>();
    RAW_BARRIER();
    STAMP(1);
    const bf16x8 k1 = *(const bf16x8*)(sm + NLATE * SG_BYTES + offK1);
    const u32x2 k2 = *(const u32x2*)(sm + NLATE * SG_BYTES + offK2);
    WAIT_LGKM0();
    if (!early) {
      __builtin_amdgcn_s_setprio(3);
#pragma unroll
      for (int i = 0; i < 33; ++i) {
        int p = P_EARLY + 24 + pair + 4 * i;
        p = p > 152 ? p - 153 : p;
        glds16(src + p * 1024, sm + p * 1024);
      }
      wait_vm<0>();
      if (lane == 0) __hip_atomic_fetch_add((LDS_AS unsigned*)sync + 5, 1u, __ATOMIC_RELAXED, __HIP_MEMORY_SCOPE_WORKGROUP);
      lds_wait(sync + 5, 4u);
      __builtin_amdgcn_s_setprio(1);
      STAMP(9);
    }

    f32x16 Oa, Ob;
    {
      const unsigned xa = hh ? 0u : qa2, xb = hh ? 0u : qb2;
      const float ma = swap32_max(max16(qk(k1, mk2(k2[0], k2[1]), qa1, mk2(xa, 0u))));
      const float mb = swap32_max(max16(qk(k1, mk2(k2[0], k2[1]), qb1, mk2(xb, 0u))));
      const unsigned nma = pack2(-ma, 0.f), nmb = pack2(-mb, 0.f);
      const s16x4 qa2e = mk2(hh ? nma : qa2, 0u), qa2o = mk2(hh ? nma : 0u, xa);
      const s16x4 qb2e = mk2(hh ? nmb : qb2, 0u), qb2o = mk2(hh ? nmb : 0u, xb);
#pragma unroll
      for (int i = 0; i < 16; ++i) { Oa[i] = 0.f; Ob[i] = 0.f; }
      unsigned aK1 = ldsBase + offK1, aK2 = ldsBase + offK2, aV = ldsBase + offV;
      int it = 0;
      const int nit = early ? 16 - NLATE : NLATE;
      int chk = early ? 1 : 99, stmp;
      unsigned tmp;
      const unsigned syn = (unsigned)(uintptr_t)(sync + 5);
      asm volatile(ATTN_LOOP_ASM
                   : [oa] "+v"(Oa), [ob] "+v"(Ob), [ak1] "+v"(aK1), [ak2] "+v"(aK2), [av] "+v"(aV), [it] "+s"(it),
                     [chk] "+s"(chk), [stmp] "=&s"(stmp), [tmp] "=&v"(tmp)
                   : [qa1] "v"(qa1), [qb1] "v"(qb1), [qa2e] "v"(qa2e), [qa2o] "v"(qa2o), [qb2e] "v"(qb2e), [qb2o] "v"(qb2o),
                     [idx] "v"(idx), [nit] "s"(nit), [syn] "v"(syn)
                   : "memory", "scc", ATTN_CLOB);
    }
    STAMP(2);
    f32x4* xch = (f32x4*)(sm + T_XCH + pair * 6144) + lane;
    if (early) {
      if (lane == 0) __hip_atomic_fetch_add((LDS_AS unsigned*)sync, 1u, __ATOMIC_RELAXED, __HIP_MEMORY_SCOPE_WORKGROUP);
      lds_wait(sync, 4u);
      lds_wait(sync + 6, 1u);
#pragma unroll
      for (int i = 0; i < 3; ++i) {
        xch[i * 64] = f32x4{Oa[4 * i], Oa[4 * i + 1], Oa[4 * i + 2], Oa[4 * i + 3]};
        xch[(3 + i) * 64] = f32x4{Ob[4 * i], Ob[4 * i + 1], Ob[4 * i + 2], Ob[4 * i + 3]};
      }
      WAIT_LGKM0();
      if (lane == 0) sync[1 + pair] = 1u;
      const char* wsrc = (const char*)(wo + n0) + (size_t)(pair * 144 + (lane >> 3)) * (DM * 2);
      const int c0 = ((lane & 7) ^ (wsw(lane >> 3) << 1)) << 4;
#pragma unroll
      for (int i = 0; i < 18; ++i) glds16(wsrc + i * 8 * DM * 2 + (c0 ^ ((i & 1) << 6)), sm + T_WO + pair * 18432 + i * 1024);
    } else {
      lds_wait(sync + 1 + pair, 1u);
      float big = 0.f;
#pragma unroll
      for (int i = 0; i < 3; ++i) {
        const f32x4 pa = xch[i * 64], pb = xch[(3 + i) * 64];
#pragma unroll
        for (int e = 0; e < 4; ++e) if (4 * i + e < 11) {
          Oa[4 * i + e] += pa[e]; Ob[4 * i + e] += pb[e];
          big += fabsf(Oa[4 * i + e]) + fabsf(Ob[4 * i + e]);
        }
      }
      if (__any(!(big < 1e30f))) attn_exact(ws + WS_IMG, offK1, offK2, offV, hh, qa1, qb1, qa2, qb2, Oa, Ob);
      STAMP(3);
      const auto la = __builtin_amdgcn_permlane32_swap(__float_as_uint(Oa[10]), __float_as_uint(Oa[10]), false, false);
      const auto lb = __builtin_amdgcn_permlane32_swap(__float_as_uint(Ob[10]), __float_as_uint(Ob[10]), false, false);
      const float ia = __builtin_amdgcn_rcpf(__uint_as_float(la[0])), ib = __builtin_amdgcn_rcpf(__uint_as_float(lb[0]));
      char* ost = sm + T_XCH + pair * 6144;
      char* od = ost + r * 36 + hh * 8;
      *(unsigned*)(od) = pack2(Oa[0] * ia, Oa[1] * ia);        *(unsigned*)(od + 1168) = pack2(Ob[0] * ib, Ob[1] * ib);
      *(unsigned*)(od + 4) = pack2(Oa[2] * ia, Oa[3] * ia);    *(unsigned*)(od + 1172) = pack2(Ob[2] * ib, Ob[3] * ib);
      *(unsigned*)(od + 16) = pack2(Oa[4] * ia, Oa[5] * ia);   *(unsigned*)(od + 1184) = pack2(Ob[4] * ib, Ob[5] * ib);
      *(unsigned*)(od + 20) = pack2(Oa[6] * ia, Oa[7] * ia);   *(unsigned*)(od + 1188) = pack2(Ob[6] * ib, Ob[7] * ib);
      if (!hh) { *(unsigned*)(od + 32) = pack2(Oa[8] * ia, Oa[9] * ia); *(unsigned*)(od + 1200) = pack2(Ob[8] * ib, Ob[9] * ib); }
      u32x4 ov[3];
#pragma unroll
      for (int i = 0; i < 3; ++i) ov[i] = *(const u32x4*)(ost + (lane & 1) * 1168 + (i * 32 + (lane >> 1)) * 16);
#pragma unroll
      for (int i = 0; i < 3; ++i) {
#pragma unroll
        for (int e = 0; e < 4; ++e) ov[i][e] = min(ov[i][e], O_EMPTY - 1u);
        if (i < 2 || lane < 16)
          __builtin_amdgcn_raw_buffer_store_b128(ov[i], orsrc, (((tq >> 4) * 72 + i * 32 + (lane >> 1)) * 16 + (tq & 15) + (lane & 1)) * 16, 0, 16);
      }
    }
  }
  STAMP(4);
  if (!has_tile) { wait_vm<0>(); STAMP_FLUSH(1); return; }

  const int cwi = wave - 2;
  const int kh = cwi >= 5 ? 1 : 0, cw = cwi - kh * 5;
  const int g = lane >> 4, tl = lane & 15;
  u32x2 xv[2]; float gt;
  if (wave >= 2) {
    const int te = min(t0 + cw * 16 + tl, S_LEN - 1);
#pragma unroll
    for (int h = 0; h < 2; ++h) xv[h] = *(const u32x2*)(x + (size_t)te * DM + n0 + kh * 32 + h * 16 + g * 4);
    gt = ((const float*)(ws + WS_G))[te];
  }
  if (wave == 0) {
    const int ci = lane == 0 ? CNT_F + mt : CNT_Z + nt * 2 + ((lane == 1 ? t0 : t0 + 79) < 1024 ? 0 : 1);
    cnt_wait<2>(ws, ci * 128, lane == 0 ? (mt == 25 ? 6u : 10u) : 64u);
  }
  if (wave >= 4 && wave < 8) wait_vm<0>();
  RAW_BARRIER();
  STAMP(5);
  if (wave < 2) {
    const __amdgpu_buffer_rsrc_t zrsrc = __builtin_amdgcn_make_buffer_rsrc((void*)(ws + WS_Z), 0, (int)Z_BYTES, 0x00020000);
    u32x4 zv[5];
#pragma unroll
    for (int i = 0; i < 5; ++i) {
      const int ci = (wave * 5 + i) * 64 + lane, c = ci / 10, k = ci - c * 10;
      zv[i] = __builtin_amdgcn_raw_buffer_load_b128(zrsrc, ((n0 + c) * S_LEN + t0 + k * 8) * 2, 0, 16);
    }
#pragma unroll
    for (int i = 0; i < 5; ++i) *(u32x4*)(sm + T_Z + ((wave * 5 + i) * 64 + lane) * 16) = zv[i];
    WAIT_LGKM0();
  } else {
    float bx[2][4];
    const float omg = 1.0f - gt;
#pragma unroll
    for (int h = 0; h < 2; ++h) {
      bx[h][0] = omg * bflo(xv[h][0]); bx[h][1] = omg * bfhi(xv[h][0]);
      bx[h][2] = omg * bflo(xv[h][1]); bx[h][3] = omg * bfhi(xv[h][1]);
    }
    const int rgp = mt * 5 + cw;
    const int obase = ((rgp * 18 + kh * 9) * 64 + lane) * 16;
    const char* wl = sm + T_WO + kh * 9 * 4096;
    constexpr int NPRE = 5;
    bf16x8 wpre[NPRE][4];
    int so[9];
#pragma unroll
    for (int s = 0; s < 9; ++s) so[s] = s + nt >= 9 ? s + nt - 9 : s + nt;
#pragma unroll
    for (int s = 0; s < NPRE; ++s)
#pragma unroll
      for (int nb = 0; nb < 4; ++nb) wpre[s][nb] = w_frag(wl + so[9 - NPRE + s] * 4096, lane, 0, nb);
    unsigned wq4[4];
#pragma unroll
    for (int nb = 0; nb < 4; ++nb) {
      const int k = g * 8 + (tl >> 2);
      wq4[nb] = (unsigned)(uintptr_t)(LDS_AS char*)sm + T_WO + kh * 9 * 4096 + k * 128 + ((nb ^ wsw(k)) << 5) + (lane & 3) * 8;
    }
#pragma unroll 1
    for (int spin = 0; spin < (1 << 22); ++spin) {
      asm volatile("" ::: "memory");
      const unsigned v = __builtin_amdgcn_raw_buffer_load_b32(orsrc, obase, nt * 1024, 16);
      if (__all(v != O_EMPTY)) break;
      __builtin_amdgcn_s_sleep(1);
    }
    STAMP(6);
    f32x4 acc[4];
#pragma unroll 1
    for (int spin = 0; spin < (1 << 20); ++spin) {
      asm volatile("" : "+v"(wq4[0]), "+v"(wq4[1]), "+v"(wq4[2]), "+v"(wq4[3]) :: "memory");
      u32x4 of[9];
#pragma unroll
      for (int s = 0; s < 9; ++s) of[s] = __builtin_amdgcn_raw_buffer_load_b128(orsrc, obase, so[s] * 1024, 16);
      __builtin_amdgcn_sched_barrier(0);
#pragma unroll
      for (int nb = 0; nb < 4; ++nb) acc[nb] = f32x4{0.f, 0.f, 0.f, 0.f};
      unsigned mx = 0;
#pragma unroll
      for (int s = 0; s < 9; ++s) {
#pragma unroll
        for (int nb = 0; nb < 4; ++nb) {
          bf16x8 wf;
          if (s >= 9 - NPRE) wf = wpre[s - (9 - NPRE)][nb];
          else {
            LDS_AS bf16x4* wp = (LDS_AS bf16x4*)(uintptr_t)(wq4[nb] + so[s] * 4096);
            wf = __builtin_shufflevector(__builtin_amdgcn_ds_read_tr16_b64_v4bf16(wp), __builtin_amdgcn_ds_read_tr16_b64_v4bf16(wp + 64), 0, 1, 2, 3, 4, 5, 6, 7);
          }
          acc[nb] = MFMA16(wf, __builtin_bit_cast(bf16x8, of[s]), acc[nb]);
        }
        mx = max(max(mx, max(of[s][0], of[s][1])), max(of[s][2], of[s][3]));
      }
      if (__all(mx != O_EMPTY)) break;
    }
    f32x4* pd = (f32x4*)(sm + T_PART + cwi * 2048) + lane;
    pd[0] = kh ? acc[0] : acc[2];
    pd[64] = kh ? acc[1] : acc[3];
    STAMP(7);
    RAW_BARRIER();
    const f32x4* ps = (const f32x4*)(sm + T_PART + (kh ? cw : cw + 5) * 2048) + lane;
    char* sE = sm + T_EPI + cwi * 1280;
    f32x4 pa[2]; u32x2 z[2];
#pragma unroll
    for (int h = 0; h < 2; ++h) {
      pa[h] = ps[h * 64];
      z[h] = __builtin_bit_cast(u32x2, tr_read(sm + T_Z + ((kh * 2 + h) * 16 + g * 4 + (tl >> 2)) * 160 + (cw * 16 + (tl & 3) * 4) * 2));
    }
    asm volatile("" ::: "memory");
#pragma unroll
    for (int h = 0; h < 2; ++h) {
      const f32x4 a = (kh ? acc[2 + h] : acc[h]) + pa[h];
      *(u32x2*)(sE + tl * 80 + h * 32 + g * 8) =
          u32x2{pack2(gt * a[0] + bx[h][0] * bflo(z[h][0]), gt * a[1] + bx[h][1] * bfhi(z[h][0])),
                pack2(gt * a[2] + bx[h][2] * bflo(z[h][1]), gt * a[3] + bx[h][3] * bfhi(z[h][1]))};
    }
    const u32x4 o = *(const u32x4*)(sE + (lane >> 2) * 80 + (lane & 3) * 16);
    if (t0 + cw * 16 < S_LEN) *(u32x4*)(out + (size_t)(t0 + cw * 16 + (lane >> 2)) * DM + n0 + kh * 32 + (lane & 3) * 8) = o;
  }
  STAMP(8);
  STAMP_FLUSH(1);
}

extern "C" __global__ void k9_end() {}

extern "C" void kernel_launch(void* const* d_in, const int* in_sizes, int n_in, void* d_out, int out_size,
                              void* d_ws, size_t ws_size, hipStream_t stream) {
  const u16* x = (const u16*)d_in[0];
  const u16* wq = (const u16*)d_in[1];
  const u16* wk = (const u16*)d_in[2];
  const u16* wv = (const u16*)d_in[3];
  const u16* wo = (const u16*)d_in[4];
  const u16* bk = (const u16*)d_in[6];
  const u16* bv = (const u16*)d_in[7];
  const u16* shk = (const u16*)d_in[8];
  const u16* hk = (const u16*)d_in[9];
  const u16* dk = (const u16*)d_in[10];
  const u16* gw = (const u16*)d_in[11];
  char* ws = (char*)d_ws;
  k1_proj<<<248, 832, 0, stream>>>(x, wq, wk, wv, gw, bk, bv, shk, ws);
  k2_attn<<<256, 768, 0, stream>>>(hk, dk, x, wo, ws, (u16*)d_out);
}
```

```cpp
#include <hip/hip_runtime.h>
#include <hip/hip_bf16.h>
#include <stdint.h>

typedef __attribute__((ext_vector_type(4))) __bf16 bf16x4;
typedef __attribute__((ext_vector_type(8))) __bf16 bf16x8;
typedef __attribute__((ext_vector_type(16))) __bf16 bf16x16;
typedef __attribute__((ext_vector_type(4))) short s16x4;
typedef __attribute__((ext_vector_type(4))) float f32x4;
typedef __attribute__((ext_vector_type(16))) float f32x16;
typedef __attribute__((ext_vector_type(2))) unsigned u32x2;
typedef __attribute__((ext_vector_type(4))) unsigned u32x4;
typedef unsigned short u16;
typedef __attribute__((ext_vector_type(4), aligned(4))) unsigned u32x4_a4;

#define LDS_AS __attribute__((address_space(3)))
#define GLB_AS __attribute__((address_space(1)))

constexpr int S_LEN = 2048, DM = 576, DH = 18;
constexpr int PS_COPY = 2112;
constexpr int SG_K1 = 0, SG_K2 = 4096, SG_V = 4624, SG_BYTES = 9744;
constexpr int IMG_PIECES = 156;
constexpr int IMG_BYTES = IMG_PIECES * 1024;

constexpr size_t WS_CNT = 0;
constexpr size_t WS_Q = 8192;
constexpr size_t WS_IMG = WS_Q + (size_t)S_LEN * DM * 2;
constexpr size_t WS_UR = WS_IMG + IMG_BYTES;
constexpr size_t WS_PS = WS_UR + (size_t)DM * PS_COPY * 2;
constexpr size_t WS_G = WS_PS + (size_t)DM * 2 * PS_COPY * 2;
constexpr size_t WS_O = WS_G + S_LEN * 4;
constexpr size_t O_BYTES = (size_t)S_LEN * DM * 2;
constexpr size_t WS_Z = WS_O + O_BYTES;
constexpr size_t Z_BYTES = (size_t)S_LEN * DM * 2;
constexpr int CNT_Z = 0, CNT_F = 18;
constexpr unsigned O_EMPTY = 0xffffffffu;

#define STAMP_DECL do {} while (0)
#define STAMP(i) do {} while (0)
#define CSTAMP(i) do {} while (0)
#define STAMP_FLUSH(kern) do {} while (0)
#define WAIT_LGKM0() do { asm volatile("" ::: "memory"); __builtin_amdgcn_s_waitcnt(0xC07F); asm volatile("" ::: "memory"); } while (0)
#define RAW_BARRIER() do { WAIT_LGKM0(); __builtin_amdgcn_s_barrier(); asm volatile("" ::: "memory"); } while (0)
template <int N> __device__ __forceinline__ void wait_vm() {
  asm volatile("s_waitcnt vmcnt(%0)" :: "i"(N) : "memory");
  if (N == 0) __builtin_amdgcn_s_waitcnt(0x0F70);
}

__device__ __forceinline__ float bflo(unsigned w) { return __uint_as_float(w << 16); }
__device__ __forceinline__ float bfhi(unsigned w) { return __uint_as_float(w & 0xffff0000u); }
__device__ __forceinline__ unsigned pack2(float lo, float hi) {
  typedef __attribute__((ext_vector_type(2))) __bf16 bf16x2;
  bf16x2 v; v[0] = (__bf16)lo; v[1] = (__bf16)hi;
  return __builtin_bit_cast(unsigned, v);
}
__device__ __forceinline__ float rbf(float x) { return (float)(__bf16)x; }
__device__ __forceinline__ void glds16(const void* g, void* lds) {
  __builtin_amdgcn_global_load_lds((const GLB_AS unsigned*)g, (LDS_AS unsigned*)lds, 16, 0, 0);
}
__device__ __forceinline__ bf16x4 tr_read(const char* p) {
  return __builtin_amdgcn_ds_read_tr16_b64_v4bf16((LDS_AS bf16x4*)p);
}
__device__ __forceinline__ int vpos(int d) {
  const int a = d - (d > 11 && d < 16);
  return d > 18 ? 15 : (d == 11 ? 19 : a);
}
#define MFMA16(a, b, c) __builtin_amdgcn_mfma_f32_16x16x32_bf16(a, b, c, 0, 0, 0)

__device__ __forceinline__ int wsw(int k) { return ((k >> 1) & 1) | ((k >> 2) & 2); }
__device__ __forceinline__ bf16x8 a_frag(const char* sA, int lane, int rg, int s) {
  const int row = rg * 16 + (lane & 15);
  return *(const bf16x8*)(sA + row * 128 + (((s * 4 + (lane >> 4)) ^ ((row >> 1) & 7)) << 4));
}
__device__ __forceinline__ bf16x8 w_frag(const char* sW, int lane, int s, int nb) {
  const int k = s * 32 + (lane >> 4) * 8 + ((lane & 15) >> 2);
  const char* b = sW + k * 128 + ((nb ^ wsw(k)) << 5) + (lane & 3) * 8;
  const bf16x4 lo = tr_read(b), hi = tr_read(b + 512);
  return __builtin_shufflevector(lo, hi, 0, 1, 2, 3, 4, 5, 6, 7);
}

template <int NP, int NSLOT, int SLOT, int DEPTH>
__device__ __forceinline__ void ring_loader(const char* const (&base)[NP], const int (&step)[NP], char* const (&dst)[NP], int rot) {
  auto issue = [&](int j) {
    int ch = j + rot; ch = ch >= 9 ? ch - 9 : ch;
#pragma unroll
    for (int i = 0; i < NP; ++i) glds16(base[i] + ch * step[i], dst[i] + (j % NSLOT) * SLOT);
  };
#pragma unroll
  for (int j = 0; j < DEPTH; ++j) issue(j);
#define RL_STEP(j) do { wait_vm<NP * ((8 - (j)) < (DEPTH - 1) ? (8 - (j)) : (DEPTH - 1))>(); __builtin_amdgcn_s_barrier(); \
    asm volatile("" ::: "memory"); if ((j) + DEPTH < 9) issue((j) + DEPTH); } while (0)
  RL_STEP(0); RL_STEP(1); RL_STEP(2); RL_STEP(3); RL_STEP(4); RL_STEP(5); RL_STEP(6); RL_STEP(7); RL_STEP(8);
#undef RL_STEP
}

#define K1_DEPTH 3
#define ST_AUX 16
__device__ __forceinline__ void code_prefetch_exit(bool both, int lane) {
  unsigned long long pc; unsigned d1, d2;
  asm volatile("s_getpc_b64 %0\nL_pf_%=:\n s_mov_b32 %1, k2_attn-L_pf_%=\n s_mov_b32 %2, k9_end-L_pf_%=" : "=s"(pc), "=s"(d1), "=s"(d2));
  const unsigned last = (both ? d2 : d1) - 4u;
  unsigned o[5], t[5];
#pragma unroll
  for (int i = 0; i < 5; ++i) o[i] = min((unsigned)(lane * 128 + i * 8192), last);
  asm volatile("global_load_dword %0, %5, %10\n global_load_dword %1, %6, %10\n global_load_dword %2, %7, %10\n"
               " global_load_dword %3, %8, %10\n global_load_dword %4, %9, %10\n s_endpgm"
               : "=&v"(t[0]), "=&v"(t[1]), "=&v"(t[2]), "=&v"(t[3]), "=&v"(t[4])
               : "v"(o[0]), "v"(o[1]), "v"(o[2]), "v"(o[3]), "v"(o[4]), "s"(pc) : "memory");
}

extern "C" __global__ void __launch_bounds__(704) k1_proj(const u16* __restrict__ x, const u16* __restrict__ wq,
                                               const u16* __restrict__ wk, const u16* __restrict__ wv,
                                               const u16* __restrict__ gw, const u16* __restrict__ bk,
                                               const u16* __restrict__ bv, const u16* __restrict__ shk, char* __restrict__ ws) {
  __shared__ __attribute__((aligned(256))) char sm[6 * 24576];
  const int tid = threadIdx.x, lane = tid & 63;
  const int wave = __builtin_amdgcn_readfirstlane(tid >> 6);
  const int grp = blockIdx.x & 7, loc = blockIdx.x >> 3;
  if (wave == 0) code_prefetch_exit(loc == 30, lane);
  const int g = lane >> 4, tl = lane & 15;
  STAMP_DECL;
  STAMP(0);
  if (loc < 18) {
    const int mh = loc >= 9 ? 1 : 0, nt = loc - mh * 9;
    const int t0 = (grp * 2 + mh) * 128, n0 = nt * 64;
    constexpr int SLOT = 24576, NSLOT = 6;
    if (wave < 7) {
      const int lw = wave - 1;
      int rot = nt + 4 * mh + grp; rot = rot >= 18 ? rot - 18 : (rot >= 9 ? rot - 9 : rot);
      const char* base[4]; int step[4]; char* dst[4];
#pragma unroll
      for (int i = 0; i < 4; ++i) {
        const int pc = lw + 6 * i;
        const bool isW = pc < 8;
        const int row = (isW ? pc : pc - 8) * 8 + (lane >> 3);
        const int sw = isW ? wsw(row) << 1 : (row >> 1) & 7;
        base[i] = (isW ? (const char*)(wq + n0) : (const char*)(x + (size_t)t0 * DM)) + (unsigned)row * (DM * 2) + (((lane & 7) ^ sw) << 4);
        step[i] = isW ? 64 * DM * 2 : 128;
        dst[i] = sm + pc * 1024;
      }
      ring_loader<4, NSLOT, SLOT, K1_DEPTH>(base, step, dst, rot);
    } else {
      const int cwv = wave - 7;
      const int nh = cwv & 1, rh = cwv >> 1;
      f32x4 acc[4][2];
#pragma unroll
      for (int i = 0; i < 4; ++i) { acc[i][0] = f32x4{0.f, 0.f, 0.f, 0.f}; acc[i][1] = acc[i][0]; }
      bf16x8 af[2][8], wf[2][4];
      auto rd = [&](int j, bf16x8 (&a)[8], bf16x8 (&w)[4]) {
        const char* sl = sm + (j >= NSLOT ? j - NSLOT : j) * SLOT;
#pragma unroll
        for (int s = 0; s < 2; ++s) {
#pragma unroll
          for (int h = 0; h < 2; ++h) w[s * 2 + h] = w_frag(sl, lane, s, nh * 2 + h);
#pragma unroll
          for (int i = 0; i < 4; ++i) a[s * 4 + i] = a_frag(sl + 8192, lane, rh * 4 + i, s);
        }
      };
      auto mm = [&](bf16x8 (&a)[8], bf16x8 (&w)[4]) {
#pragma unroll
        for (int s = 0; s < 2; ++s)
#pragma unroll
          for (int i = 0; i < 4; ++i)
#pragma unroll
            for (int h = 0; h < 2; ++h) acc[i][h] = MFMA16(w[s * 2 + h], a[s * 4 + i], acc[i][h]);
      };
      auto mix = [&]() {
#pragma unroll
        for (int i = 0; i < 8; ++i) { __builtin_amdgcn_sched_group_barrier(0x8, 1, 0); __builtin_amdgcn_sched_group_barrier(0x100, 2, 0); }
        __builtin_amdgcn_sched_group_barrier(0x8, 8, 0);
        __builtin_amdgcn_sched_barrier(0);
      };
      RAW_BARRIER();
      STAMP(1);
      rd(0, af[0], wf[0]);
#pragma unroll 1
      for (int j = 1; j < 9; j += 2) {
        RAW_BARRIER(); __builtin_amdgcn_sched_barrier(0); rd(j, af[1], wf[1]); mm(af[0], wf[0]); mix();
        RAW_BARRIER(); __builtin_amdgcn_sched_barrier(0); rd(j + 1, af[0], wf[0]); mm(af[1], wf[1]); mix();
      }
      mm(af[0], wf[0]);
      STAMP(2);
      const float qscale = 0.23570226039551584f * 1.4426950408889634f;
      char* sE = sm + 3 * SLOT + cwv * 5120;
#pragma unroll
      for (int i = 0; i < 4; ++i)
#pragma unroll
        for (int h = 0; h < 2; ++h) {
          const f32x4 a = acc[i][h] * qscale;
          *(u32x2*)(sE + (i * 16 + tl) * 80 + h * 32 + g * 8) = u32x2{pack2(a[0], a[1]), pack2(a[2], a[3])};
        }
      const __amdgpu_buffer_rsrc_t qr = __builtin_amdgcn_make_buffer_rsrc((void*)(ws + WS_Q), 0, S_LEN * DM * 2, 0x00020000);
#pragma unroll
      for (int i = 0; i < 4; ++i) {
        const int row = i * 16 + (lane >> 2);
        const u32x4 v = *(const u32x4*)(sE + row * 80 + (lane & 3) * 16);
        __builtin_amdgcn_raw_buffer_store_b128(v, qr, ((t0 + rh * 64 + row) * DM + n0 + nh * 32 + (lane & 3) * 8) * 2, 0, ST_AUX);
      }
    }
  } else if (loc < 22) {
    const int t0 = (grp * 4 + loc - 18) * 64;
    char* sW = sm + 73728;
    const int k = min(tid - 64, 575);
    const u32x4 k0 = *(const u32x4_a4*)(wk + k * DH), k1 = *(const u32x4_a4*)(wk + k * DH + 8);
    const u32x4 v0 = *(const u32x4_a4*)(wv + k * DH), v1 = *(const u32x4_a4*)(wv + k * DH + 8);
    const unsigned k2 = *(const unsigned*)(wk + k * DH + 16), v2 = *(const unsigned*)(wv + k * DH + 16);
    const unsigned gk = gw[k];
    if (wave < 9) {
      const int row = (wave - 1) * 8 + (lane >> 3);
      const char* xs = (const char*)x + (unsigned)(t0 + row) * (DM * 2) + (((lane & 7) ^ ((row >> 1) & 7)) << 4);
#pragma unroll
      for (int ch = 0; ch < 9; ++ch) glds16(xs + ch * 128, sm + ch * 8192 + (wave - 1) * 1024);
    }
    if (blockIdx.x == 144 && wave == 10) *(unsigned*)(ws + WS_CNT + lane * 128) = 0u;
    {
      char* d = sW + k * 128;
      const int sw = wsw(k);
      *(u32x4*)(d + (sw << 5)) = k0; *(u32x4*)(d + (sw << 5) + 16) = k1;
      *(u32x4*)(d + ((1 ^ sw) << 5)) = v0; *(u32x4*)(d + ((1 ^ sw) << 5) + 16) = v1;
      *(u32x4*)(d + ((2 ^ sw) << 5)) = u32x4{k2, gk, v2, 0u}; *(u32x4*)(d + ((2 ^ sw) << 5) + 16) = u32x4{0u, 0u, 0u, 0u};
    }
    wait_vm<0>();
    RAW_BARRIER();
    STAMP(1);
    if (wave < 9) {
      const int rg = (wave - 1) & 3, kh = (wave - 1) >> 2;
      const int tw = t0 + rg * 16;
      f32x4 aK = {0.f, 0.f, 0.f, 0.f}, aV = aK, aX = aK;
      bf16x8 fa[9], fw[9][3];
#pragma unroll
      for (int q = 0; q < 9; ++q) {
        fa[q] = a_frag(sm + q * 8192, lane, rg, kh);
#pragma unroll
        for (int nb = 0; nb < 3; ++nb) fw[q][nb] = w_frag(sW + q * 8192, lane, kh, nb);
      }
      __builtin_amdgcn_sched_barrier(0);
#pragma unroll
      for (int q = 0; q < 9; ++q) {
        aK = MFMA16(fw[q][0], fa[q], aK); aV = MFMA16(fa[q], fw[q][1], aV); aX = MFMA16(fw[q][2], fa[q], aX);
      }
      RAW_BARRIER();
      f32x4* ex = (f32x4*)(sm + rg * 4096) + lane;
      if (kh) { ex[0] = aK; ex[64] = aX; } else { ex[128] = aV; }
      RAW_BARRIER();
      STAMP(2);
      const int t = tw + tl;
      const __amdgpu_buffer_rsrc_t ir = __builtin_amdgcn_make_buffer_rsrc((void*)(ws + WS_IMG), 0, IMG_BYTES, 0x00020000);
      const int img = (tw >> 7) * SG_BYTES, vimg = img + SG_V + ((tw >> 4) & 7) * 640;
      const int tile = (tw >> 5) & 3, r = t & 31;
      if (kh) {
        aV += ex[128];
        __builtin_amdgcn_raw_buffer_store_b64(u32x2{pack2(aV[0], aV[1]), pack2(aV[2], aV[3])}, ir, vimg + (g >> 1) * 320 + (g & 1) * 8 + vpos(tl) * 16, 0, ST_AUX);
        if (lane < 5) {
          const unsigned val = (tl & 1) ? 0x3f803f80u : 0u;
          const u32x4 cv = lane < 4 ? u32x4{val, val, val, val} : u32x4{0x3f80u, 0u, 0x3f80u, 0u};
          __builtin_amdgcn_raw_buffer_store_b128(cv, ir, lane < 4 ? vimg + (tl >> 1) * 320 + ((tl & 1) ? 18 : 15) * 16 : img + SG_K2 + 512, 0, ST_AUX);
        }
      } else {
        aK += ex[0]; aX += ex[64];
        __builtin_amdgcn_raw_buffer_store_b64(u32x2{pack2(aK[0], aK[1]), pack2(aK[2], aK[3])}, ir,
                                              img + SG_K1 + tile * 1024 + (r + 32 * (g >> 1)) * 16 + (g & 1) * 8, 0, ST_AUX);
        const unsigned e01 = pack2(aX[0], aX[1]);
        if (g == 0) {
          __builtin_amdgcn_raw_buffer_store_b32(e01, ir, img + SG_K2 + r * 16 + tile * 4, 0, ST_AUX);
          const float lg = rbf(aX[2]);
          ((float*)(ws + WS_G))[t] = rbf(__builtin_amdgcn_rcpf(1.0f + __expf(-lg)));
        } else if (g == 1) {
          const int d = vimg + (tl >> 3) * 320 + (tl & 7) * 2;
          __builtin_amdgcn_raw_buffer_store_b16((u16)e01, ir, d + 16 * 16, 0, ST_AUX);
          __builtin_amdgcn_raw_buffer_store_b16((u16)(e01 >> 16), ir, d + 17 * 16, 0, ST_AUX);
        }
      }
    }
  } else {
    const int t0 = grp * 256, n0 = (loc - 22) * 64;
    constexpr int UP = 520;
    const __amdgpu_buffer_rsrc_t ur = __builtin_amdgcn_make_buffer_rsrc((void*)(ws + WS_UR), 0, DM * PS_COPY * 2, 0x00020000);
    if (wave < 9) {
      const int kid = tid - 64, cg = kid & 7, st = kid >> 3;
      const int c0 = n0 + cg * 8, ts = t0 + st * 4;
      u32x4 kr[7], vr[4], sk[4];
#pragma unroll
      for (int i = 0; i < 7; ++i) kr[i] = *(const u32x4*)(bk + (size_t)max(ts - 3 + i, 0) * DM + c0);
#pragma unroll
      for (int i = 0; i < 4; ++i) vr[i] = *(const u32x4*)(bv + (size_t)(ts + i) * DM + c0);
#pragma unroll
      for (int i = 0; i < 4; ++i) sk[i] = *(const u32x4*)(shk + c0 * 4 + i * 8);
      if (ts == 0) { kr[0] = u32x4{0u, 0u, 0u, 0u}; kr[1] = kr[0]; kr[2] = kr[0]; }
#pragma unroll
      for (int c = 0; c < 8; ++c) {
        const unsigned w01 = sk[c >> 1][(c & 1) * 2], w23 = sk[c >> 1][(c & 1) * 2 + 1];
        const float w0 = bflo(w01), w1 = bfhi(w01), w2 = bflo(w23), w3 = bfhi(w23);
        float kk[7], o[4];
#pragma unroll
        for (int i = 0; i < 7; ++i) kk[i] = (c & 1) ? bfhi(kr[i][c >> 1]) : bflo(kr[i][c >> 1]);
#pragma unroll
        for (int i = 0; i < 4; ++i) {
          const float a = kk[i] * w3 + kk[i + 1] * w2 + kk[i + 2] * w1 + kk[i + 3] * w0;
          o[i] = a * ((c & 1) ? bfhi(vr[i][c >> 1]) : bflo(vr[i][c >> 1]));
        }
        *(u32x2*)(sm + (cg * 8 + c) * UP + (252 - st * 4) * 2) = u32x2{pack2(o[3], o[2]), pack2(o[1], o[0])};
      }
    } else if (grp == 0 && wave < 11) {
      __builtin_amdgcn_raw_buffer_store_b128(u32x4{0u, 0u, 0u, 0u}, ur, ((n0 + lane) * PS_COPY + 2048 + (wave - 9) * 8) * 2, 0, ST_AUX);
    }
    STAMP(1);
    RAW_BARRIER();
    if (wave < 9) {
      u32x2 lo[4], hi[4];
#pragma unroll
      for (int i = 0; i < 4; ++i) {
        const char* sp = sm + ((wave - 1) * 8 + i * 2 + (lane >> 5)) * UP + (lane & 31) * 16;
        lo[i] = *(const u32x2*)sp; hi[i] = *(const u32x2*)(sp + 8);
      }
#pragma unroll
      for (int i = 0; i < 4; ++i)
        __builtin_amdgcn_raw_buffer_store_b128(u32x4{lo[i][0], lo[i][1], hi[i][0], hi[i][1]}, ur,
            ((n0 + (wave - 1) * 8 + i * 2 + (lane >> 5)) * PS_COPY + (7 - grp) * 256 + (lane & 31) * 8) * 2, 0, ST_AUX);
    }
  }
  STAMP(3);
  STAMP_FLUSH(0);
}

__device__ __forceinline__ void lds_wait(volatile LDS_AS unsigned* p, unsigned need) {
#pragma unroll 1
  for (int spin = 0; spin < (1 << 24); ++spin) {
    if (__builtin_amdgcn_readfirstlane(*p) >= need) break;
    __builtin_amdgcn_s_sleep(2);
  }
  asm volatile("" ::: "memory");
}

__device__ __forceinline__ void cnt_add(char* ws, int byte_off) {
  __hip_atomic_fetch_add((GLB_AS unsigned*)(ws + WS_CNT + byte_off), 1u, __ATOMIC_RELAXED, __HIP_MEMORY_SCOPE_AGENT);
}
template <int SLP>
__device__ __forceinline__ void cnt_wait(char* ws, int byte_off, unsigned need) {
  GLB_AS unsigned* cp = (GLB_AS unsigned*)(ws + WS_CNT + byte_off);
#pragma unroll 1
  for (int spin = 0; spin < (1 << 22); ++spin) {
    const unsigned v = __hip_atomic_load(cp, __ATOMIC_RELAXED, __HIP_MEMORY_SCOPE_AGENT);
    if (__all(v >= need)) break;
    __builtin_amdgcn_s_sleep(SLP);
  }
  asm volatile("" ::: "memory");
}

#define PS_AUX 16
#define SSM_PACE() do { __builtin_amdgcn_sched_barrier(0); asm volatile("s_nop 3"); __builtin_amdgcn_sched_barrier(0); } while (0)
template <int N0>
__device__ __forceinline__ void ssm_unit(int c, const u16* __restrict__ hk, const u16* __restrict__ dk,
                                         char* ws, int lane) {
  constexpr int NM = N0 + 4;
  const int i = lane & 15, g = lane >> 4;
  const int pso = (int)(WS_PS - WS_UR) + (c + (N0 ? 1 : 0)) * (PS_COPY * 2);
  const __amdgpu_buffer_rsrc_t ur = __builtin_amdgcn_make_buffer_rsrc((void*)(ws + WS_UR + (size_t)c * (PS_COPY * 2)), 0, pso + PS_COPY * 2, 0x00020000);
  int ao = ((i & 8) ? 0 : pso) + (2030 - 256 * (NM - 1) - 2 * (i & 7) + 8 * g) * 2;
  const __amdgpu_buffer_rsrc_t hr = __builtin_amdgcn_make_buffer_rsrc((void*)(hk + (size_t)c * S_LEN), 0, S_LEN * 2, 0x00020000);
  int bo = (16 * i - 16 + 8 * g) * 2, bo1 = bo + 512;
  asm volatile("" : "+v"(bo1), "+v"(ao));
  f32x4 acc[4];
#pragma unroll
  for (int n = 0; n < 4; ++n) acc[n] = f32x4{0.f, 0.f, 0.f, 0.f};
  u32x4 A[NM], B[NM];
  auto ldA = [&](int m) { A[m] = __builtin_amdgcn_raw_buffer_load_b128(ur, ao, 512 * (NM - 1 - m), 0); };
  auto ldB = [&](int j) {
    if (j == 0) B[0] = __builtin_amdgcn_raw_buffer_load_b128(hr, bo, 0, 0);
    else B[j] = __builtin_amdgcn_raw_buffer_load_b128(hr, bo1, 512 * (j - 1), 0);
  };
#pragma unroll
  for (int j = 0; j < NM; ++j) ldB(j);
  const float dd = bflo(dk[c]);
  {
    constexpr int NC = NM / 2;
    u32x4 d[NC]; unsigned nx[NC];
#pragma unroll
    for (int q = 0; q < NC; ++q) {
      d[q] = __builtin_amdgcn_raw_buffer_load_b128(ur, (4 - NC + q) * 1024 + lane * 16, 0, 0);
      nx[q] = __builtin_amdgcn_raw_buffer_load_b32(ur, (4 - NC + q) * 1024 + lane * 16 + 16, 0, 0);
    }
    if (lane < 2) __builtin_amdgcn_raw_buffer_store_b128(u32x4{0u, 0u, 0u, 0u}, ur, pso + 4096 + lane * 16, 0, PS_AUX);
#pragma unroll
    for (int q = 0; q < NC; ++q) {
      const u32x4 sh = {__builtin_amdgcn_alignbit(d[q][1], d[q][0], 16), __builtin_amdgcn_alignbit(d[q][2], d[q][1], 16),
                        __builtin_amdgcn_alignbit(d[q][3], d[q][2], 16), __builtin_amdgcn_alignbit(nx[q], d[q][3], 16)};
      __builtin_amdgcn_raw_buffer_store_b128(sh, ur, pso + (4 - NC + q) * 1024 + lane * 16, 0, PS_AUX);
    }
    wait_vm<0>();
  }
#pragma unroll
  for (int m = 0; m < NM; ++m) ldA(m);
  u32x2 u[4];
#pragma unroll
  for (int n = 0; n < 4; ++n) {
    int uo = (2044 - (256 * (N0 + n) + 16 * i + 8 * (g & 1) + 4 * (g >> 1))) * 2;
    asm volatile("" : "+v"(uo));
    u[n] = __builtin_amdgcn_raw_buffer_load_b64(ur, uo, 0, 0);
  }
  if (N0) {
#pragma unroll 1
    for (int rho = 0; rho < 8; ++rho) {
      if (rho < 7) { ao -= 64; bo -= 64; bo1 -= 64; asm volatile("" : "+v"(ao), "+v"(bo), "+v"(bo1)); }
#pragma unroll
      for (int m = 0; m < NM; ++m) {
#pragma unroll
        for (int n = 3; n >= 0; --n)
          if (N0 + n - m >= 0) {
            acc[n] = MFMA16(__builtin_bit_cast(bf16x8, A[m]), __builtin_bit_cast(bf16x8, B[N0 + n - m]), acc[n]);
            SSM_PACE();
          }
        __builtin_amdgcn_sched_barrier(0);
        ldA(m); ldB(NM - 1 - m);
        __builtin_amdgcn_sched_barrier(0);
      }
    }
  } else {
    u32x4 A2[NM], B2[NM];
    auto round = [&](u32x4 (&Ac)[NM], u32x4 (&Bc)[NM], u32x4 (&An)[NM], u32x4 (&Bn)[NM]) {
#pragma unroll
      for (int m = 0; m < NM; ++m) {
        An[m] = __builtin_amdgcn_raw_buffer_load_b128(ur, ao, 512 * (NM - 1 - m), 0);
        Bn[m] = m == 0 ? __builtin_amdgcn_raw_buffer_load_b128(hr, bo, 0, 0) : __builtin_amdgcn_raw_buffer_load_b128(hr, bo1, 512 * (m - 1), 0);
      }
      __builtin_amdgcn_sched_barrier(0);
#pragma unroll
      for (int m = 0; m < NM; ++m)
#pragma unroll
        for (int n = 3; n >= m; --n) {
          acc[n] = MFMA16(__builtin_bit_cast(bf16x8, Ac[m]), __builtin_bit_cast(bf16x8, Bc[n - m]), acc[n]);
          SSM_PACE();
        }
      __builtin_amdgcn_sched_barrier(0);
    };
#pragma unroll 1
    for (int rho = 0; rho < 8; rho += 2) {
      ao -= 64; bo -= 64; bo1 -= 64; asm volatile("" : "+v"(ao), "+v"(bo), "+v"(bo1));
      round(A, B, A2, B2);
      if (rho < 6) { ao -= 64; bo -= 64; bo1 -= 64; asm volatile("" : "+v"(ao), "+v"(bo), "+v"(bo1)); }
      round(A2, B2, A, B);
    }
  }
  const __amdgpu_buffer_rsrc_t zr = __builtin_amdgcn_make_buffer_rsrc((void*)(ws + WS_Z + (size_t)c * (S_LEN * 2)), 0, S_LEN * 2, 0x00020000);
#pragma unroll
  for (int n = 0; n < 4; ++n) {
    const int tq = 256 * (N0 + n) + 16 * i + 8 * (g & 1) + 4 * (g >> 1);
    const auto s02 = __builtin_amdgcn_permlane32_swap(__float_as_uint(acc[n][0]), __float_as_uint(acc[n][2]), false, false);
    const auto s13 = __builtin_amdgcn_permlane32_swap(__float_as_uint(acc[n][1]), __float_as_uint(acc[n][3]), false, false);
    const float z0 = __uint_as_float(s02[0]) + bfhi(u[n][1]) * dd;
    const float z1 = __uint_as_float(s02[1]) + bflo(u[n][1]) * dd;
    const float z2 = __uint_as_float(s13[0]) + bfhi(u[n][0]) * dd;
    const float z3 = __uint_as_float(s13[1]) + bflo(u[n][0]) * dd;
    __builtin_amdgcn_raw_buffer_store_b64(u32x2{pack2(z0, z1), pack2(z2, z3)}, zr, tq * 2, 0, 16);
  }
  wait_vm<0>();
  if (lane == 0) cnt_add(ws, (CNT_Z + (c >> 6) * 2 + (N0 ? 1 : 0)) * 128);
}

__device__ __forceinline__ f32x16 qk(bf16x8 k1, s16x4 k2, bf16x8 q1, s16x4 q2) {
  f32x16 z;
#pragma unroll
  for (int i = 0; i < 16; ++i) z[i] = 0.f;
  f32x16 s = __builtin_amdgcn_mfma_f32_32x32x16_bf16(k1, q1, z, 0, 0, 0);
  return __builtin_amdgcn_mfma_f32_32x32x8bf16_1k(k2, q2, s, 0, 0, 0);
}
__device__ __forceinline__ float max16(f32x16 s) {
  float m = fmaxf(s[0], s[1]);
#pragma unroll
  for (int i = 2; i < 16; ++i) m = fmaxf(m, s[i]);
  return m;
}
__device__ __forceinline__ s16x4 mk2(unsigned a, unsigned b) { return __builtin_bit_cast(s16x4, u32x2{a, b}); }
__device__ __forceinline__ float swap32_max(float m) {
  const auto s = __builtin_amdgcn_permlane32_swap(__float_as_uint(m), __float_as_uint(m), false, false);
  return fmaxf(__uint_as_float(s[0]), __uint_as_float(s[1]));
}

#define ATTN_CLOB "v96","v97","v98","v99","v100","v101","v102","v103","v104","v105","v106","v107","v108","v109","v110","v111", \
  "v112","v113","v114","v115","v116","v117","v118","v119","v120","v121","v122","v123","v124","v125","v126","v127", \
  "v128","v129","v130","v131","v132","v133","v134","v135","v136","v137","v138","v139","v140","v141","v142","v143"
#define EX1(a) "v_exp_f32 v" #a ", v" #a "\n"
#define EX2(a, b) EX1(a) EX1(b)
#define EX3(a, b, c) EX1(a) EX1(b) EX1(c)
#define EX4(a, b, c, d) EX1(a) EX1(b) EX1(c) EX1(d)
#define ATILE(OVHI, OK1N, K2C, QB2, K2N, QA2, OVLON, X3, X4, X6, X7) \
  EX2(96, 97) \
  "v_smfmac_f32_32x32x32_bf16 %[ob], v[140:143], v[120:127], %[idx]\n" \
  "ds_read_b128 v[140:143], %[av] offset:" #OVHI "\n" \
  EX3(98, 99, 100) \
  "v_mfma_f32_32x32x16_bf16 v[112:127], v[128:131], %[qb1], 0\n" \
  "ds_read_b128 v[128:131], %[ak1] offset:" #OK1N "\n" \
  EX4(101, 102, 103, 104) \
  "v_mfma_f32_32x32x8_bf16 v[112:127], " K2C ", %[" QB2 "], v[112:127]\n" \
  X3 \
  EX4(105, 106, 107, 108) \
  "v_smfmac_f32_32x32x32_bf16 %[oa], v[136:139], v[96:103], %[idx]\n" \
  X4 \
  EX3(109, 110, 111) \
  EX2(112, 113) \
  "v_smfmac_f32_32x32x32_bf16 %[oa], v[140:143], v[104:111], %[idx]\n" \
  "s_waitcnt lgkmcnt(0)\n" \
  EX3(114, 115, 116) \
  "v_mfma_f32_32x32x16_bf16 v[96:111], v[128:131], %[qa1], 0\n" \
  X6 \
  EX4(117, 118, 119, 120) \
  "v_mfma_f32_32x32x8_bf16 v[96:111], " K2N ", %[" QA2 "], v[96:111]\n" \
  X7 \
  EX4(121, 122, 123, 124) \
  "v_smfmac_f32_32x32x32_bf16 %[ob], v[136:139], v[112:119], %[idx]\n" \
  "ds_read_b128 v[136:139], %[av] offset:" #OVLON "\n" \
  EX3(125, 126, 127)
#define ITSTAMP
#define ITCLOB
#define WL2 "s_waitcnt lgkmcnt(2)\n"
#define WL1 "s_waitcnt lgkmcnt(1)\n"
#define ATTN_LOOP_ASM \
  "ds_read_b128 v[128:131], %[ak1]\n" \
  "ds_read_b128 v[132:135], %[ak2]\n" \
  "ds_read_b128 v[136:139], %[av]\n" \
  "v_mov_b32 v140, 0\n v_mov_b32 v141, 0\n v_mov_b32 v142, 0\n v_mov_b32 v143, 0\n" \
  "v_mov_b32 v120, 0\n v_mov_b32 v121, 0\n v_mov_b32 v122, 0\n v_mov_b32 v123, 0\n" \
  "v_mov_b32 v124, 0\n v_mov_b32 v125, 0\n v_mov_b32 v126, 0\n v_mov_b32 v127, 0\n" \
  "s_waitcnt lgkmcnt(1)\n" \
  "v_mfma_f32_32x32x16_bf16 v[96:111], v[128:131], %[qa1], 0\n" \
  "v_mfma_f32_32x32x8_bf16 v[96:111], v[132:133], %[qa2e], v[96:111]\n" \
  "s_nop 11\n" \
  "L_attn_loop_%=:\n" ITSTAMP \
  ATILE(640, 1024, "v[132:133]", "qb2e", "v[132:133]", "qa2o", 1280, WL2, WL1, "", "") \
  ATILE(1920, 2048, "v[132:133]", "qb2o", "v[134:135]", "qa2e", 2560, WL2, WL1, "", "") \
  ATILE(3200, 3072, "v[134:135]", "qb2e", "v[134:135]", "qa2o", 3840, WL2, WL1, "s_cmp_eq_u32 %[it], %[chk]\n", \
        "s_cbranch_scc0 L_attn_nobar_%=\n L_attn_spin_%=:\n ds_read_b32 %[tmp], %[syn]\n s_add_i32 %[chk], %[chk], 16\n s_waitcnt lgkmcnt(0)\n" \
        " v_readfirstlane_b32 %[stmp], %[tmp]\n s_cmp_gt_u32 %[chk], 0x10000000\n s_cbranch_scc1 L_attn_nobar_%=\n s_cmp_lt_u32 %[stmp], 4\n" \
        " s_cbranch_scc1 L_attn_spin_%=\n L_attn_nobar_%=:\n") \
  ATILE(4480, 9744, "v[134:135]", "qb2o", "v[132:133]", "qa2e", 9744, \
        "ds_read_b128 v[132:135], %[ak2] offset:9744\n s_waitcnt lgkmcnt(3)\n", WL2, "s_add_i32 %[it], %[it], 1\n", "s_cmp_lt_i32 %[it], %[nit]\n") \
  "v_add_u32 %[ak1], 9744, %[ak1]\n" \
  "v_add_u32 %[ak2], 9744, %[ak2]\n" \
  "v_add_u32 %[av], 9744, %[av]\n" \
  "s_cbranch_scc1 L_attn_loop_%=\n" \
  "v_smfmac_f32_32x32x32_bf16 %[ob], v[140:143], v[120:127], %[idx]\n" \
  "s_waitcnt lgkmcnt(0)\n" \
  "s_nop 15\n"

__device__ __forceinline__ void attn_exact(const char* img, int offK1, int offK2, int offV, int hh, bf16x8 qa1, bf16x8 qb1,
                                           unsigned qa2, unsigned qb2, f32x16& Oa, f32x16& Ob) {
  const unsigned xa = hh ? 0u : qa2, xb = hh ? 0u : qb2;
  float ma = -INFINITY, mb = -INFINITY;
#pragma unroll 1
  for (int tile = 0; tile < 64; ++tile) {
    const char* base = img + (tile >> 2) * SG_BYTES;
    const s16x4 kk = mk2(*(const unsigned*)(base + offK2 + (hh ? 0 : (tile & 3) * 4)), 0u);
    const bf16x8 k1 = *(const bf16x8*)(base + offK1 + (tile & 3) * 1024);
    ma = fmaxf(ma, max16(qk(k1, kk, qa1, mk2(xa, 0u))));
    mb = fmaxf(mb, max16(qk(k1, kk, qb1, mk2(xb, 0u))));
  }
  ma = swap32_max(ma); mb = swap32_max(mb);
  const s16x4 ea = mk2(hh ? pack2(-ma, 0.f) : qa2, 0u), eb = mk2(hh ? pack2(-mb, 0.f) : qb2, 0u);
#pragma unroll
  for (int i = 0; i < 16; ++i) { Oa[i] = 0.f; Ob[i] = 0.f; }
#pragma unroll 1
  for (int tile = 0; tile < 64; ++tile) {
    const char* base = img + (tile >> 2) * SG_BYTES;
    const s16x4 kk = mk2(*(const unsigned*)(base + offK2 + (hh ? 0 : (tile & 3) * 4)), 0u);
    const bf16x8 k1 = *(const bf16x8*)(base + offK1 + (tile & 3) * 1024);
    const bf16x8 vlo = *(const bf16x8*)(base + offV + (tile & 3) * 1280), vhi = *(const bf16x8*)(base + offV + (tile & 3) * 1280 + 640);
    f32x16 S = qk(k1, kk, qa1, ea);
#pragma unroll
    for (int i = 0; i < 16; ++i) S[i] = __builtin_amdgcn_exp2f(S[i]);
    Oa = __builtin_amdgcn_smfmac_f32_32x32x32_bf16(vlo, __builtin_bit_cast(bf16x16, __builtin_shufflevector(S, S, 0, 1, 2, 3, 4, 5, 6, 7)), Oa, 0xDDDDDDDD, 0, 0);
    Oa = __builtin_amdgcn_smfmac_f32_32x32x32_bf16(vhi, __builtin_bit_cast(bf16x16, __builtin_shufflevector(S, S, 8, 9, 10, 11, 12, 13, 14, 15)), Oa, 0xDDDDDDDD, 0, 0);
    S = qk(k1, kk, qb1, eb);
#pragma unroll
    for (int i = 0; i < 16; ++i) S[i] = __builtin_amdgcn_exp2f(S[i]);
    Ob = __builtin_amdgcn_smfmac_f32_32x32x32_bf16(vlo, __builtin_bit_cast(bf16x16, __builtin_shufflevector(S, S, 0, 1, 2, 3, 4, 5, 6, 7)), Ob, 0xDDDDDDDD, 0, 0);
    Ob = __builtin_amdgcn_smfmac_f32_32x32x32_bf16(vhi, __builtin_bit_cast(bf16x16, __builtin_shufflevector(S, S, 8, 9, 10, 11, 12, 13, 14, 15)), Ob, 0xDDDDDDDD, 0, 0);
  }
}

#define EARLY_POST_PRIO 2
#define OUT_AUX 16
#define NLATE 6
constexpr int T_WO = 58624;
constexpr int T_XCH = T_WO + 73728;
constexpr int T_Z = 0;
constexpr int T_PART = 10240;
constexpr int T_EPI = 30720;
constexpr int T_SYNC = 163840 - 64;
static_assert(T_WO >= NLATE * SG_BYTES && T_XCH + 24576 <= T_SYNC && T_EPI + 12800 <= T_WO, "lds");
constexpr int P_EARLY = NLATE * SG_BYTES / 1024;
constexpr int NG_HI = (153 - (P_EARLY + 20) + 3) / 4;
static_assert(P_EARLY + 20 + NG_HI * 4 <= IMG_PIECES && 12 + (32 - NG_HI) * 4 >= P_EARLY && (P_EARLY + 20) * 1024 >= (NLATE + 2) * SG_BYTES, "dma plan");

__device__ __forceinline__ void img_group(const char* src, char* sm, int p) {
  const GLB_AS unsigned* g = (const GLB_AS unsigned*)(src + p * 1024);
  LDS_AS unsigned* l = (LDS_AS unsigned*)(sm + p * 1024);
  __builtin_amdgcn_global_load_lds(g, l, 16, 0, 0);
  __builtin_amdgcn_global_load_lds(g, l, 16, 1024, 0);
  __builtin_amdgcn_global_load_lds(g, l, 16, 2048, 0);
  __builtin_amdgcn_global_load_lds(g, l, 16, 3072, 0);
}

extern "C" __global__ void __launch_bounds__(768) k2_attn(const u16* __restrict__ hk, const u16* __restrict__ dk,
                                               const u16* __restrict__ x, const u16* __restrict__ wo,
                                               char* ws, u16* __restrict__ out) {
  __shared__ __attribute__((aligned(256))) char sm[163840];
  const int tid = threadIdx.x, lane = tid & 63;
  const int wave = __builtin_amdgcn_readfirstlane(tid >> 6);
  const int b = blockIdx.x;
  const int grp = b & 7, loc = b >> 3;
  const bool has_tile = loc < (grp < 2 ? 30 : 29);
  const int qid = has_tile ? grp * 29 + (grp < 2 ? grp : 2) + loc : 0;
  const int mt = qid / 9, nt = qid - mt * 9;
  const int t0 = mt * 80, n0 = nt * 64;
  const __amdgpu_buffer_rsrc_t orsrc = __builtin_amdgcn_make_buffer_rsrc((void*)(ws + WS_O), 0, (int)O_BYTES, 0x00020000);
  STAMP_DECL;
  STAMP(0);

  volatile LDS_AS unsigned* sync = (volatile LDS_AS unsigned*)(sm + T_SYNC);
  if (wave < 4) {
    __builtin_amdgcn_s_setprio(3);
    const int W = b + 256 * wave;
    const char* src = ws + WS_IMG + lane * 16;
    img_group(src, sm, P_EARLY + wave * 4);
    img_group(src, sm, wave == 0 ? P_EARLY + 16 : wave * 4 - 4);
    if (wave == 0 && lane < 8) sync[lane] = 0u;
    wait_vm<0>();
    RAW_BARRIER();
#pragma unroll
    for (int i = 0; i < 8; ++i) {
      int p = P_EARLY + 20 + (wave + 4 * i) * 4;
      p = p > 152 ? p - (P_EARLY + 20 + NG_HI * 4) + 12 : p;
      img_group(src, sm, p);
    }
    wait_vm<0>();
    if (lane == 0) __hip_atomic_fetch_add((LDS_AS unsigned*)sync + 5, 1u, __ATOMIC_RELAXED, __HIP_MEMORY_SCOPE_WORKGROUP);
    STAMP(1);
    if (wave == 3) {
#pragma unroll
      for (int i = 0; i < 9; ++i)
        __builtin_amdgcn_raw_buffer_store_b128(u32x4{O_EMPTY, O_EMPTY, O_EMPTY, O_EMPTY}, orsrc,
                                               ((b >> 1) * 72 + i * 8 + (lane >> 3)) * 256 + (b & 1) * 128 + (lane & 7) * 16, 0, 16);
      wait_vm<0>();
      if (lane == 0) { cnt_add(ws, (CNT_F + b / 10) * 128); sync[6] = 1u; }
    }
    STAMP(9);
    if (W < 576) {
      ssm_unit<4>(W, hk, dk, ws, lane);
    } else {
      for (int c = W - 576; c < 576; c += 448) ssm_unit<0>(c, hk, dk, ws, lane);
    }
    STAMP(2);
  } else {
    const int pair = wave & 3;
    const bool early = wave < 8;
    if (early) __builtin_amdgcn_s_setprio(2); else __builtin_amdgcn_s_setprio(1);
    const int r = lane & 31, hh = lane >> 5;
    const int tq = b * 8 + pair * 2;
    const u16* Q = (const u16*)(ws + WS_Q) + (size_t)tq * DM + r * DH;
    const bf16x8 qa1 = __builtin_bit_cast(bf16x8, (u32x4)*(const u32x4_a4*)(Q + hh * 8));
    const bf16x8 qb1 = __builtin_bit_cast(bf16x8, (u32x4)*(const u32x4_a4*)(Q + DM + hh * 8));
    const unsigned qa2 = *(const unsigned*)(Q + 16), qb2 = *(const unsigned*)(Q + DM + 16);
    const int offK1 = SG_K1 + lane * 16;
    const int offK2 = SG_K2 + (hh ? 512 : r * 16);
    const int offV = SG_V + hh * 320 + vpos(r) * 16;
    const int idx = 0xDDDDDDDD;
    const unsigned ldsBase = (unsigned)(uintptr_t)(LDS_AS char*)sm + (early ? NLATE * SG_BYTES : 0);

    f32x16 Oa, Ob;
    {
      const unsigned xa = hh ? 0u : qa2, xb = hh ? 0u : qb2;
      const bf16x8 k1 = *(const bf16x8*)(ws + WS_IMG + offK1);
      const unsigned k2 = *(const unsigned*)(ws + WS_IMG + offK2);
      const float ma = swap32_max(max16(qk(k1, mk2(k2, 0u), qa1, mk2(xa, 0u))));
      const float mb = swap32_max(max16(qk(k1, mk2(k2, 0u), qb1, mk2(xb, 0u))));
      const unsigned pm = pack2(-ma, -mb), nma = pm & 0xffffu, nmb = pm >> 16;
      s16x4 qa2e = mk2(hh ? nma : qa2, 0u), qa2o = mk2(hh ? nma : 0u, xa);
      s16x4 qb2e = mk2(hh ? nmb : qb2, 0u), qb2o = mk2(hh ? nmb : 0u, xb);
#pragma unroll
      for (int i = 0; i < 16; ++i) { Oa[i] = 0.f; Ob[i] = 0.f; }
      asm volatile("" : "+v"(Oa), "+v"(Ob), "+v"(qa2e), "+v"(qa2o), "+v"(qb2e), "+v"(qb2o));
      STAMP(9);
      RAW_BARRIER();
      STAMP(1); CSTAMP(10);
      unsigned aK1 = ldsBase + offK1, aK2 = ldsBase + offK2, aV = ldsBase + offV;
      int it = 0;
      const int nit = early ? 16 - NLATE : NLATE;
      int chk = 2 - (wave >> 2), stmp;
      unsigned tmp;
      const unsigned syn = (unsigned)(uintptr_t)(sync + 5);
      asm volatile(ATTN_LOOP_ASM
                   : [oa] "+v"(Oa), [ob] "+v"(Ob), [ak1] "+v"(aK1), [ak2] "+v"(aK2), [av] "+v"(aV), [it] "+s"(it),
                     [chk] "+s"(chk), [stmp] "=&s"(stmp), [tmp] "=&v"(tmp)
                   : [qa1] "v"(qa1), [qb1] "v"(qb1), [qa2e] "v"(qa2e), [qa2o] "v"(qa2o), [qb2e] "v"(qb2e), [qb2o] "v"(qb2o),
                     [idx] "v"(idx), [nit] "s"(nit), [syn] "v"(syn)
                   : "memory", "scc", ATTN_CLOB ITCLOB);
    }
    STAMP(2); CSTAMP(11);
    f32x4* xch = (f32x4*)(sm + T_XCH + pair * 6144) + lane;
    if (early) {
      __builtin_amdgcn_s_setprio(EARLY_POST_PRIO);
      if (lane == 0) __hip_atomic_fetch_add((LDS_AS unsigned*)sync, 1u, __ATOMIC_RELAXED, __HIP_MEMORY_SCOPE_WORKGROUP);
      lds_wait(sync, 4u);
      lds_wait(sync + 6, 1u);
#pragma unroll
      for (int i = 0; i < 3; ++i) {
        xch[i * 64] = f32x4{Oa[4 * i], Oa[4 * i + 1], Oa[4 * i + 2], Oa[4 * i + 3]};
        xch[(3 + i) * 64] = f32x4{Ob[4 * i], Ob[4 * i + 1], Ob[4 * i + 2], Ob[4 * i + 3]};
      }
      WAIT_LGKM0();
      if (lane == 0) sync[1 + pair] = 1u;
      const char* wsrc = (const char*)(wo + n0) + (size_t)(pair * 144 + (lane >> 3)) * (DM * 2);
      const int c0 = ((lane & 7) ^ (wsw(lane >> 3) << 1)) << 4;
#pragma unroll
      for (int i = 0; i < 18; ++i) glds16(wsrc + i * 8 * DM * 2 + (c0 ^ ((i & 1) << 6)), sm + T_WO + pair * 18432 + i * 1024);
    } else {
      lds_wait(sync + 1 + pair, 1u);
      float big = 0.f;
#pragma unroll
      for (int i = 0; i < 3; ++i) {
        const f32x4 pa = xch[i * 64], pb = xch[(3 + i) * 64];
#pragma unroll
        for (int e = 0; e < 4; ++e) if (4 * i + e < 11) {
          Oa[4 * i + e] += pa[e]; Ob[4 * i + e] += pb[e];
          big += fabsf(Oa[4 * i + e]) + fabsf(Ob[4 * i + e]);
        }
      }
      if (__any(!(big < 1e30f))) attn_exact(ws + WS_IMG, offK1, offK2, offV, hh, qa1, qb1, qa2, qb2, Oa, Ob);
      STAMP(3);
      const auto la = __builtin_amdgcn_permlane32_swap(__float_as_uint(Oa[10]), __float_as_uint(Oa[10]), false, false);
      const auto lb = __builtin_amdgcn_permlane32_swap(__float_as_uint(Ob[10]), __float_as_uint(Ob[10]), false, false);
      const float ia = __builtin_amdgcn_rcpf(__uint_as_float(la[0])), ib = __builtin_amdgcn_rcpf(__uint_as_float(lb[0]));
      char* ost = sm + T_XCH + pair * 6144;
      char* od = ost + r * 36 + hh * 8;
      *(unsigned*)(od) = pack2(Oa[0] * ia, Oa[1] * ia);        *(unsigned*)(od + 1168) = pack2(Ob[0] * ib, Ob[1] * ib);
      *(unsigned*)(od + 4) = pack2(Oa[2] * ia, Oa[3] * ia);    *(unsigned*)(od + 1172) = pack2(Ob[2] * ib, Ob[3] * ib);
      *(unsigned*)(od + 16) = pack2(Oa[4] * ia, Oa[5] * ia);   *(unsigned*)(od + 1184) = pack2(Ob[4] * ib, Ob[5] * ib);
      *(unsigned*)(od + 20) = pack2(Oa[6] * ia, Oa[7] * ia);   *(unsigned*)(od + 1188) = pack2(Ob[6] * ib, Ob[7] * ib);
      if (!hh) { *(unsigned*)(od + 32) = pack2(Oa[8] * ia, Oa[9] * ia); *(unsigned*)(od + 1200) = pack2(Ob[8] * ib, Ob[9] * ib); }
      u32x4 ov[3];
#pragma unroll
      for (int i = 0; i < 3; ++i) ov[i] = *(const u32x4*)(ost + (lane & 1) * 1168 + (i * 32 + (lane >> 1)) * 16);
#pragma unroll
      for (int i = 0; i < 3; ++i) {
#pragma unroll
        for (int e = 0; e < 4; ++e) ov[i][e] = min(ov[i][e], O_EMPTY - 1u);
        if (i < 2 || lane < 16)
          __builtin_amdgcn_raw_buffer_store_b128(ov[i], orsrc, (((tq >> 4) * 72 + i * 32 + (lane >> 1)) * 16 + (tq & 15) + (lane & 1)) * 16, 0, 16);
      }
    }
  }
  STAMP(4);
  if (!has_tile) { wait_vm<0>(); STAMP_FLUSH(1); return; }

  const int cwi = wave - 2;
  const int kh = cwi >= 5 ? 1 : 0, cw = cwi - kh * 5;
  const int g = lane >> 4, tl = lane & 15;
  u32x2 xv[2]; float gt;
  if (wave >= 2) {
    const int te = min(t0 + cw * 16 + tl, S_LEN - 1);
#pragma unroll
    for (int h = 0; h < 2; ++h) xv[h] = *(const u32x2*)(x + (size_t)te * DM + n0 + kh * 32 + h * 16 + g * 4);
    gt = ((const float*)(ws + WS_G))[te];
  }
  if (wave == 0) {
    const int ci = lane == 0 ? CNT_F + mt : CNT_Z + nt * 2 + ((lane == 1 ? t0 : t0 + 79) < 1024 ? 0 : 1);
    cnt_wait<8>(ws, ci * 128, lane == 0 ? (mt == 25 ? 6u : 10u) : 64u);
  }
  if (wave >= 4 && wave < 8) wait_vm<0>();
  RAW_BARRIER();
  STAMP(5);
  if (wave < 2) {
    const __amdgpu_buffer_rsrc_t zrsrc = __builtin_amdgcn_make_buffer_rsrc((void*)(ws + WS_Z), 0, (int)Z_BYTES, 0x00020000);
    u32x4 zv[5];
#pragma unroll
    for (int i = 0; i < 5; ++i) {
      const int ci = (wave * 5 + i) * 64 + lane, c = ci / 10, k = ci - c * 10;
      zv[i] = __builtin_amdgcn_raw_buffer_load_b128(zrsrc, ((n0 + c) * S_LEN + t0 + k * 8) * 2, 0, 16);
    }
#pragma unroll
    for (int i = 0; i < 5; ++i) *(u32x4*)(sm + T_Z + ((wave * 5 + i) * 64 + lane) * 16) = zv[i];
    WAIT_LGKM0();
  } else {
    float bx[2][4];
    const float omg = 1.0f - gt;
#pragma unroll
    for (int h = 0; h < 2; ++h) {
      bx[h][0] = omg * bflo(xv[h][0]); bx[h][1] = omg * bfhi(xv[h][0]);
      bx[h][2] = omg * bflo(xv[h][1]); bx[h][3] = omg * bfhi(xv[h][1]);
    }
    const int rgp = mt * 5 + cw;
    const int obase = ((rgp * 18 + kh * 9) * 64 + lane) * 16;
    const char* wl = sm + T_WO + kh * 9 * 4096;
    constexpr int NPRE = 5;
    bf16x8 wpre[NPRE][4];
    int so[9];
#pragma unroll
    for (int s = 0; s < 9; ++s) so[s] = s + nt >= 9 ? s + nt - 9 : s + nt;
#pragma unroll
    for (int s = 0; s < NPRE; ++s)
#pragma unroll
      for (int nb = 0; nb < 4; ++nb) wpre[s][nb] = w_frag(wl + so[9 - NPRE + s] * 4096, lane, 0, nb);
    unsigned wq4[4];
#pragma unroll
    for (int nb = 0; nb < 4; ++nb) {
      const int k = g * 8 + (tl >> 2);
      wq4[nb] = (unsigned)(uintptr_t)(LDS_AS char*)sm + T_WO + kh * 9 * 4096 + k * 128 + ((nb ^ wsw(k)) << 5) + (lane & 3) * 8;
    }
#pragma unroll 1
    for (int spin = 0; spin < (1 << 22); ++spin) {
      asm volatile("" ::: "memory");
      const unsigned v = __builtin_amdgcn_raw_buffer_load_b32(orsrc, obase, nt * 1024, 16);
      if (__all(v != O_EMPTY)) break;
      __builtin_amdgcn_s_sleep(1);
    }
    STAMP(6);
    f32x4 acc[4];
#pragma unroll 1
    for (int spin = 0; spin < (1 << 20); ++spin) {
      asm volatile("" : "+v"(wq4[0]), "+v"(wq4[1]), "+v"(wq4[2]), "+v"(wq4[3]) :: "memory");
      u32x4 of[9];
#pragma unroll
      for (int s = 0; s < 9; ++s) of[s] = __builtin_amdgcn_raw_buffer_load_b128(orsrc, obase, so[s] * 1024, 16);
      __builtin_amdgcn_sched_barrier(0);
#pragma unroll
      for (int nb = 0; nb < 4; ++nb) acc[nb] = f32x4{0.f, 0.f, 0.f, 0.f};
      unsigned mx = 0;
#pragma unroll
      for (int s = 0; s < 9; ++s) {
#pragma unroll
        for (int nb = 0; nb < 4; ++nb) {
          bf16x8 wf;
          if (s >= 9 - NPRE) wf = wpre[s - (9 - NPRE)][nb];
          else {
            LDS_AS bf16x4* wp = (LDS_AS bf16x4*)(uintptr_t)(wq4[nb] + so[s] * 4096);
            wf = __builtin_shufflevector(__builtin_amdgcn_ds_read_tr16_b64_v4bf16(wp), __builtin_amdgcn_ds_read_tr16_b64_v4bf16(wp + 64), 0, 1, 2, 3, 4, 5, 6, 7);
          }
          acc[nb] = MFMA16(wf, __builtin_bit_cast(bf16x8, of[s]), acc[nb]);
        }
        mx = max(max(mx, max(of[s][0], of[s][1])), max(of[s][2], of[s][3]));
      }
      if (__all(mx != O_EMPTY)) break;
    }
    f32x4* pd = (f32x4*)(sm + T_PART + cwi * 2048) + lane;
    pd[0] = kh ? acc[0] : acc[2];
    pd[64] = kh ? acc[1] : acc[3];
    STAMP(7);
    RAW_BARRIER();
    const f32x4* ps = (const f32x4*)(sm + T_PART + (kh ? cw : cw + 5) * 2048) + lane;
    char* sE = sm + T_EPI + cwi * 1280;
    f32x4 pa[2]; u32x2 z[2];
#pragma unroll
    for (int h = 0; h < 2; ++h) {
      pa[h] = ps[h * 64];
      z[h] = __builtin_bit_cast(u32x2, tr_read(sm + T_Z + ((kh * 2 + h) * 16 + g * 4 + (tl >> 2)) * 160 + (cw * 16 + (tl & 3) * 4) * 2));
    }
    asm volatile("" ::: "memory");
#pragma unroll
    for (int h = 0; h < 2; ++h) {
      const f32x4 a = (kh ? acc[2 + h] : acc[h]) + pa[h];
      *(u32x2*)(sE + tl * 80 + h * 32 + g * 8) =
          u32x2{pack2(gt * a[0] + bx[h][0] * bflo(z[h][0]), gt * a[1] + bx[h][1] * bfhi(z[h][0])),
                pack2(gt * a[2] + bx[h][2] * bflo(z[h][1]), gt * a[3] + bx[h][3] * bfhi(z[h][1]))};
    }
    const u32x4 o = *(const u32x4*)(sE + (lane >> 2) * 80 + (lane & 3) * 16);
    const __amdgpu_buffer_rsrc_t outr = __builtin_amdgcn_make_buffer_rsrc((void*)out, 0, S_LEN * DM * 2, 0x00020000);
    __builtin_amdgcn_raw_buffer_store_b128(o, outr, ((t0 + cw * 16 + (lane >> 2)) * DM + n0 + kh * 32 + (lane & 3) * 8) * 2, 0, OUT_AUX);
  }
  STAMP(8);
  STAMP_FLUSH(1);
}

extern "C" __global__ void k9_end() {}

extern "C" void kernel_launch(void* const* d_in, const int* in_sizes, int n_in, void* d_out, int out_size,
                              void* d_ws, size_t ws_size, hipStream_t stream) {
  const u16* x = (const u16*)d_in[0];
  const u16* wq = (const u16*)d_in[1];
  const u16* wk = (const u16*)d_in[2];
  const u16* wv = (const u16*)d_in[3];
  const u16* wo = (const u16*)d_in[4];
  const u16* bk = (const u16*)d_in[6];
  const u16* bv = (const u16*)d_in[7];
  const u16* shk = (const u16*)d_in[8];
  const u16* hk = (const u16*)d_in[9];
  const u16* dk = (const u16*)d_in[10];
  const u16* gw = (const u16*)d_in[11];
  char* ws = (char*)d_ws;
  k1_proj<<<248, 704, 0, stream>>>(x, wq, wk, wv, gw, bk, bv, shk, ws);
  k2_attn<<<256, 768, 0, stream>>>(hk, dk, x, wo, ws, (u16*)d_out);
}
```

```cpp
#include <hip/hip_runtime.h>
#include <hip/hip_bf16.h>
#include <stdint.h>

typedef __attribute__((ext_vector_type(4))) __bf16 bf16x4;
typedef __attribute__((ext_vector_type(8))) __bf16 bf16x8;
typedef __attribute__((ext_vector_type(16))) __bf16 bf16x16;
typedef __attribute__((ext_vector_type(4))) short s16x4;
typedef __attribute__((ext_vector_type(4))) float f32x4;
typedef __attribute__((ext_vector_type(16))) float f32x16;
typedef __attribute__((ext_vector_type(2))) unsigned u32x2;
typedef __attribute__((ext_vector_type(4))) unsigned u32x4;
typedef unsigned short u16;
typedef __attribute__((ext_vector_type(4), aligned(4))) unsigned u32x4_a4;

#define LDS_AS __attribute__((address_space(3)))
#define GLB_AS __attribute__((address_space(1)))

constexpr int S_LEN = 2048, DM = 576, DH = 18;
constexpr int PS_COPY = 2112;
constexpr int SG_K1 = 0, SG_K2 = 4096, SG_V = 4624, SG_BYTES = 9744;
constexpr int IMG_PIECES = 156;
constexpr int IMG_BYTES = IMG_PIECES * 1024;

constexpr size_t WS_CNT = 0;
constexpr size_t WS_FLAG = 8192;
constexpr int FL_E = 0, FL_Q = 128, FL_S = FL_Q + 16 * 128, FL_U = FL_S + 8 * 512;
constexpr size_t WS_Q = 16384;
constexpr size_t WS_IMG = WS_Q + (size_t)S_LEN * DM * 2;
constexpr size_t WS_UR = WS_IMG + IMG_BYTES;
constexpr size_t WS_PS = WS_UR + (size_t)DM * S_LEN * 2;
constexpr size_t WS_G = WS_PS + (size_t)DM * 4 * PS_COPY * 2;
constexpr size_t WS_O = WS_G + S_LEN * 4;
constexpr size_t O_BYTES = (size_t)S_LEN * DM * 2;
constexpr size_t WS_Z = WS_O + O_BYTES;
constexpr size_t Z_BYTES = (size_t)S_LEN * DM * 2;
constexpr int CNT_Z = 0, CNT_F = 18;
constexpr unsigned O_EMPTY = 0xffffffffu;

#define STAMP_DECL do {} while (0)
#define STAMP_ARG
#define STAMP_PASS
#define STAMP(i) do {} while (0)
#define CSTAMP(i) do {} while (0)
#define STAMP_FLUSH(kern) do {} while (0)
#define WAIT_LGKM0() do { asm volatile("" ::: "memory"); __builtin_amdgcn_s_waitcnt(0xC07F); asm volatile("" ::: "memory"); } while (0)
#define RAW_BARRIER() do { WAIT_LGKM0(); __builtin_amdgcn_s_barrier(); asm volatile("" ::: "memory"); } while (0)
template <int N> __device__ __forceinline__ void wait_vm() {
  asm volatile("s_waitcnt vmcnt(%0)" :: "i"(N) : "memory");
  if (N == 0) __builtin_amdgcn_s_waitcnt(0x0F70);
}

__device__ __forceinline__ float bflo(unsigned w) { return __uint_as_float(w << 16); }
__device__ __forceinline__ float bfhi(unsigned w) { return __uint_as_float(w & 0xffff0000u); }
__device__ __forceinline__ unsigned pack2(float lo, float hi) {
  typedef __attribute__((ext_vector_type(2))) __bf16 bf16x2;
  bf16x2 v; v[0] = (__bf16)lo; v[1] = (__bf16)hi;
  return __builtin_bit_cast(unsigned, v);
}
__device__ __forceinline__ float rbf(float x) { return (float)(__bf16)x; }
__device__ __forceinline__ void glds16(const void* g, void* lds) {
  __builtin_amdgcn_global_load_lds((const GLB_AS unsigned*)g, (LDS_AS unsigned*)lds, 16, 0, 0);
}
__device__ __forceinline__ bf16x4 tr_read(const char* p) {
  return __builtin_amdgcn_ds_read_tr16_b64_v4bf16((LDS_AS bf16x4*)p);
}
__device__ __forceinline__ int vpos(int d) {
  const int a = d - (d > 11 && d < 16);
  return d > 18 ? 15 : (d == 11 ? 19 : a);
}
#define MFMA16(a, b, c) __builtin_amdgcn_mfma_f32_16x16x32_bf16(a, b, c, 0, 0, 0)

__device__ __forceinline__ int wsw(int k) { return ((k >> 1) & 1) | ((k >> 2) & 2); }
__device__ __forceinline__ bf16x8 a_frag(const char* sA, int lane, int rg, int s) {
  const int row = rg * 16 + (lane & 15);
  return *(const bf16x8*)(sA + row * 128 + (((s * 4 + (lane >> 4)) ^ ((row >> 1) & 7)) << 4));
}
__device__ __forceinline__ bf16x8 w_frag(const char* sW, int lane, int s, int nb) {
  const int k = s * 32 + (lane >> 4) * 8 + ((lane & 15) >> 2);
  const char* b = sW + k * 128 + ((nb ^ wsw(k)) << 5) + (lane & 3) * 8;
  const bf16x4 lo = tr_read(b), hi = tr_read(b + 512);
  return __builtin_shufflevector(lo, hi, 0, 1, 2, 3, 4, 5, 6, 7);
}

template <int NP, int NSLOT, int SLOT, int DEPTH>
__device__ __forceinline__ void ring_loader(const char* const (&base)[NP], const int (&step)[NP], char* const (&dst)[NP], int rot) {
  auto issue = [&](int j) {
    int ch = j + rot; ch = ch >= 9 ? ch - 9 : ch;
#pragma unroll
    for (int i = 0; i < NP; ++i) glds16(base[i] + ch * step[i], dst[i] + ((j + 3) % NSLOT) * SLOT);
  };
#pragma unroll
  for (int j = 0; j < DEPTH; ++j) issue(j);
#define RL_STEP(j) do { wait_vm<NP * ((8 - (j)) < (DEPTH - 1) ? (8 - (j)) : (DEPTH - 1))>(); __builtin_amdgcn_s_barrier(); \
    asm volatile("" ::: "memory"); if ((j) + DEPTH < 9) issue((j) + DEPTH); } while (0)
  RL_STEP(0); RL_STEP(1); RL_STEP(2); RL_STEP(3); RL_STEP(4); RL_STEP(5); RL_STEP(6); RL_STEP(7); RL_STEP(8);
#undef RL_STEP
}

#define K1_DEPTH 3
typedef unsigned long long u64;
__device__ __forceinline__ void flag_set(char* ws, int off, u64 tag) {
  __hip_atomic_store((GLB_AS u64*)(ws + WS_FLAG + off), tag, __ATOMIC_RELAXED, __HIP_MEMORY_SCOPE_AGENT);
}
template <int SLP>
__device__ __forceinline__ void flag_wait(char* ws, int off, u64 tag) {
  GLB_AS u64* fp = (GLB_AS u64*)(ws + WS_FLAG + off);
#pragma unroll 1
  for (int spin = 0; spin < (1 << 22); ++spin) {
    const u64 v = __hip_atomic_load(fp, __ATOMIC_RELAXED, __HIP_MEMORY_SCOPE_AGENT);
    if (__all(v == tag)) break;
    __builtin_amdgcn_s_sleep(SLP);
  }
  asm volatile("" ::: "memory");
}
__device__ __forceinline__ void code_prefetch(bool all, int lane) {
  u64 pc; unsigned d;
  asm volatile("s_getpc_b64 %0\nL_pf_%=:\n s_mov_b32 %1, k9_end-L_pf_%=" : "=s"(pc), "=s"(d));
  unsigned o[4], t[4];
#pragma unroll
  for (int i = 0; i < 4; ++i) o[i] = min((unsigned)(lane * 128 + i * 8192), all ? d - 4u : 12288u);
  asm volatile("global_load_dword %0, %4, %8\n global_load_dword %1, %5, %8\n global_load_dword %2, %6, %8\n"
               " global_load_dword %3, %7, %8\n s_waitcnt vmcnt(0)"
               : "=&v"(t[0]), "=&v"(t[1]), "=&v"(t[2]), "=&v"(t[3])
               : "v"(o[0]), "v"(o[1]), "v"(o[2]), "v"(o[3]), "s"(pc) : "memory");
}
constexpr int T_SYNC = 163840 - 64;
__device__ __forceinline__ u64 lds_tag(char* sm) { return *(volatile LDS_AS u64*)(sm + T_SYNC + 56); }

__device__ __forceinline__ void q_tile(const u16* __restrict__ x, const u16* __restrict__ wq, char* ws, char* sm,
                                       int grp, int loc, int wave, int lane STAMP_ARG) {
  const int g = lane >> 4, tl = lane & 15;
  const int mh = loc >= 9 ? 1 : 0, nt = loc - mh * 9;
  const int t0 = (grp * 2 + mh) * 128, n0 = nt * 64;
  constexpr int SLOT = 24576, NSLOT = 6;
  const __amdgpu_buffer_rsrc_t qr = __builtin_amdgcn_make_buffer_rsrc((void*)(ws + WS_Q), 0, S_LEN * DM * 2, 0x00020000);
  if (wave < 6) {
    int rot = nt + 4 * mh + grp; rot = rot >= 18 ? rot - 18 : (rot >= 9 ? rot - 9 : rot);
    const char* base[4]; int step[4]; char* dst[4];
#pragma unroll
    for (int i = 0; i < 4; ++i) {
      const int pc = wave + 6 * i;
      const bool isW = pc < 8;
      const int row = (isW ? pc : pc - 8) * 8 + (lane >> 3);
      const int sw = isW ? wsw(row) << 1 : (row >> 1) & 7;
      base[i] = (isW ? (const char*)(wq + n0) : (const char*)(x + (size_t)t0 * DM)) + (unsigned)row * (DM * 2) + (((lane & 7) ^ sw) << 4);
      step[i] = isW ? 64 * DM * 2 : 128;
      dst[i] = sm + pc * 1024;
    }
    ring_loader<4, NSLOT, SLOT, K1_DEPTH>(base, step, dst, rot);
  } else if (wave < 8) {
    RAW_BARRIER();
    if (wave == 7) {
#pragma unroll
      for (int i = 0; i < 4; ++i) {
#pragma unroll
        for (int k = 0; k < 4; ++k)
          __builtin_amdgcn_raw_buffer_store_b128(u32x4{O_EMPTY, O_EMPTY, O_EMPTY, O_EMPTY}, qr, ((t0 + (i * 4 + k) * 8 + (lane >> 3)) * DM + n0 + (lane & 7) * 8) * 2, 0, 16);
        RAW_BARRIER();
      }
      RAW_BARRIER(); RAW_BARRIER(); RAW_BARRIER();
      wait_vm<0>();
      if (lane == 0) flag_set(ws, FL_Q + (grp * 2 + mh) * 128 + nt * 8, lds_tag(sm));
      RAW_BARRIER();
    } else {
#pragma unroll
      for (int j = 1; j < 9; ++j) RAW_BARRIER();
    }
  } else {
    const int cwv = wave - 8;
    f32x4 acc[2][4];
#pragma unroll
    for (int i = 0; i < 2; ++i)
#pragma unroll
      for (int nb = 0; nb < 4; ++nb) acc[i][nb] = f32x4{0.f, 0.f, 0.f, 0.f};
    bf16x8 af[2][4], wf[2][8];
    auto rd = [&](int j, bf16x8 (&a)[4], bf16x8 (&w)[8]) {
      const char* sl = sm + (j >= 3 ? j - 3 : j + 3) * SLOT;
#pragma unroll
      for (int s = 0; s < 2; ++s) {
#pragma unroll
        for (int i = 0; i < 2; ++i) a[s * 2 + i] = a_frag(sl + 8192, lane, cwv * 2 + i, s);
#pragma unroll
        for (int nb = 0; nb < 4; ++nb) w[s * 4 + nb] = w_frag(sl, lane, s, nb);
      }
    };
    auto mm = [&](bf16x8 (&a)[4], bf16x8 (&w)[8]) {
#pragma unroll
      for (int s = 0; s < 2; ++s)
#pragma unroll
        for (int nb = 0; nb < 4; ++nb)
#pragma unroll
          for (int i = 0; i < 2; ++i) acc[i][nb] = MFMA16(w[s * 4 + nb], a[s * 2 + i], acc[i][nb]);
    };
    auto mix = [&]() {
#pragma unroll
      for (int i = 0; i < 10; ++i) { __builtin_amdgcn_sched_group_barrier(0x8, 1, 0); __builtin_amdgcn_sched_group_barrier(0x100, 2, 0); }
      __builtin_amdgcn_sched_group_barrier(0x8, 6, 0);
      __builtin_amdgcn_sched_barrier(0);
    };
    RAW_BARRIER();
    STAMP(12);
    rd(0, af[0], wf[0]);
#pragma unroll 1
    for (int j = 1; j < 9; j += 2) {
      RAW_BARRIER(); __builtin_amdgcn_sched_barrier(0); rd(j, af[1], wf[1]); mm(af[0], wf[0]); mix();
      RAW_BARRIER(); __builtin_amdgcn_sched_barrier(0); rd(j + 1, af[0], wf[0]); mm(af[1], wf[1]); mix();
    }
    mm(af[0], wf[0]);
    const float qscale = 0.23570226039551584f * 1.4426950408889634f;
    char* sE = sm + 4 * SLOT + cwv * 4608;
#pragma unroll
    for (int i = 0; i < 2; ++i)
#pragma unroll
      for (int nb = 0; nb < 4; ++nb) {
        const f32x4 a = acc[i][nb] * qscale;
        *(u32x2*)(sE + (i * 16 + tl) * 144 + nb * 32 + g * 8) =
            u32x2{min(pack2(a[0], a[1]), O_EMPTY - 1u), min(pack2(a[2], a[3]), O_EMPTY - 1u)};
      }
#pragma unroll
    for (int i = 0; i < 4; ++i) {
      const int row = i * 8 + (lane >> 3);
      const u32x4 v = *(const u32x4*)(sE + row * 144 + (lane & 7) * 16);
      __builtin_amdgcn_raw_buffer_store_b128(v, qr, ((t0 + cwv * 32 + row) * DM + n0 + (lane & 7) * 8) * 2, 0, 16);
    }
  }
}

struct URegs { u32x4 kr[7], vr[4], sk[4]; };
__device__ __forceinline__ void u_load(URegs& R, const u16* __restrict__ bk, const u16* __restrict__ bv, const u16* __restrict__ shk,
                                       int t0, int n0, int kid) {
  const int c0 = n0 + (kid & 7) * 8, ts = t0 + (kid >> 3) * 4;
#pragma unroll
  for (int i = 0; i < 7; ++i) R.kr[i] = *(const u32x4*)(bk + (size_t)max(ts - 3 + i, 0) * DM + c0);
#pragma unroll
  for (int i = 0; i < 4; ++i) R.vr[i] = *(const u32x4*)(bv + (size_t)(ts + i) * DM + c0);
#pragma unroll
  for (int i = 0; i < 4; ++i) R.sk[i] = *(const u32x4*)(shk + c0 * 4 + i * 8);
}
template <class F>
__device__ __forceinline__ void kvg_tile(const u16* __restrict__ x, const u16* __restrict__ wk, const u16* __restrict__ wv,
                                         const u16* __restrict__ gw, char* ws, char* sm, int si, int tid, int wave, int lane, F&& mid) {
  const int g = lane >> 4, tl = lane & 15;
  const int t0 = si * 32;
  char* sW = sm + 36864;
  if (wave != 6 && wave != 7) {
    const int k = min(wave < 6 ? tid : tid - 128, 575);
    const u32x4 k0 = *(const u32x4_a4*)(wk + k * DH), k1 = *(const u32x4_a4*)(wk + k * DH + 8);
    const u32x4 v0 = *(const u32x4_a4*)(wv + k * DH), v1 = *(const u32x4_a4*)(wv + k * DH + 8);
    const unsigned k2 = *(const unsigned*)(wk + k * DH + 16), v2 = *(const unsigned*)(wv + k * DH + 16);
    const unsigned gk = gw[k];
    if (wave < 4) {
      const int row = wave * 8 + (lane >> 3);
      const char* xs = (const char*)x + (unsigned)(t0 + row) * (DM * 2) + (((lane & 7) ^ ((row >> 1) & 7)) << 4);
#pragma unroll
      for (int ch = 0; ch < 9; ++ch) glds16(xs + ch * 128, sm + ch * 4096 + wave * 1024);
    }
    char* d = sW + k * 128;
    const int sw = wsw(k);
    *(u32x4*)(d + (sw << 5)) = k0; *(u32x4*)(d + (sw << 5) + 16) = k1;
    *(u32x4*)(d + ((1 ^ sw) << 5)) = v0; *(u32x4*)(d + ((1 ^ sw) << 5) + 16) = v1;
    *(u32x4*)(d + ((2 ^ sw) << 5)) = u32x4{k2, gk, v2, 0u}; *(u32x4*)(d + ((2 ^ sw) << 5) + 16) = u32x4{0u, 0u, 0u, 0u};
  } else if (si == 0 && wave == 7) {
    __hip_atomic_store((GLB_AS unsigned*)(ws + WS_CNT + lane * 128), 0u, __ATOMIC_RELAXED, __HIP_MEMORY_SCOPE_AGENT);
  }
  wait_vm<0>();
  RAW_BARRIER();
  mid();
  const int rg = wave & 1, kh = (wave >> 1) & 1, sub = wave >> 2;
  f32x4 acc = {0.f, 0.f, 0.f, 0.f};
  {
    bf16x8 fa[9], fw[9];
#pragma unroll
    for (int q = 0; q < 9; ++q) { fa[q] = a_frag(sm + q * 4096, lane, rg, kh); fw[q] = w_frag(sW + q * 8192, lane, kh, sub); }
    __builtin_amdgcn_sched_barrier(0);
    if (sub == 1) {
#pragma unroll
      for (int q = 0; q < 9; ++q) acc = MFMA16(fa[q], fw[q], acc);
    } else {
#pragma unroll
      for (int q = 0; q < 9; ++q) acc = MFMA16(fw[q], fa[q], acc);
    }
  }
  f32x4* ex = (f32x4*)(sm + 114688 + (sub * 2 + rg) * 1024) + lane;
  if (kh) *ex = acc;
  RAW_BARRIER();
  if (!kh) {
    acc += *ex;
    const int tw = t0 + rg * 16, t = tw + tl;
    const __amdgpu_buffer_rsrc_t ir = __builtin_amdgcn_make_buffer_rsrc((void*)(ws + WS_IMG), 0, IMG_BYTES, 0x00020000);
    const int img = (tw >> 7) * SG_BYTES, vimg = img + SG_V + ((tw >> 4) & 7) * 640;
    const int tile = (tw >> 5) & 3, r = t & 31;
    const unsigned p01 = pack2(acc[0], acc[1]), p23 = pack2(acc[2], acc[3]);
    if (sub == 0) {
      __builtin_amdgcn_raw_buffer_store_b64(u32x2{p01, p23}, ir, img + SG_K1 + tile * 1024 + (r + 32 * (g >> 1)) * 16 + (g & 1) * 8, 0, 16);
      if (lane < 5) {
        const unsigned val = (tl & 1) ? 0x3f803f80u : 0u;
        const u32x4 cv = lane < 4 ? u32x4{val, val, val, val} : u32x4{0x3f80u, 0u, 0x3f80u, 0u};
        __builtin_amdgcn_raw_buffer_store_b128(cv, ir, lane < 4 ? vimg + (tl >> 1) * 320 + ((tl & 1) ? 18 : 15) * 16 : img + SG_K2 + 512, 0, 16);
      }
    } else if (sub == 1) {
      __builtin_amdgcn_raw_buffer_store_b64(u32x2{p01, p23}, ir, vimg + (g >> 1) * 320 + (g & 1) * 8 + vpos(tl) * 16, 0, 16);
    } else if (g == 0) {
      __builtin_amdgcn_raw_buffer_store_b32(p01, ir, img + SG_K2 + r * 16 + tile * 4, 0, 16);
      const float lg = rbf(acc[2]);
      __hip_atomic_store((GLB_AS float*)(ws + WS_G) + t, rbf(__builtin_amdgcn_rcpf(1.0f + __expf(-lg))), __ATOMIC_RELAXED, __HIP_MEMORY_SCOPE_AGENT);
    } else if (g == 1) {
      const int d = vimg + (tl >> 3) * 320 + (tl & 7) * 2;
      __builtin_amdgcn_raw_buffer_store_b16((u16)p01, ir, d + 16 * 16, 0, 16);
      __builtin_amdgcn_raw_buffer_store_b16((u16)(p01 >> 16), ir, d + 17 * 16, 0, 16);
    }
    wait_vm<0>();
  }
  RAW_BARRIER();
  if (tid < 8) flag_set(ws, FL_S + tid * 512 + si * 8, lds_tag(sm));
}

__device__ __forceinline__ void u_finish(URegs& R, bool act, char* ws, char* sm, int rows, int t0, int cgp, int kid, int wave, int lane) {
  constexpr int UP = 520, UB = 122880;
  if (act) {
    const int cg = kid & 7, st = kid >> 3;
    if (t0 + st == 0) { R.kr[0] = u32x4{0u, 0u, 0u, 0u}; R.kr[1] = R.kr[0]; R.kr[2] = R.kr[0]; }
#pragma unroll
    for (int c = 0; c < 8; ++c) {
      const unsigned w01 = R.sk[c >> 1][(c & 1) * 2], w23 = R.sk[c >> 1][(c & 1) * 2 + 1];
      const float w0 = bflo(w01), w1 = bfhi(w01), w2 = bflo(w23), w3 = bfhi(w23);
      float kk[7], o[4];
#pragma unroll
      for (int i = 0; i < 7; ++i) kk[i] = (c & 1) ? bfhi(R.kr[i][c >> 1]) : bflo(R.kr[i][c >> 1]);
#pragma unroll
      for (int i = 0; i < 4; ++i) {
        const float a = kk[i] * w3 + kk[i + 1] * w2 + kk[i + 2] * w1 + kk[i + 3] * w0;
        o[i] = a * ((c & 1) ? bfhi(R.vr[i][c >> 1]) : bflo(R.vr[i][c >> 1]));
      }
      *(u32x2*)(sm + UB + (cg * 8 + c) * UP + (rows - 4 - st * 4) * 2) = u32x2{pack2(o[3], o[2]), pack2(o[1], o[0])};
    }
  }
  RAW_BARRIER();
  if (wave >= 8) {
    const __amdgpu_buffer_rsrc_t ur = __builtin_amdgcn_make_buffer_rsrc((void*)(ws + WS_UR), 0, DM * S_LEN * 2, 0x00020000);
    u32x2 lo[8], hi[8];
#pragma unroll
    for (int i = 0; i < 8; ++i) {
      const char* sp = sm + UB + ((wave - 8) * 16 + i * 2 + (lane >> 5)) * UP + (lane & 31) * 16;
      lo[i] = *(const u32x2*)sp; hi[i] = *(const u32x2*)(sp + 8);
    }
    if ((lane & 31) * 8 < rows) {
#pragma unroll
      for (int i = 0; i < 8; ++i)
        __builtin_amdgcn_raw_buffer_store_b128(u32x4{lo[i][0], lo[i][1], hi[i][0], hi[i][1]}, ur,
            ((cgp * 64 + (wave - 8) * 16 + i * 2 + (lane >> 5)) * S_LEN + (S_LEN - t0 - rows) + (lane & 31) * 8) * 2, 0, 16);
    }
    wait_vm<0>();
    unsigned n = 0;
    if (lane == 0) n = __hip_atomic_fetch_add((LDS_AS unsigned*)(sm + T_SYNC) + 9, 1u, __ATOMIC_RELAXED, __HIP_MEMORY_SCOPE_WORKGROUP);
    if (__builtin_amdgcn_readfirstlane(n) == 3u && lane * 128 < rows) flag_set(ws, FL_U + cgp * 128 + ((t0 >> 7) + lane) * 8, lds_tag(sm));
  }
}

__device__ __forceinline__ void lds_wait(volatile LDS_AS unsigned* p, unsigned need) {
#pragma unroll 1
  for (int spin = 0; spin < (1 << 24); ++spin) {
    if (__builtin_amdgcn_readfirstlane(*p) >= need) break;
    __builtin_amdgcn_s_sleep(2);
  }
  asm volatile("" ::: "memory");
}

__device__ __forceinline__ void cnt_add(char* ws, int byte_off) {
  __hip_atomic_fetch_add((GLB_AS unsigned*)(ws + WS_CNT + byte_off), 1u, __ATOMIC_RELAXED, __HIP_MEMORY_SCOPE_AGENT);
}
template <int SLP>
__device__ __forceinline__ void cnt_wait(char* ws, int byte_off, unsigned need) {
  GLB_AS unsigned* cp = (GLB_AS unsigned*)(ws + WS_CNT + byte_off);
#pragma unroll 1
  for (int spin = 0; spin < (1 << 22); ++spin) {
    const unsigned v = __hip_atomic_load(cp, __ATOMIC_RELAXED, __HIP_MEMORY_SCOPE_AGENT);
    if (__all(v >= need)) break;
    __builtin_amdgcn_s_sleep(SLP);
  }
  asm volatile("" ::: "memory");
}

#define SSM_HI 4
#define SSM_PRIO 1
#define LATE_PRIO 0
#define PS_AUX 16
#define SSM_PACE() do {} while (0)
template <int N0>
__device__ __forceinline__ void ssm_unit(int c, const u16* __restrict__ hk, const u16* __restrict__ dk,
                                         char* ws, int lane) {
  constexpr int NM = N0 + 4;
  const int i = lane & 15, g = lane >> 4;
  const __amdgpu_buffer_rsrc_t ur = __builtin_amdgcn_make_buffer_rsrc((void*)(ws + WS_PS + (size_t)(c * 2 + (N0 ? 1 : 0)) * (PS_COPY * 4)), 0,
                                                                      PS_COPY * 4, 0x00020000);
  int ao = (((i & 8) ? 0 : PS_COPY) + 2030 - 256 * (NM - 1) - 2 * (i & 7) + 8 * g) * 2;
  const __amdgpu_buffer_rsrc_t hr = __builtin_amdgcn_make_buffer_rsrc((void*)(hk + (size_t)c * S_LEN), 0, S_LEN * 2, 0x00020000);
  int bo = (16 * i - 16 + 8 * g) * 2, bo1 = bo + 512;
  asm volatile("" : "+v"(bo1), "+v"(ao));
  f32x4 acc[4];
#pragma unroll
  for (int n = 0; n < 4; ++n) acc[n] = f32x4{0.f, 0.f, 0.f, 0.f};
  u32x4 A[NM], B[NM];
  auto ldA = [&](int m) { A[m] = __builtin_amdgcn_raw_buffer_load_b128(ur, ao, 512 * (NM - 1 - m), 0); };
  auto ldB = [&](int j) {
    if (j == 0) B[0] = __builtin_amdgcn_raw_buffer_load_b128(hr, bo, 0, 0);
    else B[j] = __builtin_amdgcn_raw_buffer_load_b128(hr, bo1, 512 * (j - 1), 0);
  };
#pragma unroll
  for (int j = 0; j < NM; ++j) ldB(j);
  const float dd = bflo(dk[c]);
  wait_vm<0>();
  const unsigned tch = __builtin_amdgcn_raw_buffer_load_b32(ur, (lane >> 5) * (PS_COPY * 2) + (N0 ? (lane & 31) * 128 : 2048 + (lane & 31) * 64), 0, 0);
#pragma unroll
  for (int m = 0; m < NM; ++m) ldA(m);
  u32x2 u[4];
#pragma unroll
  for (int n = 0; n < 4; ++n) {
    int uo = (2044 - (256 * (N0 + n) + 16 * i + 8 * (g & 1) + 4 * (g >> 1))) * 2;
    asm volatile("" : "+v"(uo));
    u[n] = __builtin_amdgcn_raw_buffer_load_b64(ur, uo, 0, 0);
  }
  asm volatile("" :: "v"(tch));
  if (N0) {
#pragma unroll 1
    for (int rho = 0; rho < 8; ++rho) {
      if (rho == SSM_HI) __builtin_amdgcn_s_setprio(SSM_PRIO);
      if (rho < 7) { ao -= 64; bo -= 64; bo1 -= 64; asm volatile("" : "+v"(ao), "+v"(bo), "+v"(bo1)); }
#pragma unroll
      for (int m = 0; m < NM; ++m) {
#pragma unroll
        for (int n = 3; n >= 0; --n)
          if (N0 + n - m >= 0) {
            acc[n] = MFMA16(__builtin_bit_cast(bf16x8, A[m]), __builtin_bit_cast(bf16x8, B[N0 + n - m]), acc[n]);
            SSM_PACE();
          }
        __builtin_amdgcn_sched_barrier(0);
        ldA(m); ldB(NM - 1 - m);
        __builtin_amdgcn_sched_barrier(0);
      }
    }
  } else {
    u32x4 A2[NM], B2[NM];
    auto round = [&](u32x4 (&Ac)[NM], u32x4 (&Bc)[NM], u32x4 (&An)[NM], u32x4 (&Bn)[NM]) {
#pragma unroll
      for (int m = 0; m < NM; ++m) {
        An[m] = __builtin_amdgcn_raw_buffer_load_b128(ur, ao, 512 * (NM - 1 - m), 0);
        Bn[m] = m == 0 ? __builtin_amdgcn_raw_buffer_load_b128(hr, bo, 0, 0) : __builtin_amdgcn_raw_buffer_load_b128(hr, bo1, 512 * (m - 1), 0);
      }
      __builtin_amdgcn_sched_barrier(0);
#pragma unroll
      for (int m = 0; m < NM; ++m)
#pragma unroll
        for (int n = 3; n >= m; --n) {
          acc[n] = MFMA16(__builtin_bit_cast(bf16x8, Ac[m]), __builtin_bit_cast(bf16x8, Bc[n - m]), acc[n]);
          SSM_PACE();
        }
      __builtin_amdgcn_sched_barrier(0);
    };
#pragma unroll 1
    for (int rho = 0; rho < 8; rho += 2) {
      if (rho == SSM_HI) __builtin_amdgcn_s_setprio(SSM_PRIO);
      ao -= 64; bo -= 64; bo1 -= 64; asm volatile("" : "+v"(ao), "+v"(bo), "+v"(bo1));
      round(A, B, A2, B2);
      if (rho < 6) { ao -= 64; bo -= 64; bo1 -= 64; asm volatile("" : "+v"(ao), "+v"(bo), "+v"(bo1)); }
      round(A2, B2, A, B);
    }
  }
  __builtin_amdgcn_s_setprio(3);
  const __amdgpu_buffer_rsrc_t zr = __builtin_amdgcn_make_buffer_rsrc((void*)(ws + WS_Z + (size_t)c * (S_LEN * 2)), 0, S_LEN * 2, 0x00020000);
#pragma unroll
  for (int n = 0; n < 4; ++n) {
    const int tq = 256 * (N0 + n) + 16 * i + 8 * (g & 1) + 4 * (g >> 1);
    const auto s02 = __builtin_amdgcn_permlane32_swap(__float_as_uint(acc[n][0]), __float_as_uint(acc[n][2]), false, false);
    const auto s13 = __builtin_amdgcn_permlane32_swap(__float_as_uint(acc[n][1]), __float_as_uint(acc[n][3]), false, false);
    const float z0 = __uint_as_float(s02[0]) + bfhi(u[n][1]) * dd;
    const float z1 = __uint_as_float(s02[1]) + bflo(u[n][1]) * dd;
    const float z2 = __uint_as_float(s13[0]) + bfhi(u[n][0]) * dd;
    const float z3 = __uint_as_float(s13[1]) + bflo(u[n][0]) * dd;
    __builtin_amdgcn_raw_buffer_store_b64(u32x2{pack2(z0, z1), pack2(z2, z3)}, zr, tq * 2, 0, 16);
  }
  wait_vm<0>();
  if (lane == 0) cnt_add(ws, (CNT_Z + (c >> 6) * 2 + (N0 ? 1 : 0)) * 128);
}

__device__ __forceinline__ f32x16 qk(bf16x8 k1, s16x4 k2, bf16x8 q1, s16x4 q2) {
  f32x16 z;
#pragma unroll
  for (int i = 0; i < 16; ++i) z[i] = 0.f;
  f32x16 s = __builtin_amdgcn_mfma_f32_32x32x16_bf16(k1, q1, z, 0, 0, 0);
  return __builtin_amdgcn_mfma_f32_32x32x8bf16_1k(k2, q2, s, 0, 0, 0);
}
__device__ __forceinline__ float max16(f32x16 s) {
  float m = fmaxf(s[0], s[1]);
#pragma unroll
  for (int i = 2; i < 16; ++i) m = fmaxf(m, s[i]);
  return m;
}
__device__ __forceinline__ s16x4 mk2(unsigned a, unsigned b) { return __builtin_bit_cast(s16x4, u32x2{a, b}); }
__device__ __forceinline__ float swap32_max(float m) {
  const auto s = __builtin_amdgcn_permlane32_swap(__float_as_uint(m), __float_as_uint(m), false, false);
  return fmaxf(__uint_as_float(s[0]), __uint_as_float(s[1]));
}

#define ATTN_CLOB "v96","v97","v98","v99","v100","v101","v102","v103","v104","v105","v106","v107","v108","v109","v110","v111", \
  "v112","v113","v114","v115","v116","v117","v118","v119","v120","v121","v122","v123","v124","v125","v126","v127", \
  "v128","v129","v130","v131","v132","v133","v134","v135","v136","v137","v138","v139","v140","v141","v142","v143"
#define EX1(a) "v_exp_f32 v" #a ", v" #a "\n"
#define EX2(a, b) EX1(a) EX1(b)
#define EX3(a, b, c) EX1(a) EX1(b) EX1(c)
#define EX4(a, b, c, d) EX1(a) EX1(b) EX1(c) EX1(d)
#define ATILE(OVHI, OK1N, K2C, QB2, K2N, QA2, OVLON, X3, X4, X6, X7) \
  EX2(96, 97) \
  "v_smfmac_f32_32x32x32_bf16 %[ob], v[140:143], v[120:127], %[idx]\n" \
  "ds_read_b128 v[140:143], %[av] offset:" OVHI "\n" \
  EX3(98, 99, 100) \
  "v_mfma_f32_32x32x8_bf16 v[112:127], " K2C ", %[" QB2 "], 0\n" \
  "s_waitcnt lgkmcnt(1)\n" \
  EX4(101, 102, 103, 104) \
  "v_mfma_f32_32x32x16_bf16 v[112:127], v[128:131], %[qb1], v[112:127]\n" \
  "ds_read_b128 v[128:131], %[ak1] offset:" OK1N "\n" \
  X3 \
  EX4(105, 106, 107, 108) \
  "v_smfmac_f32_32x32x32_bf16 %[oa], v[136:139], v[96:103], %[idx]\n" \
  X4 \
  EX3(109, 110, 111) \
  EX2(112, 113) \
  "v_smfmac_f32_32x32x32_bf16 %[oa], v[140:143], v[104:111], %[idx]\n" \
  "s_waitcnt lgkmcnt(0)\n" \
  EX3(114, 115, 116) \
  "v_mfma_f32_32x32x8_bf16 v[96:111], " K2N ", %[" QA2 "], 0\n" \
  X6 \
  EX4(117, 118, 119, 120) \
  "v_mfma_f32_32x32x16_bf16 v[96:111], v[128:131], %[qa1], v[96:111]\n" \
  X7 \
  EX4(121, 122, 123, 124) \
  "v_smfmac_f32_32x32x32_bf16 %[ob], v[136:139], v[112:119], %[idx]\n" \
  "ds_read_b128 v[136:139], %[av] offset:" OVLON "\n" \
  EX3(125, 126, 127)
#define ITSTAMP
#define ITCLOB
#define WL2 "s_waitcnt lgkmcnt(2)\n"
#define WL1 "s_waitcnt lgkmcnt(1)\n"
#define SPIN(T) "s_cbranch_scc0 L_attn_go" T "_%=\n L_attn_spin" T "_%=:\n ds_read_b32 %[tmp], %[syn]\n s_add_i32 %[chk], %[chk], 16\n s_waitcnt lgkmcnt(0)\n" \
  " v_readfirstlane_b32 %[stmp], %[tmp]\n s_cmp_gt_u32 %[chk], 0x10000000\n s_cbranch_scc1 L_attn_go" T "_%=\n s_cmp_lt_u32 %[stmp], 4\n" \
  " s_cbranch_scc1 L_attn_spin" T "_%=\n L_attn_go" T "_%=:\n"
#define ASG(O, A6, A7, B6, B7) \
  ATILE(O "+640", O "+1024", "v[132:133]", "qb2e", "v[132:133]", "qa2o", O "+1280", "", WL1, "", "") \
  ATILE(O "+1920", O "+2048", "v[132:133]", "qb2o", "v[134:135]", "qa2e", O "+2560", "", WL1, "", "") \
  ATILE(O "+3200", O "+3072", "v[134:135]", "qb2e", "v[134:135]", "qa2o", O "+3840", "", WL1, A6, A7) \
  ATILE(O "+4480", O "+9744", "v[134:135]", "qb2o", "v[132:133]", "qa2e", O "+9744", \
        "ds_read_b128 v[132:135], %[ak2] offset:" O "+9744\n", WL2, B6, B7)
#define ATTN_LOOP_ASM \
  "ds_read_b128 v[128:131], %[ak1]\n" \
  "ds_read_b128 v[132:135], %[ak2]\n" \
  "ds_read_b128 v[136:139], %[av]\n" \
  "v_mov_b32 v140, 0\n v_mov_b32 v141, 0\n v_mov_b32 v142, 0\n v_mov_b32 v143, 0\n" \
  "v_mov_b32 v120, 0\n v_mov_b32 v121, 0\n v_mov_b32 v122, 0\n v_mov_b32 v123, 0\n" \
  "v_mov_b32 v124, 0\n v_mov_b32 v125, 0\n v_mov_b32 v126, 0\n v_mov_b32 v127, 0\n" \
  "s_waitcnt lgkmcnt(1)\n" \
  "v_mfma_f32_32x32x16_bf16 v[96:111], v[128:131], %[qa1], 0\n" \
  "v_mfma_f32_32x32x8_bf16 v[96:111], v[132:133], %[qa2e], v[96:111]\n" \
  "s_nop 11\n" \
  "L_attn_loop_%=:\n" ITSTAMP \
  ASG("0", "s_cmp_eq_u32 %[it], %[chk]\n", SPIN("a"), "s_add_i32 %[it], %[it], 1\n", "") \
  ASG("9744", "s_cmp_eq_u32 %[it], %[chk]\n", SPIN("b"), "s_add_i32 %[it], %[it], 1\n", "s_cmp_lt_i32 %[it], %[nit]\n") \
  "v_add_u32 %[ak1], 19488, %[ak1]\n" \
  "v_add_u32 %[ak2], 19488, %[ak2]\n" \
  "v_add_u32 %[av], 19488, %[av]\n" \
  "s_cbranch_scc1 L_attn_loop_%=\n" \
  "v_smfmac_f32_32x32x32_bf16 %[ob], v[140:143], v[120:127], %[idx]\n" \
  "s_waitcnt lgkmcnt(0)\n" \
  "s_nop 15\n"

__device__ __forceinline__ f32x16 attn_exact(__amdgpu_buffer_rsrc_t ir, int offK1, int offK2, int offV, int hh, bf16x8 q1, unsigned q2) {
  const s16x4 x2 = mk2(hh ? 0u : q2, 0u);
  float m = -INFINITY;
#pragma unroll 1
  for (int tile = 0; tile < 64; ++tile) {
    const int base = (tile >> 2) * SG_BYTES;
    const s16x4 kk = mk2(__builtin_amdgcn_raw_buffer_load_b32(ir, base + offK2 + (hh ? 0 : (tile & 3) * 4), 0, 16), 0u);
    const bf16x8 k1 = __builtin_bit_cast(bf16x8, __builtin_amdgcn_raw_buffer_load_b128(ir, base + offK1 + (tile & 3) * 1024, 0, 16));
    m = fmaxf(m, max16(qk(k1, kk, q1, x2)));
  }
  m = swap32_max(m);
  f32x16 O;
#pragma unroll
  for (int i = 0; i < 16; ++i) O[i] = 0.f;
#pragma unroll 1
  for (int tile = 0; tile < 64; ++tile) {
    const int base = (tile >> 2) * SG_BYTES;
    const s16x4 kk = mk2(__builtin_amdgcn_raw_buffer_load_b32(ir, base + offK2 + (hh ? 0 : (tile & 3) * 4), 0, 16), 0u);
    const bf16x8 k1 = __builtin_bit_cast(bf16x8, __builtin_amdgcn_raw_buffer_load_b128(ir, base + offK1 + (tile & 3) * 1024, 0, 16));
    const bf16x8 vlo = __builtin_bit_cast(bf16x8, __builtin_amdgcn_raw_buffer_load_b128(ir, base + offV + (tile & 3) * 1280, 0, 16));
    const bf16x8 vhi = __builtin_bit_cast(bf16x8, __builtin_amdgcn_raw_buffer_load_b128(ir, base + offV + (tile & 3) * 1280 + 640, 0, 16));
    f32x16 S = qk(k1, kk, q1, x2);
#pragma unroll
    for (int i = 0; i < 16; ++i) S[i] = __builtin_amdgcn_exp2f(S[i] - m);
    O = __builtin_amdgcn_smfmac_f32_32x32x32_bf16(vlo, __builtin_bit_cast(bf16x16, __builtin_shufflevector(S, S, 0, 1, 2, 3, 4, 5, 6, 7)), O, 0xDDDDDDDD, 0, 0);
    O = __builtin_amdgcn_smfmac_f32_32x32x32_bf16(vhi, __builtin_bit_cast(bf16x16, __builtin_shufflevector(S, S, 8, 9, 10, 11, 12, 13, 14, 15)), O, 0xDDDDDDDD, 0, 0);
  }
  return O;
}

#define EARLY_POST_PRIO 2
#define OUT_AUX 16
#define Q_SLEEP 4
#define KV_DELAY 32
#define NLATE 6
constexpr int T_QX = 16384;
constexpr int T_WO = 58624;
constexpr int T_XCH = T_WO + 73728;
constexpr int T_Z = 0;
constexpr int T_PART = 10240;
constexpr int T_EPI = 30720;
static_assert(NLATE % 2 == 0 && T_WO >= NLATE * SG_BYTES && T_XCH + 24576 <= T_SYNC && T_EPI + 12800 <= T_WO, "lds");
constexpr int P_EARLY = NLATE * SG_BYTES / 1024;
constexpr int NG_HI = (153 - (P_EARLY + 20) + 3) / 4;
static_assert(P_EARLY + 20 + NG_HI * 4 <= IMG_PIECES && 12 + (32 - NG_HI) * 4 >= P_EARLY && (P_EARLY + 20) * 1024 >= (NLATE + 2) * SG_BYTES, "dma plan");
static_assert(NG_HI >= 16 && T_QX >= 12288 && T_QX + 12288 <= P_EARLY * 1024, "q hand-over area");

__device__ __forceinline__ void img_group(const char* src, char* sm, int p) {
  const GLB_AS unsigned* g = (const GLB_AS unsigned*)(src + p * 1024);
  LDS_AS unsigned* l = (LDS_AS unsigned*)(sm + p * 1024);
  __builtin_amdgcn_global_load_lds(g, l, 16, 0, 16);
  __builtin_amdgcn_global_load_lds(g, l, 16, 1024, 16);
  __builtin_amdgcn_global_load_lds(g, l, 16, 2048, 16);
  __builtin_amdgcn_global_load_lds(g, l, 16, 3072, 16);
}

extern "C" __global__ void __launch_bounds__(768) k_main(const u16* __restrict__ x, const u16* __restrict__ wq,
                                               const u16* __restrict__ wk, const u16* __restrict__ wv,
                                               const u16* __restrict__ gw, const u16* __restrict__ bk,
                                               const u16* __restrict__ bv, const u16* __restrict__ shk, char* ws,
                                               const u16* __restrict__ hk, const u16* __restrict__ dk,
                                               const u16* __restrict__ wo, u16* __restrict__ out) {
  __shared__ __attribute__((aligned(256))) char sm[163840];
  const int tid = threadIdx.x, lane = tid & 63;
  const int wave = __builtin_amdgcn_readfirstlane(tid >> 6);
  const int b = blockIdx.x;
  const int grp = b & 7, loc = b >> 3;
  volatile LDS_AS unsigned* sync = (volatile LDS_AS unsigned*)(sm + T_SYNC);
  STAMP_DECL;
  STAMP(0);
  if (wave == 6) code_prefetch(loc >= 24, lane);
  if (wave == 7) {
    const u64 e = __hip_atomic_load((GLB_AS u64*)(ws + WS_FLAG + FL_E), __ATOMIC_RELAXED, __HIP_MEMORY_SCOPE_AGENT) + 1u;
    if (lane < 16) sync[lane] = lane < 14 ? 0u : (lane == 14 ? (unsigned)e : (unsigned)(e >> 32));
  }
  if (loc < 18) {
    q_tile(x, wq, ws, sm, grp, loc, wave, lane STAMP_PASS);
  } else {
    const bool big = loc >= 26, any = big || loc < 24;
    const int rows = big ? 256 : 128, ut0 = grp * 256 + (big ? 0 : (loc & 1) * 128), cgp = big ? loc - 26 : 6 + ((loc - 18) >> 1);
    const int kid = big ? (wave < 6 ? tid : tid - 256) : (wave < 4 ? tid - 128 : tid - 512);
    const bool act = any && (wave >= 10 || (big ? wave < 6 : (wave & 14) == 2));
    URegs R;
    auto ld = [&]() { if (act) u_load(R, bk, bv, shk, ut0, cgp * 64, kid); asm volatile("" ::: "memory"); };
    if (big) { __builtin_amdgcn_s_sleep(KV_DELAY); ld(); }
    else kvg_tile(x, wk, wv, gw, ws, sm, grp * 8 + loc - 18, tid, wave, lane, ld);
    STAMP(12);
    if (any) u_finish(R, act, ws, sm, rows, ut0, cgp, kid, wave, lane);
  }
  STAMP(13);

  const bool has_tile = loc < (grp < 2 ? 30 : 29);
  const int qid = has_tile ? grp * 29 + (grp < 2 ? grp : 2) + loc : 0;
  const int mt = qid / 9, nt = qid - mt * 9;
  const int t0 = mt * 80, n0 = nt * 64;
  const __amdgpu_buffer_rsrc_t orsrc = __builtin_amdgcn_make_buffer_rsrc((void*)(ws + WS_O), 0, (int)O_BYTES, 0x00020000);
  const u64 tag = lds_tag(sm);

  if (wave < 4) {
    __builtin_amdgcn_s_setprio(3);
    const int W = b + 256 * wave;
    if (wave == 0) { flag_wait<4>(ws, FL_S + grp * 512 + lane * 8, tag); if (lane == 0) sync[7] = 1u; }
    else lds_wait(sync + 7, 1u);
    STAMP(14);
    const char* src = ws + WS_IMG + lane * 16;
    img_group(src, sm, P_EARLY + wave * 4);
    img_group(src, sm, wave == 0 ? P_EARLY + 16 : wave * 4 - 4);
    wait_vm<0>();
    RAW_BARRIER();
#pragma unroll
    for (int i = 0; i < 8; ++i) {
      if (i == 4) lds_wait(sync + 10, 4u);
      int p = P_EARLY + 20 + (wave + 4 * i) * 4;
      p = p > 152 ? p - (P_EARLY + 20 + NG_HI * 4) + 12 : p;
      img_group(src, sm, p);
    }
    wait_vm<0>();
    if (lane == 0) __hip_atomic_fetch_add((LDS_AS unsigned*)sync + 5, 1u, __ATOMIC_RELAXED, __HIP_MEMORY_SCOPE_WORKGROUP);
    {
      const int c1 = W < 576 ? W : W - 576, c2 = (W >= 576 && W < 576 + 128) ? W - 128 : c1;
      flag_wait<8>(ws, FL_U + (((lane & 16) ? c2 : c1) >> 6) * 128 + (lane & 15) * 8, tag);
      const __amdgpu_buffer_rsrc_t us = __builtin_amdgcn_make_buffer_rsrc((void*)(ws + WS_UR), 0, DM * S_LEN * 2, 0x00020000);
      const __amdgpu_buffer_rsrc_t up = __builtin_amdgcn_make_buffer_rsrc((void*)(ws + WS_PS), 0, (int)(WS_G - WS_PS), 0x00020000);
      u32x4 ud[4]; unsigned un[4]; int upo[4];
#pragma unroll
      for (int q = 0; q < 4; ++q) {
        const int cc = q < 2 ? c1 : c2, blk = W < 576 ? q : 2 + (q & 1);
        const int uo = cc * (S_LEN * 2) + blk * 1024 + lane * 16;
        upo[q] = (cc * 2 + (W < 576 ? 1 : 0)) * (PS_COPY * 4) + blk * 1024 + lane * 16;
        ud[q] = __builtin_amdgcn_raw_buffer_load_b128(us, uo, 0, 16);
        un[q] = __builtin_amdgcn_raw_buffer_load_b32(us, uo + 16, 0, 16);
        if (blk == 3 && lane == 63) un[q] = 0u;
      }
#pragma unroll
      for (int q = 0; q < 4; ++q) {
        const u32x4 sh = {__builtin_amdgcn_alignbit(ud[q][1], ud[q][0], 16), __builtin_amdgcn_alignbit(ud[q][2], ud[q][1], 16),
                          __builtin_amdgcn_alignbit(ud[q][3], ud[q][2], 16), __builtin_amdgcn_alignbit(un[q], ud[q][3], 16)};
        __builtin_amdgcn_raw_buffer_store_b128(ud[q], up, upo[q], 0, PS_AUX);
        __builtin_amdgcn_raw_buffer_store_b128(sh, up, upo[q] + PS_COPY * 2, 0, PS_AUX);
        if ((q & 1) && lane < 4 && (q == 3 || W >= 576))
          __builtin_amdgcn_raw_buffer_store_b128(u32x4{0u, 0u, 0u, 0u}, up, upo[q] + 1024 + (lane >> 1) * (PS_COPY * 2 - 32), 0, PS_AUX);
      }
    }
    STAMP(1);
    if (wave == 3) {
#pragma unroll
      for (int i = 0; i < 9; ++i)
        __builtin_amdgcn_raw_buffer_store_b128(u32x4{O_EMPTY, O_EMPTY, O_EMPTY, O_EMPTY}, orsrc,
                                               ((b >> 1) * 72 + i * 8 + (lane >> 3)) * 256 + (b & 1) * 128 + (lane & 7) * 16, 0, 16);
      wait_vm<0>();
      if (lane == 0) { cnt_add(ws, (CNT_F + b / 10) * 128); sync[6] = 1u; }
    }
    STAMP(9);
    if (W < 576) {
      ssm_unit<4>(W, hk, dk, ws, lane);
    } else {
      for (int c = W - 576; c < 576; c += 448) ssm_unit<0>(c, hk, dk, ws, lane);
    }
    if (b == 0 && wave == 1) {
      cnt_wait<8>(ws, (CNT_F + min(lane, 25)) * 128, lane >= 25 ? 6u : 10u);
      if (lane == 0) flag_set(ws, FL_E, tag + 1u);
    }
    STAMP(2);
  } else {
    const int pair = wave & 3;
    const bool early = wave < 8;
    __builtin_amdgcn_s_setprio(2);
    const int r = lane & 31, hh = lane >> 5;
    const int tq = b * 8 + pair * 2;
    const int offK1 = SG_K1 + lane * 16;
    const int offK2 = SG_K2 + (hh ? 512 : r * 16);
    const int offV = SG_V + hh * 320 + vpos(r) * 16;
    const int idx = 0xDDDDDDDD;
    const unsigned ldsBase = (unsigned)(uintptr_t)(LDS_AS char*)sm + (early ? NLATE * SG_BYTES : 0);

    const __amdgpu_buffer_rsrc_t irsrc = __builtin_amdgcn_make_buffer_rsrc((void*)(ws + WS_IMG), 0, IMG_BYTES, 0x00020000);
    bf16x8 qa1, qb1; unsigned qa2, qb2;
    f32x16 Oa, Ob;
    {
#pragma unroll
      for (int i = 0; i < 16; ++i) { Oa[i] = 0.f; Ob[i] = 0.f; }
      unsigned aK1 = ldsBase + offK1, aK2 = ldsBase + offK2, aV = ldsBase + offV;
      asm volatile("" : "+v"(Oa), "+v"(Ob), "+v"(aK1), "+v"(aK2), "+v"(aV) :: "memory");
      const __amdgpu_buffer_rsrc_t qr = __builtin_amdgcn_make_buffer_rsrc((void*)(ws + WS_Q + (size_t)tq * (DM * 2)), 0, DM * 4, 0x00020000);
      auto load_q = [&]() {
#pragma unroll 1
        for (int spin = 0; spin < (1 << 22); ++spin) {
          asm volatile("" ::: "memory");
          const u32x4 ta = __builtin_amdgcn_raw_buffer_load_b128(qr, r * 36 + hh * 16, 0, 16);
          const u32x4 tb = __builtin_amdgcn_raw_buffer_load_b128(qr, r * 36 + hh * 16 + DM * 2, 0, 16);
          qa2 = __builtin_amdgcn_raw_buffer_load_b32(qr, r * 36 + 32, 0, 16);
          qb2 = __builtin_amdgcn_raw_buffer_load_b32(qr, r * 36 + 32 + DM * 2, 0, 16);
          qa1 = __builtin_bit_cast(bf16x8, ta); qb1 = __builtin_bit_cast(bf16x8, tb);
          const unsigned mq = max(max(max(max(ta[0], ta[1]), max(ta[2], ta[3])), max(max(tb[0], tb[1]), max(tb[2], tb[3]))), max(qa2, qb2));
          if (__all(mq != O_EMPTY)) break;
          __builtin_amdgcn_s_sleep(Q_SLEEP);
        }
      };
      u32x4* qx = (u32x4*)(sm + T_QX + pair * 3072) + lane;
      unsigned pm;
      if (early) {
        if (wave == 7) { flag_wait<4>(ws, FL_Q + (b >> 4) * 128 + min(lane, 8) * 8, tag); if (lane == 0) sync[8] = 1u; }
        lds_wait(sync + 7, 1u);
        const bf16x8 k1 = __builtin_bit_cast(bf16x8, __builtin_amdgcn_raw_buffer_load_b128(irsrc, offK1, 0, 16));
        const unsigned k2 = __builtin_amdgcn_raw_buffer_load_b32(irsrc, offK2, 0, 16);
        lds_wait(sync + 8, 1u);
        STAMP(14);
        load_q();
        const float ma = swap32_max(max16(qk(k1, mk2(k2, 0u), qa1, mk2(hh ? 0u : qa2, 0u))));
        const float mb = swap32_max(max16(qk(k1, mk2(k2, 0u), qb1, mk2(hh ? 0u : qb2, 0u))));
        pm = pack2(-ma, -mb);
        qx[0] = __builtin_bit_cast(u32x4, qa1); qx[64] = __builtin_bit_cast(u32x4, qb1); qx[128] = u32x4{qa2, qb2, pm, 0u};
        STAMP(9);
        RAW_BARRIER();
      } else {
        RAW_BARRIER();
        qa1 = __builtin_bit_cast(bf16x8, qx[0]); qb1 = __builtin_bit_cast(bf16x8, qx[64]);
        const u32x4 e = qx[128];
        qa2 = e[0]; qb2 = e[1]; pm = e[2];
        WAIT_LGKM0();
        if (lane == 0) __hip_atomic_fetch_add((LDS_AS unsigned*)sync + 10, 1u, __ATOMIC_RELAXED, __HIP_MEMORY_SCOPE_WORKGROUP);
        __builtin_amdgcn_s_setprio(LATE_PRIO);
      }
      STAMP(1); CSTAMP(10);
      const unsigned xa = hh ? 0u : qa2, xb = hh ? 0u : qb2;
      const unsigned nma = pm & 0xffffu, nmb = pm >> 16;
      const s16x4 qa2e = mk2(hh ? nma : qa2, 0u), qa2o = mk2(hh ? nma : 0u, xa);
      const s16x4 qb2e = mk2(hh ? nmb : qb2, 0u), qb2o = mk2(hh ? nmb : 0u, xb);
      int it = 0;
      const int nit = early ? 16 - NLATE : NLATE;
      int chk = 2 - (wave >> 2), stmp;
      unsigned tmp;
      const unsigned syn = (unsigned)(uintptr_t)(sync + 5);
      asm volatile(ATTN_LOOP_ASM
                   : [oa] "+v"(Oa), [ob] "+v"(Ob), [ak1] "+v"(aK1), [ak2] "+v"(aK2), [av] "+v"(aV), [it] "+s"(it),
                     [chk] "+s"(chk), [stmp] "=&s"(stmp), [tmp] "=&v"(tmp)
                   : [qa1] "v"(qa1), [qb1] "v"(qb1), [qa2e] "v"(qa2e), [qa2o] "v"(qa2o), [qb2e] "v"(qb2e), [qb2o] "v"(qb2o),
                     [idx] "v"(idx), [nit] "s"(nit), [syn] "v"(syn)
                   : "memory", "scc", ATTN_CLOB ITCLOB);
    }
    STAMP(2); CSTAMP(11);
    f32x4* xch = (f32x4*)(sm + T_XCH + pair * 6144) + lane;
    if (early) {
      __builtin_amdgcn_s_setprio(EARLY_POST_PRIO);
      if (lane == 0) __hip_atomic_fetch_add((LDS_AS unsigned*)sync, 1u, __ATOMIC_RELAXED, __HIP_MEMORY_SCOPE_WORKGROUP);
      lds_wait(sync, 4u);
      lds_wait(sync + 6, 1u);
#pragma unroll
      for (int i = 0; i < 3; ++i) {
        xch[i * 64] = f32x4{Oa[4 * i], Oa[4 * i + 1], Oa[4 * i + 2], Oa[4 * i + 3]};
        xch[(3 + i) * 64] = f32x4{Ob[4 * i], Ob[4 * i + 1], Ob[4 * i + 2], Ob[4 * i + 3]};
      }
      WAIT_LGKM0();
      if (lane == 0) sync[1 + pair] = 1u;
      const char* wsrc = (const char*)(wo + n0) + (size_t)(pair * 144 + (lane >> 3)) * (DM * 2);
      const int c0 = ((lane & 7) ^ (wsw(lane >> 3) << 1)) << 4;
#pragma unroll
      for (int i = 0; i < 18; ++i) glds16(wsrc + i * 8 * DM * 2 + (c0 ^ ((i & 1) << 6)), sm + T_WO + pair * 18432 + i * 1024);
    } else {
      __builtin_amdgcn_s_setprio(3);
      lds_wait(sync + 1 + pair, 1u);
      float big = 0.f;
#pragma unroll
      for (int i = 0; i < 3; ++i) {
        const f32x4 pa = xch[i * 64], pb = xch[(3 + i) * 64];
#pragma unroll
        for (int e = 0; e < 4; ++e) if (4 * i + e < 11) {
          Oa[4 * i + e] += pa[e]; Ob[4 * i + e] += pb[e];
          big += fabsf(Oa[4 * i + e]) + fabsf(Ob[4 * i + e]);
        }
      }
      if (__any(!(big < 1e30f) || (!hh && !(fminf(Oa[10], Ob[10]) > 1e-12f)))) {
        Oa = attn_exact(irsrc, offK1, offK2, offV, hh, qa1, qa2);
        Ob = attn_exact(irsrc, offK1, offK2, offV, hh, qb1, qb2);
      }
      STAMP(3);
      const auto la = __builtin_amdgcn_permlane32_swap(__float_as_uint(Oa[10]), __float_as_uint(Oa[10]), false, false);
      const auto lb = __builtin_amdgcn_permlane32_swap(__float_as_uint(Ob[10]), __float_as_uint(Ob[10]), false, false);
      const float ia = __builtin_amdgcn_rcpf(__uint_as_float(la[0])), ib = __builtin_amdgcn_rcpf(__uint_as_float(lb[0]));
      char* ost = sm + T_XCH + pair * 6144;
      char* od = ost + r * 36 + hh * 8;
      *(unsigned*)(od) = pack2(Oa[0] * ia, Oa[1] * ia);        *(unsigned*)(od + 1168) = pack2(Ob[0] * ib, Ob[1] * ib);
      *(unsigned*)(od + 4) = pack2(Oa[2] * ia, Oa[3] * ia);    *(unsigned*)(od + 1172) = pack2(Ob[2] * ib, Ob[3] * ib);
      *(unsigned*)(od + 16) = pack2(Oa[4] * ia, Oa[5] * ia);   *(unsigned*)(od + 1184) = pack2(Ob[4] * ib, Ob[5] * ib);
      *(unsigned*)(od + 20) = pack2(Oa[6] * ia, Oa[7] * ia);   *(unsigned*)(od + 1188) = pack2(Ob[6] * ib, Ob[7] * ib);
      if (!hh) { *(unsigned*)(od + 32) = pack2(Oa[8] * ia, Oa[9] * ia); *(unsigned*)(od + 1200) = pack2(Ob[8] * ib, Ob[9] * ib); }
      u32x4 ov[3];
#pragma unroll
      for (int i = 0; i < 3; ++i) ov[i] = *(const u32x4*)(ost + (lane & 1) * 1168 + (i * 32 + (lane >> 1)) * 16);
#pragma unroll
      for (int i = 0; i < 3; ++i) {
#pragma unroll
        for (int e = 0; e < 4; ++e) ov[i][e] = min(ov[i][e], O_EMPTY - 1u);
        if (i < 2 || lane < 16)
          __builtin_amdgcn_raw_buffer_store_b128(ov[i], orsrc, (((tq >> 4) * 72 + i * 32 + (lane >> 1)) * 16 + (tq & 15) + (lane & 1)) * 16, 0, 16);
      }
    }
  }
  STAMP(4);
  if (!has_tile) { wait_vm<0>(); STAMP_FLUSH(1); return; }

  const int cwi = wave - 2;
  const int kh = cwi >= 5 ? 1 : 0, cw = cwi - kh * 5;
  const int g = lane >> 4, tl = lane & 15;
  u32x2 xv[2]; float gt;
  if (wave >= 2) {
    const int te = min(t0 + cw * 16 + tl, S_LEN - 1);
#pragma unroll
    for (int h = 0; h < 2; ++h) xv[h] = *(const u32x2*)(x + (size_t)te * DM + n0 + kh * 32 + h * 16 + g * 4);
    gt = __hip_atomic_load((GLB_AS float*)(ws + WS_G) + te, __ATOMIC_RELAXED, __HIP_MEMORY_SCOPE_AGENT);
  }
  if (wave == 4) cnt_wait<8>(ws, (CNT_F + mt) * 128, mt == 25 ? 6u : 10u);
  if (wave >= 4 && wave < 8) wait_vm<0>();
  RAW_BARRIER();
  STAMP(5);
  if (wave < 2) {
    cnt_wait<2>(ws, (CNT_Z + nt * 2 + (((lane & 1) ? t0 : t0 + 79) < 1024 ? 0 : 1)) * 128, 64u);
    const __amdgpu_buffer_rsrc_t zrsrc = __builtin_amdgcn_make_buffer_rsrc((void*)(ws + WS_Z), 0, (int)Z_BYTES, 0x00020000);
    u32x4 zv[5];
#pragma unroll
    for (int i = 0; i < 5; ++i) {
      const int ci = (wave * 5 + i) * 64 + lane, c = ci / 10, k = ci - c * 10;
      zv[i] = __builtin_amdgcn_raw_buffer_load_b128(zrsrc, ((n0 + c) * S_LEN + t0 + k * 8) * 2, 0, 16);
    }
#pragma unroll
    for (int i = 0; i < 5; ++i) *(u32x4*)(sm + T_Z + ((wave * 5 + i) * 64 + lane) * 16) = zv[i];
    WAIT_LGKM0();
  } else {
    float bx[2][4];
    const float omg = 1.0f - gt;
#pragma unroll
    for (int h = 0; h < 2; ++h) {
      bx[h][0] = omg * bflo(xv[h][0]); bx[h][1] = omg * bfhi(xv[h][0]);
      bx[h][2] = omg * bflo(xv[h][1]); bx[h][3] = omg * bfhi(xv[h][1]);
    }
    const int rgp = mt * 5 + cw;
    const int obase = ((rgp * 18 + kh * 9) * 64 + lane) * 16;
    const char* wl = sm + T_WO + kh * 9 * 4096;
    constexpr int NPRE = 5;
    bf16x8 wpre[NPRE][4];
    int so[9];
#pragma unroll
    for (int s = 0; s < 9; ++s) so[s] = s + nt >= 9 ? s + nt - 9 : s + nt;
#pragma unroll
    for (int s = 0; s < NPRE; ++s)
#pragma unroll
      for (int nb = 0; nb < 4; ++nb) wpre[s][nb] = w_frag(wl + so[9 - NPRE + s] * 4096, lane, 0, nb);
    unsigned wq4[4];
#pragma unroll
    for (int nb = 0; nb < 4; ++nb) {
      const int k = g * 8 + (tl >> 2);
      wq4[nb] = (unsigned)(uintptr_t)(LDS_AS char*)sm + T_WO + kh * 9 * 4096 + k * 128 + ((nb ^ wsw(k)) << 5) + (lane & 3) * 8;
    }
    STAMP(6);
    f32x4 acc[4];
#pragma unroll 1
    for (int spin = 0; spin < (1 << 20); ++spin) {
      asm volatile("" : "+v"(wq4[0]), "+v"(wq4[1]), "+v"(wq4[2]), "+v"(wq4[3]) :: "memory");
      u32x4 of[9];
#pragma unroll
      for (int s = 0; s < 9; ++s) of[s] = __builtin_amdgcn_raw_buffer_load_b128(orsrc, obase, so[s] * 1024, 16);
      __builtin_amdgcn_sched_barrier(0);
#pragma unroll
      for (int nb = 0; nb < 4; ++nb) acc[nb] = f32x4{0.f, 0.f, 0.f, 0.f};
      unsigned mx = 0;
#pragma unroll
      for (int s = 0; s < 9; ++s) {
#pragma unroll
        for (int nb = 0; nb < 4; ++nb) {
          bf16x8 wf;
          if (s >= 9 - NPRE) wf = wpre[s - (9 - NPRE)][nb];
          else {
            LDS_AS bf16x4* wp = (LDS_AS bf16x4*)(uintptr_t)(wq4[nb] + so[s] * 4096);
            wf = __builtin_shufflevector(__builtin_amdgcn_ds_read_tr16_b64_v4bf16(wp), __builtin_amdgcn_ds_read_tr16_b64_v4bf16(wp + 64), 0, 1, 2, 3, 4, 5, 6, 7);
          }
          acc[nb] = MFMA16(wf, __builtin_bit_cast(bf16x8, of[s]), acc[nb]);
        }
        mx = max(max(mx, max(of[s][0], of[s][1])), max(of[s][2], of[s][3]));
      }
      if (__all(mx != O_EMPTY)) break;
    }
    f32x4* pd = (f32x4*)(sm + T_PART + cwi * 2048) + lane;
    pd[0] = kh ? acc[0] : acc[2];
    pd[64] = kh ? acc[1] : acc[3];
    STAMP(7);
    RAW_BARRIER();
    const f32x4* ps = (const f32x4*)(sm + T_PART + (kh ? cw : cw + 5) * 2048) + lane;
    char* sE = sm + T_EPI + cwi * 1280;
    f32x4 pa[2]; u32x2 z[2];
#pragma unroll
    for (int h = 0; h < 2; ++h) {
      pa[h] = ps[h * 64];
      z[h] = __builtin_bit_cast(u32x2, tr_read(sm + T_Z + ((kh * 2 + h) * 16 + g * 4 + (tl >> 2)) * 160 + (cw * 16 + (tl & 3) * 4) * 2));
    }
    asm volatile("" ::: "memory");
#pragma unroll
    for (int h = 0; h < 2; ++h) {
      const f32x4 a = (kh ? acc[2 + h] : acc[h]) + pa[h];
      *(u32x2*)(sE + tl * 80 + h * 32 + g * 8) =
          u32x2{pack2(gt * a[0] + bx[h][0] * bflo(z[h][0]), gt * a[1] + bx[h][1] * bfhi(z[h][0])),
                pack2(gt * a[2] + bx[h][2] * bflo(z[h][1]), gt * a[3] + bx[h][3] * bfhi(z[h][1]))};
    }
    const u32x4 o = *(const u32x4*)(sE + (lane >> 2) * 80 + (lane & 3) * 16);
    const __amdgpu_buffer_rsrc_t outr = __builtin_amdgcn_make_buffer_rsrc((void*)out, 0, S_LEN * DM * 2, 0x00020000);
    __builtin_amdgcn_raw_buffer_store_b128(o, outr, ((t0 + cw * 16 + (lane >> 2)) * DM + n0 + kh * 32 + (lane & 3) * 8) * 2, 0, OUT_AUX);
  }
  STAMP(8);
  STAMP_FLUSH(1);
}

extern "C" __global__ void k9_end() {}

extern "C" void kernel_launch(void* const* d_in, const int* in_sizes, int n_in, void* d_out, int out_size,
                              void* d_ws, size_t ws_size, hipStream_t stream) {
  const u16* x = (const u16*)d_in[0];
  const u16* wq = (const u16*)d_in[1];
  const u16* wk = (const u16*)d_in[2];
  const u16* wv = (const u16*)d_in[3];
  const u16* wo = (const u16*)d_in[4];
  const u16* bk = (const u16*)d_in[6];
  const u16* bv = (const u16*)d_in[7];
  const u16* shk = (const u16*)d_in[8];
  const u16* hk = (const u16*)d_in[9];
  const u16* dk = (const u16*)d_in[10];
  const u16* gw = (const u16*)d_in[11];
  char* ws = (char*)d_ws;
  k_main<<<256, 768, 0, stream>>>(x, wq, wk, wv, gw, bk, bv, shk, ws, hk, dk, wo, (u16*)d_out);
}
```

```cpp
#include <hip/hip_runtime.h>
#include <hip/hip_bf16.h>
#include <stdint.h>

typedef __attribute__((ext_vector_type(4))) __bf16 bf16x4;
typedef __attribute__((ext_vector_type(8))) __bf16 bf16x8;
typedef __attribute__((ext_vector_type(16))) __bf16 bf16x16;
typedef __attribute__((ext_vector_type(4))) short s16x4;
typedef __attribute__((ext_vector_type(4))) float f32x4;
typedef __attribute__((ext_vector_type(16))) float f32x16;
typedef __attribute__((ext_vector_type(2))) unsigned u32x2;
typedef __attribute__((ext_vector_type(4))) unsigned u32x4;
typedef unsigned short u16;
typedef __attribute__((ext_vector_type(4), aligned(4))) unsigned u32x4_a4;

#define LDS_AS __attribute__((address_space(3)))
#define GLB_AS __attribute__((address_space(1)))

constexpr int S_LEN = 2048, DM = 576, DH = 18;
constexpr int PS_COPY = 2112;
constexpr int SG_K1 = 0, SG_K2 = 4096, SG_V = 4624, SG_BYTES = 9744;
constexpr int IMG_PIECES = 156;
constexpr int IMG_BYTES = IMG_PIECES * 1024;

constexpr size_t WS_CNT = 0;
constexpr size_t WS_FLAG = 8192;
constexpr int FL_E = 0, FL_Q = 128, FL_S = FL_Q + 16 * 128, FL_U = FL_S + 8 * 512;
constexpr size_t WS_Q = 16384;
constexpr size_t WS_IMG = WS_Q + (size_t)S_LEN * DM * 2;
constexpr size_t WS_UR = WS_IMG + IMG_BYTES;
constexpr size_t WS_PS = WS_UR + (size_t)DM * S_LEN * 2;
constexpr size_t WS_G = WS_PS + (size_t)DM * 4 * PS_COPY * 2;
constexpr size_t WS_O = WS_G + S_LEN * 4;
constexpr size_t O_BYTES = (size_t)S_LEN * DM * 2;
constexpr size_t WS_Z = WS_O + O_BYTES;
constexpr size_t Z_BYTES = (size_t)S_LEN * DM * 2;
constexpr int CNT_Z = 0, CNT_F = 18;
constexpr unsigned O_EMPTY = 0xffffffffu;

#define STAMP_DECL do {} while (0)
#define STAMP_ARG
#define STAMP_PASS
#define STAMP(i) do {} while (0)
#define CSTAMP(i) do {} while (0)
#define STAMP_FLUSH(kern) do {} while (0)
#define WAIT_LGKM0() do { asm volatile("" ::: "memory"); __builtin_amdgcn_s_waitcnt(0xC07F); asm volatile("" ::: "memory"); } while (0)
#define RAW_BARRIER() do { WAIT_LGKM0(); __builtin_amdgcn_s_barrier(); asm volatile("" ::: "memory"); } while (0)
template <int N> __device__ __forceinline__ void wait_vm() {
  asm volatile("s_waitcnt vmcnt(%0)" :: "i"(N) : "memory");
  if (N == 0) __builtin_amdgcn_s_waitcnt(0x0070);
}

__device__ __forceinline__ float bflo(unsigned w) { return __uint_as_float(w << 16); }
__device__ __forceinline__ float bfhi(unsigned w) { return __uint_as_float(w & 0xffff0000u); }
__device__ __forceinline__ unsigned pack2(float lo, float hi) {
  typedef __attribute__((ext_vector_type(2))) __bf16 bf16x2;
  bf16x2 v; v[0] = (__bf16)lo; v[1] = (__bf16)hi;
  return __builtin_bit_cast(unsigned, v);
}
__device__ __forceinline__ float rbf(float x) { return (float)(__bf16)x; }
__device__ __forceinline__ void glds16(const void* g, void* lds) {
  __builtin_amdgcn_global_load_lds((const GLB_AS unsigned*)g, (LDS_AS unsigned*)lds, 16, 0, 0);
}
__device__ __forceinline__ bf16x4 tr_read(const char* p) {
  return __builtin_amdgcn_ds_read_tr16_b64_v4bf16((LDS_AS bf16x4*)p);
}
__device__ __forceinline__ int vpos(int d) {
  const int a = d - (d > 11 && d < 16);
  return d > 18 ? 15 : (d == 11 ? 19 : a);
}
#define MFMA16(a, b, c) __builtin_amdgcn_mfma_f32_16x16x32_bf16(a, b, c, 0, 0, 0)

__device__ __forceinline__ int wsw(int k) { return ((k >> 1) & 1) | ((k >> 2) & 2); }
__device__ __forceinline__ bf16x8 a_frag(const char* sA, int lane, int rg, int s) {
  const int row = rg * 16 + (lane & 15);
  return *(const bf16x8*)(sA + row * 128 + (((s * 4 + (lane >> 4)) ^ ((row >> 1) & 7)) << 4));
}
__device__ __forceinline__ bf16x8 w_frag(const char* sW, int lane, int s, int nb) {
  const int k = s * 32 + (lane >> 4) * 8 + ((lane & 15) >> 2);
  const char* b = sW + k * 128 + ((nb ^ wsw(k)) << 5) + (lane & 3) * 8;
  const bf16x4 lo = tr_read(b), hi = tr_read(b + 512);
  return __builtin_shufflevector(lo, hi, 0, 1, 2, 3, 4, 5, 6, 7);
}

template <int NP, int NSLOT, int SLOT, int DEPTH>
__device__ __forceinline__ void ring_loader(const char* const (&base)[NP], const int (&step)[NP], char* const (&dst)[NP], int rot) {
  auto issue = [&](int j) {
    int ch = j + rot; ch = ch >= 9 ? ch - 9 : ch;
#pragma unroll
    for (int i = 0; i < NP; ++i) glds16(base[i] + ch * step[i], dst[i] + ((j + 3) % NSLOT) * SLOT);
  };
#pragma unroll
  for (int j = 0; j < DEPTH; ++j) issue(j);
#define RL_STEP(j) do { wait_vm<NP * ((8 - (j)) < (DEPTH - 1) ? (8 - (j)) : (DEPTH - 1))>(); __builtin_amdgcn_s_barrier(); \
    asm volatile("" ::: "memory"); if ((j) + DEPTH < 9) issue((j) + DEPTH); } while (0)
  RL_STEP(0); RL_STEP(1); RL_STEP(2); RL_STEP(3); RL_STEP(4); RL_STEP(5); RL_STEP(6); RL_STEP(7); RL_STEP(8);
#undef RL_STEP
}

#define K1_DEPTH 3
__device__ __forceinline__ void lds_wait(volatile LDS_AS unsigned* p, unsigned need) {
#pragma unroll 1
  for (int spin = 0; spin < (1 << 24); ++spin) {
    if (__builtin_amdgcn_readfirstlane(*p) >= need) break;
    __builtin_amdgcn_s_sleep(2);
  }
  asm volatile("" ::: "memory");
}

typedef unsigned long long u64;
__device__ __forceinline__ void flag_set(char* ws, int off, u64 tag) {
  __hip_atomic_store((GLB_AS u64*)(ws + WS_FLAG + off), tag, __ATOMIC_RELAXED, __HIP_MEMORY_SCOPE_AGENT);
}
template <int SLP>
__device__ __forceinline__ void flag_wait(char* ws, int off, u64 tag) {
  GLB_AS u64* fp = (GLB_AS u64*)(ws + WS_FLAG + off);
#pragma unroll 1
  for (int spin = 0; spin < (1 << 22); ++spin) {
    const u64 v = __hip_atomic_load(fp, __ATOMIC_RELAXED, __HIP_MEMORY_SCOPE_AGENT);
    if (__all(v == tag)) break;
    __builtin_amdgcn_s_sleep(SLP);
  }
  asm volatile("" ::: "memory");
}
__device__ __forceinline__ void code_prefetch(bool all, int lane) {
  u64 pc; unsigned d;
  asm volatile("s_getpc_b64 %0\nL_pf_%=:\n s_mov_b32 %1, k9_end-L_pf_%=" : "=s"(pc), "=s"(d));
  unsigned o[4], t[4];
#pragma unroll
  for (int i = 0; i < 4; ++i) o[i] = min((unsigned)(lane * 128 + i * 8192), all ? d - 4u : 12288u);
  asm volatile("global_load_dword %0, %4, %8\n global_load_dword %1, %5, %8\n global_load_dword %2, %6, %8\n"
               " global_load_dword %3, %7, %8\n s_waitcnt vmcnt(0)"
               : "=&v"(t[0]), "=&v"(t[1]), "=&v"(t[2]), "=&v"(t[3])
               : "v"(o[0]), "v"(o[1]), "v"(o[2]), "v"(o[3]), "s"(pc) : "memory");
}
constexpr int T_SYNC = 163840 - 64;
__device__ __forceinline__ u64 lds_tag(char* sm) { return *(volatile LDS_AS u64*)(sm + T_SYNC + 56); }

__device__ __forceinline__ void q_tile(const u16* __restrict__ x, const u16* __restrict__ wq, char* ws, char* sm,
                                       int grp, int loc, int wave, int lane STAMP_ARG) {
  const int g = lane >> 4, tl = lane & 15;
  const int mh = loc >= 9 ? 1 : 0, nt = loc - mh * 9;
  const int t0 = (grp * 2 + mh) * 128, n0 = nt * 64;
  constexpr int SLOT = 24576, NSLOT = 6;
  const __amdgpu_buffer_rsrc_t qr = __builtin_amdgcn_make_buffer_rsrc((void*)(ws + WS_Q), 0, S_LEN * DM * 2, 0x00020000);
  if (wave < 6) {
    int rot = nt + 4 * mh + grp; rot = rot >= 18 ? rot - 18 : (rot >= 9 ? rot - 9 : rot);
    const char* base[4]; int step[4]; char* dst[4];
#pragma unroll
    for (int i = 0; i < 4; ++i) {
      const int pc = wave + 6 * i;
      const bool isW = pc < 8;
      const int row = (isW ? pc : pc - 8) * 8 + (lane >> 3);
      const int sw = isW ? wsw(row) << 1 : (row >> 1) & 7;
      base[i] = (isW ? (const char*)(wq + n0) : (const char*)(x + (size_t)t0 * DM)) + (unsigned)row * (DM * 2) + (((lane & 7) ^ sw) << 4);
      step[i] = isW ? 64 * DM * 2 : 128;
      dst[i] = sm + pc * 1024;
    }
    ring_loader<4, NSLOT, SLOT, K1_DEPTH>(base, step, dst, rot);
  } else if (wave < 8) {
    RAW_BARRIER();
    if (wave == 7) {
#pragma unroll
      for (int i = 0; i < 4; ++i) {
#pragma unroll
        for (int k = 0; k < 4; ++k)
          __builtin_amdgcn_raw_buffer_store_b128(u32x4{O_EMPTY, O_EMPTY, O_EMPTY, O_EMPTY}, qr, ((t0 + (i * 4 + k) * 8 + (lane >> 3)) * DM + n0 + (lane & 7) * 8) * 2, 0, 16);
        RAW_BARRIER();
      }
      RAW_BARRIER(); RAW_BARRIER(); RAW_BARRIER();
      wait_vm<0>();
      if (lane == 0) flag_set(ws, FL_Q + (grp * 2 + mh) * 128 + nt * 8, lds_tag(sm));
      RAW_BARRIER();
    } else {
#pragma unroll
      for (int j = 1; j < 9; ++j) RAW_BARRIER();
    }
  } else {
    const int cwv = wave - 8;
    f32x4 acc[2][4];
#pragma unroll
    for (int i = 0; i < 2; ++i)
#pragma unroll
      for (int nb = 0; nb < 4; ++nb) acc[i][nb] = f32x4{0.f, 0.f, 0.f, 0.f};
    bf16x8 af[2][4], wf[2][8];
    auto rd = [&](int j, bf16x8 (&a)[4], bf16x8 (&w)[8]) {
      const char* sl = sm + (j >= 3 ? j - 3 : j + 3) * SLOT;
#pragma unroll
      for (int s = 0; s < 2; ++s) {
#pragma unroll
        for (int i = 0; i < 2; ++i) a[s * 2 + i] = a_frag(sl + 8192, lane, cwv * 2 + i, s);
#pragma unroll
        for (int nb = 0; nb < 4; ++nb) w[s * 4 + nb] = w_frag(sl, lane, s, nb);
      }
    };
    auto mm = [&](bf16x8 (&a)[4], bf16x8 (&w)[8]) {
#pragma unroll
      for (int s = 0; s < 2; ++s)
#pragma unroll
        for (int nb = 0; nb < 4; ++nb)
#pragma unroll
          for (int i = 0; i < 2; ++i) acc[i][nb] = MFMA16(w[s * 4 + nb], a[s * 2 + i], acc[i][nb]);
    };
    auto mix = [&]() {
#pragma unroll
      for (int i = 0; i < 10; ++i) { __builtin_amdgcn_sched_group_barrier(0x8, 1, 0); __builtin_amdgcn_sched_group_barrier(0x100, 2, 0); }
      __builtin_amdgcn_sched_group_barrier(0x8, 6, 0);
      __builtin_amdgcn_sched_barrier(0);
    };
    RAW_BARRIER();
    STAMP(12);
    rd(0, af[0], wf[0]);
#pragma unroll 1
    for (int j = 1; j < 9; j += 2) {
      RAW_BARRIER(); __builtin_amdgcn_sched_barrier(0); rd(j, af[1], wf[1]); mm(af[0], wf[0]); mix();
      RAW_BARRIER(); __builtin_amdgcn_sched_barrier(0); rd(j + 1, af[0], wf[0]); mm(af[1], wf[1]); mix();
    }
    mm(af[0], wf[0]);
    const float qscale = 0.23570226039551584f * 1.4426950408889634f;
    char* sE = sm + 4 * SLOT + cwv * 4608;
#pragma unroll
    for (int i = 0; i < 2; ++i)
#pragma unroll
      for (int nb = 0; nb < 4; ++nb) {
        const f32x4 a = acc[i][nb] * qscale;
        *(u32x2*)(sE + (i * 16 + tl) * 144 + nb * 32 + g * 8) =
            u32x2{min(pack2(a[0], a[1]), O_EMPTY - 1u), min(pack2(a[2], a[3]), O_EMPTY - 1u)};
      }
#pragma unroll
    for (int i = 0; i < 4; ++i) {
      const int row = i * 8 + (lane >> 3);
      const u32x4 v = *(const u32x4*)(sE + row * 144 + (lane & 7) * 16);
      __builtin_amdgcn_raw_buffer_store_b128(v, qr, ((t0 + cwv * 32 + row) * DM + n0 + (lane & 7) * 8) * 2, 0, 16);
    }
  }
}

__device__ __forceinline__ void kvg_tile(const u16* __restrict__ x, const u16* __restrict__ wk, const u16* __restrict__ wv,
                                         const u16* __restrict__ gw, char* ws, char* sm, int si, int tid, int wave, int lane) {
  const int g = lane >> 4, tl = lane & 15;
  const int t0 = si * 32;
  char* sW = sm + 36864;
  if (wave != 6 && wave != 7) {
    const int k = min(wave < 6 ? tid : tid - 128, 575);
    const u32x4 k0 = *(const u32x4_a4*)(wk + k * DH), k1 = *(const u32x4_a4*)(wk + k * DH + 8);
    const u32x4 v0 = *(const u32x4_a4*)(wv + k * DH), v1 = *(const u32x4_a4*)(wv + k * DH + 8);
    const unsigned k2 = *(const unsigned*)(wk + k * DH + 16), v2 = *(const unsigned*)(wv + k * DH + 16);
    const unsigned gk = gw[k];
    if (wave < 4) {
      const int row = wave * 8 + (lane >> 3);
      const char* xs = (const char*)x + (unsigned)(t0 + row) * (DM * 2) + (((lane & 7) ^ ((row >> 1) & 7)) << 4);
#pragma unroll
      for (int ch = 0; ch < 9; ++ch) glds16(xs + ch * 128, sm + ch * 4096 + wave * 1024);
    }
    char* d = sW + k * 128;
    const int sw = wsw(k);
    *(u32x4*)(d + (sw << 5)) = k0; *(u32x4*)(d + (sw << 5) + 16) = k1;
    *(u32x4*)(d + ((1 ^ sw) << 5)) = v0; *(u32x4*)(d + ((1 ^ sw) << 5) + 16) = v1;
    *(u32x4*)(d + ((2 ^ sw) << 5)) = u32x4{k2, gk, v2, 0u}; *(u32x4*)(d + ((2 ^ sw) << 5) + 16) = u32x4{0u, 0u, 0u, 0u};
  } else if (si == 0 && wave == 7) {
    __hip_atomic_store((GLB_AS unsigned*)(ws + WS_CNT + lane * 128), 0u, __ATOMIC_RELAXED, __HIP_MEMORY_SCOPE_AGENT);
  }
  wait_vm<0>();
  RAW_BARRIER();
  const int rg = wave & 1, kh = (wave >> 1) & 1, sub = wave >> 2;
  f32x4 acc = {0.f, 0.f, 0.f, 0.f};
  {
    bf16x8 fa[9], fw[9];
#pragma unroll
    for (int q = 0; q < 9; ++q) { fa[q] = a_frag(sm + q * 4096, lane, rg, kh); fw[q] = w_frag(sW + q * 8192, lane, kh, sub); }
    __builtin_amdgcn_sched_barrier(0);
    if (sub == 1) {
#pragma unroll
      for (int q = 0; q < 9; ++q) acc = MFMA16(fa[q], fw[q], acc);
    } else {
#pragma unroll
      for (int q = 0; q < 9; ++q) acc = MFMA16(fw[q], fa[q], acc);
    }
  }
  f32x4* ex = (f32x4*)(sm + 114688 + (sub * 2 + rg) * 1024) + lane;
  if (kh) *ex = acc;
  RAW_BARRIER();
  if (!kh) {
    acc += *ex;
    const int tw = t0 + rg * 16, t = tw + tl;
    const __amdgpu_buffer_rsrc_t ir = __builtin_amdgcn_make_buffer_rsrc((void*)(ws + WS_IMG), 0, IMG_BYTES, 0x00020000);
    const int img = (tw >> 7) * SG_BYTES, vimg = img + SG_V + ((tw >> 4) & 7) * 640;
    const int tile = (tw >> 5) & 3, r = t & 31;
    const unsigned p01 = pack2(acc[0], acc[1]), p23 = pack2(acc[2], acc[3]);
    if (sub == 0) {
      __builtin_amdgcn_raw_buffer_store_b64(u32x2{p01, p23}, ir, img + SG_K1 + tile * 1024 + (r + 32 * (g >> 1)) * 16 + (g & 1) * 8, 0, 16);
      if (lane < 5) {
        const unsigned val = (tl & 1) ? 0x3f803f80u : 0u;
        const u32x4 cv = lane < 4 ? u32x4{val, val, val, val} : u32x4{0x3f80u, 0u, 0x3f80u, 0u};
        __builtin_amdgcn_raw_buffer_store_b128(cv, ir, lane < 4 ? vimg + (tl >> 1) * 320 + ((tl & 1) ? 18 : 15) * 16 : img + SG_K2 + 512, 0, 16);
      }
    } else if (sub == 1) {
      __builtin_amdgcn_raw_buffer_store_b64(u32x2{p01, p23}, ir, vimg + (g >> 1) * 320 + (g & 1) * 8 + vpos(tl) * 16, 0, 16);
    } else if (g == 0) {
      __builtin_amdgcn_raw_buffer_store_b32(p01, ir, img + SG_K2 + r * 16 + tile * 4, 0, 16);
      const float lg = rbf(acc[2]);
      __hip_atomic_store((GLB_AS float*)(ws + WS_G) + t, rbf(__builtin_amdgcn_rcpf(1.0f + __expf(-lg))), __ATOMIC_RELAXED, __HIP_MEMORY_SCOPE_AGENT);
    } else if (g == 1) {
      const int d = vimg + (tl >> 3) * 320 + (tl & 7) * 2;
      __builtin_amdgcn_raw_buffer_store_b16((u16)p01, ir, d + 16 * 16, 0, 16);
      __builtin_amdgcn_raw_buffer_store_b16((u16)(p01 >> 16), ir, d + 17 * 16, 0, 16);
    }
    wait_vm<0>();
  }
  RAW_BARRIER();
  if (tid < 8) flag_set(ws, FL_S + tid * 512 + si * 8, lds_tag(sm));
}

__device__ __forceinline__ void u_tile(const u16* __restrict__ bk, const u16* __restrict__ bv, const u16* __restrict__ shk,
                                       char* ws, char* sm, int grp, int j, int tid, int wave, int lane) {
  constexpr int UP = 776, UB = 98304;
  const int tA = grp * 256, tB = tA + (j & 1) * 128, cB = 6 + (j >> 1);
  {
    const bool sec = wave >= 8;
    const int cg = tid & 7, st = (tid >> 3) & 63;
    const int c0 = (sec ? cB : j) * 64 + cg * 8, ts = (sec ? tB : tA) + st * 4;
    u32x4 kr[7], vr[4], sk[4];
#pragma unroll
    for (int i = 0; i < 7; ++i) kr[i] = *(const u32x4*)(bk + (size_t)max(ts - 3 + i, 0) * DM + c0);
#pragma unroll
    for (int i = 0; i < 4; ++i) vr[i] = *(const u32x4*)(bv + (size_t)(ts + i) * DM + c0);
#pragma unroll
    for (int i = 0; i < 4; ++i) sk[i] = *(const u32x4*)(shk + c0 * 4 + i * 8);
    if (ts == 0) { kr[0] = u32x4{0u, 0u, 0u, 0u}; kr[1] = kr[0]; kr[2] = kr[0]; }
#pragma unroll
    for (int c = 0; c < 8; ++c) {
      const unsigned w01 = sk[c >> 1][(c & 1) * 2], w23 = sk[c >> 1][(c & 1) * 2 + 1];
      const float w0 = bflo(w01), w1 = bfhi(w01), w2 = bflo(w23), w3 = bfhi(w23);
      float kk[7], o[4];
#pragma unroll
      for (int i = 0; i < 7; ++i) kk[i] = (c & 1) ? bfhi(kr[i][c >> 1]) : bflo(kr[i][c >> 1]);
#pragma unroll
      for (int i = 0; i < 4; ++i) {
        const float a = kk[i] * w3 + kk[i + 1] * w2 + kk[i + 2] * w1 + kk[i + 3] * w0;
        o[i] = a * ((c & 1) ? bfhi(vr[i][c >> 1]) : bflo(vr[i][c >> 1]));
      }
      *(u32x2*)(sm + UB + (cg * 8 + c) * UP + ((sec ? 380 : 252) - st * 4) * 2) = u32x2{pack2(o[3], o[2]), pack2(o[1], o[0])};
    }
  }
  RAW_BARRIER();
}
__device__ __forceinline__ void u_store(char* ws, char* sm, int grp, int j, int wave, int lane) {
  constexpr int UP = 776, UB = 98304;
  const int tA = grp * 256, tB = tA + (j & 1) * 128, cB = 6 + (j >> 1);
  const __amdgpu_buffer_rsrc_t ur = __builtin_amdgcn_make_buffer_rsrc((void*)(ws + WS_UR), 0, DM * S_LEN * 2, 0x00020000);
  u32x4 v[12];
#pragma unroll
  for (int i = 0; i < 12; ++i) {
    const int row = (wave - 8) * 16 + (i < 8 ? i * 2 + (lane >> 5) : (i - 8) * 4 + (lane >> 4));
    const char* sp = sm + UB + row * UP + (i < 8 ? (lane & 31) * 16 : 512 + (lane & 15) * 16);
    const u32x2 lo = *(const u32x2*)sp, hi = *(const u32x2*)(sp + 8);
    v[i] = u32x4{lo[0], lo[1], hi[0], hi[1]};
  }
#pragma unroll
  for (int i = 0; i < 12; ++i) {
    const int row = (wave - 8) * 16 + (i < 8 ? i * 2 + (lane >> 5) : (i - 8) * 4 + (lane >> 4));
    const int off = i < 8 ? (j * 64 + row) * S_LEN + (S_LEN - 256 - tA) + (lane & 31) * 8 : (cB * 64 + row) * S_LEN + (S_LEN - 128 - tB) + (lane & 15) * 8;
    __builtin_amdgcn_raw_buffer_store_b128(v[i], ur, off * 2, 0, 16);
  }
}
__device__ __forceinline__ void u_publish(char* ws, char* sm, int grp, int j, int lane) {
  wait_vm<0>();
  unsigned n = 0;
  if (lane == 0) n = __hip_atomic_fetch_add((LDS_AS unsigned*)(sm + T_SYNC) + 9, 1u, __ATOMIC_RELAXED, __HIP_MEMORY_SCOPE_WORKGROUP);
  if (__builtin_amdgcn_readfirstlane(n) == 3u && lane < 3)
    flag_set(ws, FL_U + (lane < 2 ? j * 128 + (grp * 2 + lane) * 8 : (6 + (j >> 1)) * 128 + (grp * 2 + (j & 1)) * 8), lds_tag(sm));
}

__device__ __forceinline__ void cnt_add(char* ws, int byte_off) {
  __hip_atomic_fetch_add((GLB_AS unsigned*)(ws + WS_CNT + byte_off), 1u, __ATOMIC_RELAXED, __HIP_MEMORY_SCOPE_AGENT);
}
template <int SLP>
__device__ __forceinline__ void cnt_wait(char* ws, int byte_off, unsigned need) {
  GLB_AS unsigned* cp = (GLB_AS unsigned*)(ws + WS_CNT + byte_off);
#pragma unroll 1
  for (int spin = 0; spin < (1 << 22); ++spin) {
    const unsigned v = __hip_atomic_load(cp, __ATOMIC_RELAXED, __HIP_MEMORY_SCOPE_AGENT);
    if (__all(v >= need)) break;
    __builtin_amdgcn_s_sleep(SLP);
  }
  asm volatile("" ::: "memory");
}

#define SSM_HI 4
#define SSM_PRIO 1
#define LATE_PRIO 0
#define PS_AUX 16
#define SSM_PACE() do {} while (0)
template <int N0>
__device__ __forceinline__ void ssm_unit(int c, const u16* __restrict__ hk, const u16* __restrict__ dk,
                                         char* ws, int lane) {
  constexpr int NM = N0 + 4;
  const int i = lane & 15, g = lane >> 4;
  const __amdgpu_buffer_rsrc_t ur = __builtin_amdgcn_make_buffer_rsrc((void*)(ws + WS_PS + (size_t)(c * 2 + (N0 ? 1 : 0)) * (PS_COPY * 4)), 0,
                                                                      PS_COPY * 4, 0x00020000);
  int ao = (((i & 8) ? 0 : PS_COPY) + 2030 - 256 * (NM - 1) - 2 * (i & 7) + 8 * g) * 2;
  const __amdgpu_buffer_rsrc_t hr = __builtin_amdgcn_make_buffer_rsrc((void*)(hk + (size_t)c * S_LEN), 0, S_LEN * 2, 0x00020000);
  int bo = (16 * i - 16 + 8 * g) * 2, bo1 = bo + 512;
  asm volatile("" : "+v"(bo1), "+v"(ao));
  f32x4 acc[4];
#pragma unroll
  for (int n = 0; n < 4; ++n) acc[n] = f32x4{0.f, 0.f, 0.f, 0.f};
  u32x4 A[NM], B[NM];
  auto ldA = [&](int m) { A[m] = __builtin_amdgcn_raw_buffer_load_b128(ur, ao, 512 * (NM - 1 - m), 0); };
  auto ldB = [&](int j) {
    if (j == 0) B[0] = __builtin_amdgcn_raw_buffer_load_b128(hr, bo, 0, 0);
    else B[j] = __builtin_amdgcn_raw_buffer_load_b128(hr, bo1, 512 * (j - 1), 0);
  };
#pragma unroll
  for (int j = 0; j < NM; ++j) ldB(j);
  const float dd = bflo(dk[c]);
  wait_vm<0>();
  const unsigned tch = __builtin_amdgcn_raw_buffer_load_b32(ur, (lane >> 5) * (PS_COPY * 2) + (N0 ? (lane & 31) * 128 : 2048 + (lane & 31) * 64), 0, 0);
#pragma unroll
  for (int m = 0; m < NM; ++m) ldA(m);
  u32x2 u[4];
#pragma unroll
  for (int n = 0; n < 4; ++n) {
    int uo = (2044 - (256 * (N0 + n) + 16 * i + 8 * (g & 1) + 4 * (g >> 1))) * 2;
    asm volatile("" : "+v"(uo));
    u[n] = __builtin_amdgcn_raw_buffer_load_b64(ur, uo, 0, 0);
  }
  asm volatile("" :: "v"(tch));
  if (N0) {
#pragma unroll 1
    for (int rho = 0; rho < 8; ++rho) {
      if (rho == SSM_HI) __builtin_amdgcn_s_setprio(SSM_PRIO);
      if (rho < 7) { ao -= 64; bo -= 64; bo1 -= 64; asm volatile("" : "+v"(ao), "+v"(bo), "+v"(bo1)); }
#pragma unroll
      for (int m = 0; m < NM; ++m) {
#pragma unroll
        for (int n = 3; n >= 0; --n)
          if (N0 + n - m >= 0) {
            acc[n] = MFMA16(__builtin_bit_cast(bf16x8, A[m]), __builtin_bit_cast(bf16x8, B[N0 + n - m]), acc[n]);
            SSM_PACE();
          }
        __builtin_amdgcn_sched_barrier(0);
        ldA(m); ldB(NM - 1 - m);
        __builtin_amdgcn_sched_barrier(0);
      }
    }
  } else {
    u32x4 A2[NM], B2[NM];
    auto round = [&](u32x4 (&Ac)[NM], u32x4 (&Bc)[NM], u32x4 (&An)[NM], u32x4 (&Bn)[NM]) {
#pragma unroll
      for (int m = 0; m < NM; ++m) {
        An[m] = __builtin_amdgcn_raw_buffer_load_b128(ur, ao, 512 * (NM - 1 - m), 0);
        Bn[m] = m == 0 ? __builtin_amdgcn_raw_buffer_load_b128(hr, bo, 0, 0) : __builtin_amdgcn_raw_buffer_load_b128(hr, bo1, 512 * (m - 1), 0);
      }
      __builtin_amdgcn_sched_barrier(0);
#pragma unroll
      for (int m = 0; m < NM; ++m)
#pragma unroll
        for (int n = 3; n >= m; --n) {
          acc[n] = MFMA16(__builtin_bit_cast(bf16x8, Ac[m]), __builtin_bit_cast(bf16x8, Bc[n - m]), acc[n]);
          SSM_PACE();
        }
      __builtin_amdgcn_sched_barrier(0);
    };
#pragma unroll 1
    for (int rho = 0; rho < 8; rho += 2) {
      if (rho == SSM_HI) __builtin_amdgcn_s_setprio(SSM_PRIO);
      ao -= 64; bo -= 64; bo1 -= 64; asm volatile("" : "+v"(ao), "+v"(bo), "+v"(bo1));
      round(A, B, A2, B2);
      if (rho < 6) { ao -= 64; bo -= 64; bo1 -= 64; asm volatile("" : "+v"(ao), "+v"(bo), "+v"(bo1)); }
      round(A2, B2, A, B);
    }
  }
  __builtin_amdgcn_s_setprio(3);
  const __amdgpu_buffer_rsrc_t zr = __builtin_amdgcn_make_buffer_rsrc((void*)(ws + WS_Z + (size_t)c * (S_LEN * 2)), 0, S_LEN * 2, 0x00020000);
#pragma unroll
  for (int n = 0; n < 4; ++n) {
    const int tq = 256 * (N0 + n) + 16 * i + 8 * (g & 1) + 4 * (g >> 1);
    const auto s02 = __builtin_amdgcn_permlane32_swap(__float_as_uint(acc[n][0]), __float_as_uint(acc[n][2]), false, false);
    const auto s13 = __builtin_amdgcn_permlane32_swap(__float_as_uint(acc[n][1]), __float_as_uint(acc[n][3]), false, false);
    const float z0 = __uint_as_float(s02[0]) + bfhi(u[n][1]) * dd;
    const float z1 = __uint_as_float(s02[1]) + bflo(u[n][1]) * dd;
    const float z2 = __uint_as_float(s13[0]) + bfhi(u[n][0]) * dd;
    const float z3 = __uint_as_float(s13[1]) + bflo(u[n][0]) * dd;
    __builtin_amdgcn_raw_buffer_store_b64(u32x2{pack2(z0, z1), pack2(z2, z3)}, zr, tq * 2, 0, 16);
  }
  wait_vm<0>();
  if (lane == 0) cnt_add(ws, (CNT_Z + (c >> 6) * 2 + (N0 ? 1 : 0)) * 128);
}

__device__ __forceinline__ f32x16 qk(bf16x8 k1, s16x4 k2, bf16x8 q1, s16x4 q2) {
  f32x16 z;
#pragma unroll
  for (int i = 0; i < 16; ++i) z[i] = 0.f;
  f32x16 s = __builtin_amdgcn_mfma_f32_32x32x16_bf16(k1, q1, z, 0, 0, 0);
  return __builtin_amdgcn_mfma_f32_32x32x8bf16_1k(k2, q2, s, 0, 0, 0);
}
__device__ __forceinline__ float max16(f32x16 s) {
  float m = fmaxf(s[0], s[1]);
#pragma unroll
  for (int i = 2; i < 16; ++i) m = fmaxf(m, s[i]);
  return m;
}
__device__ __forceinline__ s16x4 mk2(unsigned a, unsigned b) { return __builtin_bit_cast(s16x4, u32x2{a, b}); }
__device__ __forceinline__ float swap32_max(float m) {
  const auto s = __builtin_amdgcn_permlane32_swap(__float_as_uint(m), __float_as_uint(m), false, false);
  return fmaxf(__uint_as_float(s[0]), __uint_as_float(s[1]));
}

#define ATTN_CLOB "v96","v97","v98","v99","v100","v101","v102","v103","v104","v105","v106","v107","v108","v109","v110","v111", \
  "v112","v113","v114","v115","v116","v117","v118","v119","v120","v121","v122","v123","v124","v125","v126","v127", \
  "v128","v129","v130","v131","v132","v133","v134","v135","v136","v137","v138","v139","v140","v141","v142","v143"
#define EX1(a) "v_exp_f32 v" #a ", v" #a "\n"
#define EX2(a, b) EX1(a) EX1(b)
#define EX3(a, b, c) EX1(a) EX1(b) EX1(c)
#define EX4(a, b, c, d) EX1(a) EX1(b) EX1(c) EX1(d)
#define ATILE(OVHI, OK1N, K2C, QB2, K2N, QA2, OVLON, X3, X4, X6, X7) \
  EX2(96, 97) \
  "v_smfmac_f32_32x32x32_bf16 %[ob], v[140:143], v[120:127], %[idx]\n" \
  "ds_read_b128 v[140:143], %[av] offset:" OVHI "\n" \
  EX3(98, 99, 100) \
  "v_mfma_f32_32x32x8_bf16 v[112:127], " K2C ", %[" QB2 "], 0\n" \
  "s_waitcnt lgkmcnt(1)\n" \
  EX4(101, 102, 103, 104) \
  "v_mfma_f32_32x32x16_bf16 v[112:127], v[128:131], %[qb1], v[112:127]\n" \
  "ds_read_b128 v[128:131], %[ak1] offset:" OK1N "\n" \
  X3 \
  EX4(105, 106, 107, 108) \
  "v_smfmac_f32_32x32x32_bf16 %[oa], v[136:139], v[96:103], %[idx]\n" \
  X4 \
  EX3(109, 110, 111) \
  EX2(112, 113) \
  "v_smfmac_f32_32x32x32_bf16 %[oa], v[140:143], v[104:111], %[idx]\n" \
  "s_waitcnt lgkmcnt(0)\n" \
  EX3(114, 115, 116) \
  "v_mfma_f32_32x32x8_bf16 v[96:111], " K2N ", %[" QA2 "], 0\n" \
  X6 \
  EX4(117, 118, 119, 120) \
  "v_mfma_f32_32x32x16_bf16 v[96:111], v[128:131], %[qa1], v[96:111]\n" \
  X7 \
  EX4(121, 122, 123, 124) \
  "v_smfmac_f32_32x32x32_bf16 %[ob], v[136:139], v[112:119], %[idx]\n" \
  "ds_read_b128 v[136:139], %[av] offset:" OVLON "\n" \
  EX3(125, 126, 127)
#define ITSTAMP
#define ITCLOB
#define WL2 "s_waitcnt lgkmcnt(2)\n"
#define WL1 "s_waitcnt lgkmcnt(1)\n"
#define SPIN(T) "s_cbranch_scc0 L_attn_go" T "_%=\n L_attn_spin" T "_%=:\n ds_read_b32 %[tmp], %[syn]\n s_add_i32 %[chk], %[chk], 16\n s_waitcnt lgkmcnt(0)\n" \
  " v_readfirstlane_b32 %[stmp], %[tmp]\n s_cmp_gt_u32 %[chk], 0x10000000\n s_cbranch_scc1 L_attn_go" T "_%=\n s_cmp_lt_u32 %[stmp], 4\n" \
  " s_cbranch_scc1 L_attn_spin" T "_%=\n L_attn_go" T "_%=:\n"
#define ASG(O, A6, A7, B6, B7) \
  ATILE(O "+640", O "+1024", "v[132:133]", "qb2e", "v[132:133]", "qa2o", O "+1280", "", WL1, "", "") \
  ATILE(O "+1920", O "+2048", "v[132:133]", "qb2o", "v[134:135]", "qa2e", O "+2560", "", WL1, "", "") \
  ATILE(O "+3200", O "+3072", "v[134:135]", "qb2e", "v[134:135]", "qa2o", O "+3840", "", WL1, A6, A7) \
  ATILE(O "+4480", O "+9744", "v[134:135]", "qb2o", "v[132:133]", "qa2e", O "+9744", \
        "ds_read_b128 v[132:135], %[ak2] offset:" O "+9744\n", WL2, B6, B7)
#define ATTN_LOOP_ASM \
  "ds_read_b128 v[128:131], %[ak1]\n" \
  "ds_read_b128 v[132:135], %[ak2]\n" \
  "ds_read_b128 v[136:139], %[av]\n" \
  "v_mov_b32 v140, 0\n v_mov_b32 v141, 0\n v_mov_b32 v142, 0\n v_mov_b32 v143, 0\n" \
  "v_mov_b32 v120, 0\n v_mov_b32 v121, 0\n v_mov_b32 v122, 0\n v_mov_b32 v123, 0\n" \
  "v_mov_b32 v124, 0\n v_mov_b32 v125, 0\n v_mov_b32 v126, 0\n v_mov_b32 v127, 0\n" \
  "s_waitcnt lgkmcnt(1)\n" \
  "v_mfma_f32_32x32x16_bf16 v[96:111], v[128:131], %[qa1], 0\n" \
  "v_mfma_f32_32x32x8_bf16 v[96:111], v[132:133], %[qa2e], v[96:111]\n" \
  "s_nop 11\n" \
  "L_attn_loop_%=:\n" ITSTAMP \
  ASG("0", "s_cmp_eq_u32 %[it], %[chk]\n", SPIN("a"), "s_add_i32 %[it], %[it], 1\n", "") \
  ASG("9744", "s_cmp_eq_u32 %[it], %[chk]\n", SPIN("b"), "s_add_i32 %[it], %[it], 1\n", "s_cmp_lt_i32 %[it], %[nit]\n") \
  "v_add_u32 %[ak1], 19488, %[ak1]\n" \
  "v_add_u32 %[ak2], 19488, %[ak2]\n" \
  "v_add_u32 %[av], 19488, %[av]\n" \
  "s_cbranch_scc1 L_attn_loop_%=\n" \
  "v_smfmac_f32_32x32x32_bf16 %[ob], v[140:143], v[120:127], %[idx]\n" \
  "s_waitcnt lgkmcnt(0)\n" \
  "s_nop 15\n"

__device__ __forceinline__ f32x16 attn_exact(__amdgpu_buffer_rsrc_t ir, int offK1, int offK2, int offV, int hh, bf16x8 q1, unsigned q2) {
  const s16x4 x2 = mk2(hh ? 0u : q2, 0u);
  float m = -INFINITY;
#pragma unroll 1
  for (int tile = 0; tile < 64; ++tile) {
    const int base = (tile >> 2) * SG_BYTES;
    const s16x4 kk = mk2(__builtin_amdgcn_raw_buffer_load_b32(ir, base + offK2 + (hh ? 0 : (tile & 3) * 4), 0, 16), 0u);
    const bf16x8 k1 = __builtin_bit_cast(bf16x8, __builtin_amdgcn_raw_buffer_load_b128(ir, base + offK1 + (tile & 3) * 1024, 0, 16));
    m = fmaxf(m, max16(qk(k1, kk, q1, x2)));
  }
  m = swap32_max(m);
  f32x16 O;
#pragma unroll
  for (int i = 0; i < 16; ++i) O[i] = 0.f;
#pragma unroll 1
  for (int tile = 0; tile < 64; ++tile) {
    const int base = (tile >> 2) * SG_BYTES;
    const s16x4 kk = mk2(__builtin_amdgcn_raw_buffer_load_b32(ir, base + offK2 + (hh ? 0 : (tile & 3) * 4), 0, 16), 0u);
    const bf16x8 k1 = __builtin_bit_cast(bf16x8, __builtin_amdgcn_raw_buffer_load_b128(ir, base + offK1 + (tile & 3) * 1024, 0, 16));
    const bf16x8 vlo = __builtin_bit_cast(bf16x8, __builtin_amdgcn_raw_buffer_load_b128(ir, base + offV + (tile & 3) * 1280, 0, 16));
    const bf16x8 vhi = __builtin_bit_cast(bf16x8, __builtin_amdgcn_raw_buffer_load_b128(ir, base + offV + (tile & 3) * 1280 + 640, 0, 16));
    f32x16 S = qk(k1, kk, q1, x2);
#pragma unroll
    for (int i = 0; i < 16; ++i) S[i] = __builtin_amdgcn_exp2f(S[i] - m);
    O = __builtin_amdgcn_smfmac_f32_32x32x32_bf16(vlo, __builtin_bit_cast(bf16x16, __builtin_shufflevector(S, S, 0, 1, 2, 3, 4, 5, 6, 7)), O, 0xDDDDDDDD, 0, 0);
    O = __builtin_amdgcn_smfmac_f32_32x32x32_bf16(vhi, __builtin_bit_cast(bf16x16, __builtin_shufflevector(S, S, 8, 9, 10, 11, 12, 13, 14, 15)), O, 0xDDDDDDDD, 0, 0);
  }
  return O;
}

#define EARLY_POST_PRIO 2
#define OUT_AUX 16
#define Q_SLEEP 4
#define KV_DELAY 0
#define O_SLEEP 4
#define NLATE 6
constexpr int T_QX = 16384;
constexpr int T_WO = 58624;
constexpr int T_XCH = T_WO + 73728;
constexpr int T_Z = 0;
constexpr int T_PART = 10240;
constexpr int T_EPI = 30720;
static_assert(NLATE % 2 == 0 && T_WO >= NLATE * SG_BYTES && T_XCH + 24576 <= T_SYNC && T_EPI + 12800 <= T_WO, "lds");
constexpr int P_EARLY = NLATE * SG_BYTES / 1024;
constexpr int NG_HI = (153 - (P_EARLY + 20) + 3) / 4;
static_assert(P_EARLY + 20 + NG_HI * 4 <= IMG_PIECES && 12 + (32 - NG_HI) * 4 >= P_EARLY && (P_EARLY + 20) * 1024 >= (NLATE + 2) * SG_BYTES, "dma plan");
static_assert(NG_HI >= 16 && T_QX >= 12288 && T_QX + 12288 <= P_EARLY * 1024, "q hand-over area");

__device__ __forceinline__ void img_group(const char* src, char* sm, int p) {
  const GLB_AS unsigned* g = (const GLB_AS unsigned*)(src + p * 1024);
  LDS_AS unsigned* l = (LDS_AS unsigned*)(sm + p * 1024);
  __builtin_amdgcn_global_load_lds(g, l, 16, 0, 16);
  __builtin_amdgcn_global_load_lds(g, l, 16, 1024, 16);
  __builtin_amdgcn_global_load_lds(g, l, 16, 2048, 16);
  __builtin_amdgcn_global_load_lds(g, l, 16, 3072, 16);
}

extern "C" __global__ void __launch_bounds__(768) k_main(const u16* __restrict__ x, const u16* __restrict__ wq,
                                               const u16* __restrict__ wk, const u16* __restrict__ wv,
                                               const u16* __restrict__ gw, const u16* __restrict__ bk,
                                               const u16* __restrict__ bv, const u16* __restrict__ shk, char* ws,
                                               const u16* __restrict__ hk, const u16* __restrict__ dk,
                                               const u16* __restrict__ wo, u16* __restrict__ out) {
  __shared__ __attribute__((aligned(256))) char sm[163840];
  const int tid = threadIdx.x, lane = tid & 63;
  const int wave = __builtin_amdgcn_readfirstlane(tid >> 6);
  const int b = blockIdx.x;
  const int grp = b & 7, loc = b >> 3;
  volatile LDS_AS unsigned* sync = (volatile LDS_AS unsigned*)(sm + T_SYNC);
  STAMP_DECL;
  STAMP(0);
  if (wave == 6 && loc < 26) code_prefetch(loc >= 18, lane);
  if (wave == 7) {
    const u64 e = __hip_atomic_load((GLB_AS u64*)(ws + WS_FLAG + FL_E), __ATOMIC_RELAXED, __HIP_MEMORY_SCOPE_AGENT) + 1u;
    if (lane < 16) sync[lane] = lane < 14 ? 0u : (lane == 14 ? (unsigned)e : (unsigned)(e >> 32));
  }
  if (loc < 18) q_tile(x, wq, ws, sm, grp, loc, wave, lane STAMP_PASS);
  else if (loc < 26) kvg_tile(x, wk, wv, gw, ws, sm, grp * 8 + loc - 18, tid, wave, lane);
  else { __builtin_amdgcn_s_sleep(KV_DELAY); u_tile(bk, bv, shk, ws, sm, grp, loc - 26, tid, wave, lane); }
  STAMP(13);

  const bool has_tile = loc < (grp < 2 ? 30 : 29);
  const int qid = has_tile ? grp * 29 + (grp < 2 ? grp : 2) + loc : 0;
  const int mt = qid / 9, nt = qid - mt * 9;
  const int t0 = mt * 80, n0 = nt * 64;
  const __amdgpu_buffer_rsrc_t orsrc = __builtin_amdgcn_make_buffer_rsrc((void*)(ws + WS_O), 0, (int)O_BYTES, 0x00020000);
  const u64 tag = lds_tag(sm);
  const int cwi = wave - 2;
  const int kh = cwi >= 5 ? 1 : 0, cw = cwi - kh * 5;
  const int g = lane >> 4, tl = lane & 15;
  u32x2 xv[2]; float gt;
  auto load_xg = [&]() {
    const int te = min(t0 + max(cw, 0) * 16 + tl, S_LEN - 1);
#pragma unroll
    for (int h = 0; h < 2; ++h) xv[h] = *(const u32x2*)(x + (size_t)te * DM + n0 + kh * 32 + h * 16 + g * 4);
    gt = __uint_as_float(__builtin_amdgcn_raw_buffer_load_b32(__builtin_amdgcn_make_buffer_rsrc((void*)(ws + WS_G), 0, S_LEN * 4, 0x00020000), te * 4, 0, 16));
    asm volatile("" ::: "memory");
  };

  if (wave < 4) {
    __builtin_amdgcn_s_setprio(3);
    const int W = b < 208 ? b + 208 * wave : 4 * b + wave;
    if (wave == 0) { flag_wait<2>(ws, FL_S + grp * 512 + (lane < 8 ? lane : (NLATE - 1) * 4 + (lane & 15)) * 8, tag); if (lane == 0) sync[7] = 1u; }
    else lds_wait(sync + 7, 1u);
    STAMP(14);
    const char* src = ws + WS_IMG + lane * 16;
    img_group(src, sm, P_EARLY + wave * 4);
    img_group(src, sm, wave == 0 ? P_EARLY + 16 : wave * 4 - 4);
    if (wave == 0) flag_wait<2>(ws, FL_S + grp * 512 + lane * 8, tag);
    wait_vm<0>();
    RAW_BARRIER();
#pragma unroll
    for (int i = 0; i < 8; ++i) {
      if (i == 4) lds_wait(sync + 10, 4u);
      int p = P_EARLY + 20 + (wave + 4 * i) * 4;
      p = p > 152 ? p - (P_EARLY + 20 + NG_HI * 4) + 12 : p;
      img_group(src, sm, p);
    }
    wait_vm<0>();
    if (lane == 0) __hip_atomic_fetch_add((LDS_AS unsigned*)sync + 5, 1u, __ATOMIC_RELAXED, __HIP_MEMORY_SCOPE_WORKGROUP);
    {
      const int c1 = W < 576 ? W : W - 576, c2 = (W >= 576 && W < 576 + 128) ? W - 128 : c1;
      flag_wait<8>(ws, FL_U + (((lane & 16) ? c2 : c1) >> 6) * 128 + (lane & 15) * 8, tag);
      const __amdgpu_buffer_rsrc_t us = __builtin_amdgcn_make_buffer_rsrc((void*)(ws + WS_UR), 0, DM * S_LEN * 2, 0x00020000);
      const __amdgpu_buffer_rsrc_t up = __builtin_amdgcn_make_buffer_rsrc((void*)(ws + WS_PS), 0, (int)(WS_G - WS_PS), 0x00020000);
      u32x4 ud[4]; unsigned un[4]; int upo[4];
#pragma unroll
      for (int q = 0; q < 4; ++q) {
        const int cc = q < 2 ? c1 : c2, blk = W < 576 ? q : 2 + (q & 1);
        const int uo = cc * (S_LEN * 2) + blk * 1024 + lane * 16;
        upo[q] = (cc * 2 + (W < 576 ? 1 : 0)) * (PS_COPY * 4) + blk * 1024 + lane * 16;
        ud[q] = __builtin_amdgcn_raw_buffer_load_b128(us, uo, 0, 16);
        un[q] = __builtin_amdgcn_raw_buffer_load_b32(us, uo + 16, 0, 16);
        if (blk == 3 && lane == 63) un[q] = 0u;
      }
#pragma unroll
      for (int q = 0; q < 4; ++q) {
        const u32x4 sh = {__builtin_amdgcn_alignbit(ud[q][1], ud[q][0], 16), __builtin_amdgcn_alignbit(ud[q][2], ud[q][1], 16),
                          __builtin_amdgcn_alignbit(ud[q][3], ud[q][2], 16), __builtin_amdgcn_alignbit(un[q], ud[q][3], 16)};
        __builtin_amdgcn_raw_buffer_store_b128(ud[q], up, upo[q], 0, PS_AUX);
        __builtin_amdgcn_raw_buffer_store_b128(sh, up, upo[q] + PS_COPY * 2, 0, PS_AUX);
        if ((q & 1) && lane < 4 && (q == 3 || W >= 576))
          __builtin_amdgcn_raw_buffer_store_b128(u32x4{0u, 0u, 0u, 0u}, up, upo[q] + 1024 + (lane >> 1) * (PS_COPY * 2 - 32), 0, PS_AUX);
      }
    }
    STAMP(1);
    if (wave == 3) {
#pragma unroll
      for (int i = 0; i < 9; ++i)
        __builtin_amdgcn_raw_buffer_store_b128(u32x4{O_EMPTY, O_EMPTY, O_EMPTY, O_EMPTY}, orsrc,
                                               ((b >> 1) * 72 + i * 8 + (lane >> 3)) * 256 + (b & 1) * 128 + (lane & 7) * 16, 0, 16);
      wait_vm<0>();
      if (lane == 0) { cnt_add(ws, (CNT_F + b / 10) * 128); sync[6] = 1u; }
    }
    STAMP(9);
    if (W < 576) {
      ssm_unit<4>(W, hk, dk, ws, lane);
    } else {
      for (int c = W - 576; c < 576; c += 448) ssm_unit<0>(c, hk, dk, ws, lane);
    }
    if (b == 0 && wave == 1) {
      cnt_wait<8>(ws, (CNT_F + min(lane, 25)) * 128, lane >= 25 ? 6u : 10u);
      if (lane == 0) flag_set(ws, FL_E, tag + 1u);
    }
    load_xg();
    wait_vm<0>();
    STAMP(2);
  } else {
    const int pair = wave & 3;
    const bool early = wave < 8;
    __builtin_amdgcn_s_setprio(2);
    const int r = lane & 31, hh = lane >> 5;
    const int tq = b * 8 + pair * 2;
    const int offK1 = SG_K1 + lane * 16;
    const int offK2 = SG_K2 + (hh ? 512 : r * 16);
    const int offV = SG_V + hh * 320 + vpos(r) * 16;
    const int idx = 0xDDDDDDDD;
    const unsigned ldsBase = (unsigned)(uintptr_t)(LDS_AS char*)sm + (early ? NLATE * SG_BYTES : 0);

    const __amdgpu_buffer_rsrc_t irsrc = __builtin_amdgcn_make_buffer_rsrc((void*)(ws + WS_IMG), 0, IMG_BYTES, 0x00020000);
    bf16x8 qa1, qb1; unsigned qa2, qb2;
    f32x16 Oa, Ob;
    {
#pragma unroll
      for (int i = 0; i < 16; ++i) { Oa[i] = 0.f; Ob[i] = 0.f; }
      unsigned aK1 = ldsBase + offK1, aK2 = ldsBase + offK2, aV = ldsBase + offV;
      asm volatile("" : "+v"(Oa), "+v"(Ob), "+v"(aK1), "+v"(aK2), "+v"(aV) :: "memory");
      const __amdgpu_buffer_rsrc_t qr = __builtin_amdgcn_make_buffer_rsrc((void*)(ws + WS_Q + (size_t)tq * (DM * 2)), 0, DM * 4, 0x00020000);
      auto load_q = [&]() {
#pragma unroll 1
        for (int spin = 0; spin < (1 << 22); ++spin) {
          asm volatile("" ::: "memory");
          const u32x4 ta = __builtin_amdgcn_raw_buffer_load_b128(qr, r * 36 + hh * 16, 0, 16);
          const u32x4 tb = __builtin_amdgcn_raw_buffer_load_b128(qr, r * 36 + hh * 16 + DM * 2, 0, 16);
          qa2 = __builtin_amdgcn_raw_buffer_load_b32(qr, r * 36 + 32, 0, 16);
          qb2 = __builtin_amdgcn_raw_buffer_load_b32(qr, r * 36 + 32 + DM * 2, 0, 16);
          qa1 = __builtin_bit_cast(bf16x8, ta); qb1 = __builtin_bit_cast(bf16x8, tb);
          const unsigned mq = max(max(max(max(ta[0], ta[1]), max(ta[2], ta[3])), max(max(tb[0], tb[1]), max(tb[2], tb[3]))), max(qa2, qb2));
          if (__all(mq != O_EMPTY)) break;
          __builtin_amdgcn_s_sleep(Q_SLEEP);
        }
      };
      u32x4* qx = (u32x4*)(sm + T_QX + pair * 3072) + lane;
      unsigned pm;
      if (early) {
        if (wave == 7) { flag_wait<4>(ws, lane < 9 ? FL_Q + (b >> 4) * 128 + lane * 8 : FL_S + grp * 512, tag); if (lane == 0) sync[8] = 1u; }
        else lds_wait(sync + 8, 1u);
        const bf16x8 k1 = __builtin_bit_cast(bf16x8, __builtin_amdgcn_raw_buffer_load_b128(irsrc, offK1, 0, 16));
        const unsigned k2 = hh ? 0u : __builtin_amdgcn_raw_buffer_load_b32(irsrc, offK2, 0, 16);
        STAMP(14);
        load_q();
        const float ma = swap32_max(max16(qk(k1, mk2(k2, 0u), qa1, mk2(hh ? 0u : qa2, 0u))));
        const float mb = swap32_max(max16(qk(k1, mk2(k2, 0u), qb1, mk2(hh ? 0u : qb2, 0u))));
        pm = pack2(-ma, -mb);
        qx[0] = __builtin_bit_cast(u32x4, qa1); qx[64] = __builtin_bit_cast(u32x4, qb1); qx[128] = u32x4{qa2, qb2, pm, 0u};
        STAMP(9);
        RAW_BARRIER();
      } else {
        __builtin_amdgcn_s_setprio(3);
        if (loc >= 26) { u_store(ws, sm, grp, loc - 26, wave, lane); u_publish(ws, sm, grp, loc - 26, lane); }
        RAW_BARRIER();
        qa1 = __builtin_bit_cast(bf16x8, qx[0]); qb1 = __builtin_bit_cast(bf16x8, qx[64]);
        const u32x4 e = qx[128];
        qa2 = e[0]; qb2 = e[1]; pm = e[2];
        WAIT_LGKM0();
        if (lane == 0) __hip_atomic_fetch_add((LDS_AS unsigned*)sync + 10, 1u, __ATOMIC_RELAXED, __HIP_MEMORY_SCOPE_WORKGROUP);
        __builtin_amdgcn_s_setprio(LATE_PRIO);
      }
      STAMP(1); CSTAMP(10);
      const unsigned xa = hh ? 0u : qa2, xb = hh ? 0u : qb2;
      const unsigned nma = pm & 0xffffu, nmb = pm >> 16;
      const s16x4 qa2e = mk2(hh ? nma : qa2, 0u), qa2o = mk2(hh ? nma : 0u, xa);
      const s16x4 qb2e = mk2(hh ? nmb : qb2, 0u), qb2o = mk2(hh ? nmb : 0u, xb);
      int it = 0;
      const int nit = early ? 16 - NLATE : NLATE;
      int chk = 2 - (wave >> 2), stmp;
      unsigned tmp;
      const unsigned syn = (unsigned)(uintptr_t)(sync + 5);
      asm volatile(ATTN_LOOP_ASM
                   : [oa] "+v"(Oa), [ob] "+v"(Ob), [ak1] "+v"(aK1), [ak2] "+v"(aK2), [av] "+v"(aV), [it] "+s"(it),
                     [chk] "+s"(chk), [stmp] "=&s"(stmp), [tmp] "=&v"(tmp)
                   : [qa1] "v"(qa1), [qb1] "v"(qb1), [qa2e] "v"(qa2e), [qa2o] "v"(qa2o), [qb2e] "v"(qb2e), [qb2o] "v"(qb2o),
                     [idx] "v"(idx), [nit] "s"(nit), [syn] "v"(syn)
                   : "memory", "scc", ATTN_CLOB ITCLOB);
    }
    STAMP(2); CSTAMP(11);
    load_xg();
    f32x4* xch = (f32x4*)(sm + T_XCH + pair * 6144) + lane;
    if (early) {
      __builtin_amdgcn_s_setprio(EARLY_POST_PRIO);
      if (lane == 0) __hip_atomic_fetch_add((LDS_AS unsigned*)sync, 1u, __ATOMIC_RELAXED, __HIP_MEMORY_SCOPE_WORKGROUP);
      lds_wait(sync, 4u);
      lds_wait(sync + 6, 1u);
#pragma unroll
      for (int i = 0; i < 3; ++i) {
        xch[i * 64] = f32x4{Oa[4 * i], Oa[4 * i + 1], Oa[4 * i + 2], Oa[4 * i + 3]};
        xch[(3 + i) * 64] = f32x4{Ob[4 * i], Ob[4 * i + 1], Ob[4 * i + 2], Ob[4 * i + 3]};
      }
      WAIT_LGKM0();
      if (lane == 0) sync[1 + pair] = 1u;
      const char* wsrc = (const char*)(wo + n0) + (size_t)(pair * 144 + (lane >> 3)) * (DM * 2);
      const int c0 = ((lane & 7) ^ (wsw(lane >> 3) << 1)) << 4;
#pragma unroll
      for (int i = 0; i < 18; ++i) glds16(wsrc + i * 8 * DM * 2 + (c0 ^ ((i & 1) << 6)), sm + T_WO + pair * 18432 + i * 1024);
      if (wave == 4) cnt_wait<8>(ws, (CNT_F + mt) * 128, mt == 25 ? 6u : 10u);
      wait_vm<0>();
    } else {
      __builtin_amdgcn_s_setprio(3);
      lds_wait(sync + 1 + pair, 1u);
      float big = 0.f;
#pragma unroll
      for (int i = 0; i < 3; ++i) {
        const f32x4 pa = xch[i * 64], pb = xch[(3 + i) * 64];
#pragma unroll
        for (int e = 0; e < 4; ++e) if (4 * i + e < 11) {
          Oa[4 * i + e] += pa[e]; Ob[4 * i + e] += pb[e];
          big += fabsf(Oa[4 * i + e]) + fabsf(Ob[4 * i + e]);
        }
      }
      if (__any(!(big < 1e30f) || (!hh && !(fminf(Oa[10], Ob[10]) > 1e-12f)))) {
        Oa = attn_exact(irsrc, offK1, offK2, offV, hh, qa1, qa2);
        Ob = attn_exact(irsrc, offK1, offK2, offV, hh, qb1, qb2);
      }
      STAMP(3);
      const auto la = __builtin_amdgcn_permlane32_swap(__float_as_uint(Oa[10]), __float_as_uint(Oa[10]), false, false);
      const auto lb = __builtin_amdgcn_permlane32_swap(__float_as_uint(Ob[10]), __float_as_uint(Ob[10]), false, false);
      const float ia = __builtin_amdgcn_rcpf(__uint_as_float(la[0])), ib = __builtin_amdgcn_rcpf(__uint_as_float(lb[0]));
      char* ost = sm + T_XCH + pair * 6144;
      char* od = ost + r * 36 + hh * 8;
      *(unsigned*)(od) = pack2(Oa[0] * ia, Oa[1] * ia);        *(unsigned*)(od + 1168) = pack2(Ob[0] * ib, Ob[1] * ib);
      *(unsigned*)(od + 4) = pack2(Oa[2] * ia, Oa[3] * ia);    *(unsigned*)(od + 1172) = pack2(Ob[2] * ib, Ob[3] * ib);
      *(unsigned*)(od + 16) = pack2(Oa[4] * ia, Oa[5] * ia);   *(unsigned*)(od + 1184) = pack2(Ob[4] * ib, Ob[5] * ib);
      *(unsigned*)(od + 20) = pack2(Oa[6] * ia, Oa[7] * ia);   *(unsigned*)(od + 1188) = pack2(Ob[6] * ib, Ob[7] * ib);
      if (!hh) { *(unsigned*)(od + 32) = pack2(Oa[8] * ia, Oa[9] * ia); *(unsigned*)(od + 1200) = pack2(Ob[8] * ib, Ob[9] * ib); }
      u32x4 ov[3];
#pragma unroll
      for (int i = 0; i < 3; ++i) ov[i] = *(const u32x4*)(ost + (lane & 1) * 1168 + (i * 32 + (lane >> 1)) * 16);
#pragma unroll
      for (int i = 0; i < 3; ++i) {
#pragma unroll
        for (int e = 0; e < 4; ++e) ov[i][e] = min(ov[i][e], O_EMPTY - 1u);
        if (i < 2 || lane < 16)
          __builtin_amdgcn_raw_buffer_store_b128(ov[i], orsrc, (((tq >> 4) * 72 + i * 32 + (lane >> 1)) * 16 + (tq & 15) + (lane & 1)) * 16, 0, 16);
      }
    }
  }
  STAMP(4);
  if (!has_tile) { STAMP_FLUSH(1); return; }

  RAW_BARRIER();
  STAMP(5);
  if (wave < 2) {
    cnt_wait<2>(ws, (CNT_Z + nt * 2 + (((lane & 1) ? t0 : t0 + 79) < 1024 ? 0 : 1)) * 128, 64u);
    const __amdgpu_buffer_rsrc_t zrsrc = __builtin_amdgcn_make_buffer_rsrc((void*)(ws + WS_Z), 0, (int)Z_BYTES, 0x00020000);
    u32x4 zv[5];
#pragma unroll
    for (int i = 0; i < 5; ++i) {
      const int ci = (wave * 5 + i) * 64 + lane, c = ci / 10, k = ci - c * 10;
      zv[i] = __builtin_amdgcn_raw_buffer_load_b128(zrsrc, ((n0 + c) * S_LEN + t0 + k * 8) * 2, 0, 16);
    }
#pragma unroll
    for (int i = 0; i < 5; ++i) *(u32x4*)(sm + T_Z + ((wave * 5 + i) * 64 + lane) * 16) = zv[i];
    WAIT_LGKM0();
  } else {
    float bx[2][4];
    const float omg = 1.0f - gt;
#pragma unroll
    for (int h = 0; h < 2; ++h) {
      bx[h][0] = omg * bflo(xv[h][0]); bx[h][1] = omg * bfhi(xv[h][0]);
      bx[h][2] = omg * bflo(xv[h][1]); bx[h][3] = omg * bfhi(xv[h][1]);
    }
    const int rgp = mt * 5 + cw;
    const int obase = ((rgp * 18 + kh * 9) * 64 + lane) * 16;
    const char* wl = sm + T_WO + kh * 9 * 4096;
    constexpr int NPRE = 5;
    bf16x8 wpre[NPRE][4];
    int so[9];
#pragma unroll
    for (int s = 0; s < 9; ++s) so[s] = s + nt >= 9 ? s + nt - 9 : s + nt;
#pragma unroll
    for (int s = 0; s < NPRE; ++s)
#pragma unroll
      for (int nb = 0; nb < 4; ++nb) wpre[s][nb] = w_frag(wl + so[9 - NPRE + s] * 4096, lane, 0, nb);
    unsigned wq4[4];
#pragma unroll
    for (int nb = 0; nb < 4; ++nb) {
      const int k = g * 8 + (tl >> 2);
      wq4[nb] = (unsigned)(uintptr_t)(LDS_AS char*)sm + T_WO + kh * 9 * 4096 + k * 128 + ((nb ^ wsw(k)) << 5) + (lane & 3) * 8;
    }
    STAMP(6);
    f32x4 acc[4];
#pragma unroll 1
    for (int spin = 0; spin < (1 << 20); ++spin) {
      asm volatile("" : "+v"(wq4[0]), "+v"(wq4[1]), "+v"(wq4[2]), "+v"(wq4[3]) :: "memory");
      u32x4 of[9];
#pragma unroll
      for (int s = 0; s < 9; ++s) of[s] = __builtin_amdgcn_raw_buffer_load_b128(orsrc, obase, so[s] * 1024, 16);
      __builtin_amdgcn_sched_barrier(0);
#pragma unroll
      for (int nb = 0; nb < 4; ++nb) acc[nb] = f32x4{0.f, 0.f, 0.f, 0.f};
      unsigned mx = 0;
#pragma unroll
      for (int s = 0; s < 9; ++s) {
#pragma unroll
        for (int nb = 0; nb < 4; ++nb) {
          bf16x8 wf;
          if (s >= 9 - NPRE) wf = wpre[s - (9 - NPRE)][nb];
          else {
            LDS_AS bf16x4* wp = (LDS_AS bf16x4*)(uintptr_t)(wq4[nb] + so[s] * 4096);
            wf = __builtin_shufflevector(__builtin_amdgcn_ds_read_tr16_b64_v4bf16(wp), __builtin_amdgcn_ds_read_tr16_b64_v4bf16(wp + 64), 0, 1, 2, 3, 4, 5, 6, 7);
          }
          acc[nb] = MFMA16(wf, __builtin_bit_cast(bf16x8, of[s]), acc[nb]);
        }
        mx = max(max(mx, max(of[s][0], of[s][1])), max(of[s][2], of[s][3]));
      }
      if (__all(mx != O_EMPTY)) break;
      __builtin_amdgcn_s_sleep(O_SLEEP);
    }
    f32x4* pd = (f32x4*)(sm + T_PART + cwi * 2048) + lane;
    pd[0] = kh ? acc[0] : acc[2];
    pd[64] = kh ? acc[1] : acc[3];
    STAMP(7);
    RAW_BARRIER();
    const f32x4* ps = (const f32x4*)(sm + T_PART + (kh ? cw : cw + 5) * 2048) + lane;
    char* sE = sm + T_EPI + cwi * 1280;
    f32x4 pa[2]; u32x2 z[2];
#pragma unroll
    for (int h = 0; h < 2; ++h) {
      pa[h] = ps[h * 64];
      z[h] = __builtin_bit_cast(u32x2, tr_read(sm + T_Z + ((kh * 2 + h) * 16 + g * 4 + (tl >> 2)) * 160 + (cw * 16 + (tl & 3) * 4) * 2));
    }
    asm volatile("" ::: "memory");
#pragma unroll
    for (int h = 0; h < 2; ++h) {
      const f32x4 a = (kh ? acc[2 + h] : acc[h]) + pa[h];
      *(u32x2*)(sE + tl * 80 + h * 32 + g * 8) =
          u32x2{pack2(gt * a[0] + bx[h][0] * bflo(z[h][0]), gt * a[1] + bx[h][1] * bfhi(z[h][0])),
                pack2(gt * a[2] + bx[h][2] * bflo(z[h][1]), gt * a[3] + bx[h][3] * bfhi(z[h][1]))};
    }
    const u32x4 o = *(const u32x4*)(sE + (lane >> 2) * 80 + (lane & 3) * 16);
    const __amdgpu_buffer_rsrc_t outr = __builtin_amdgcn_make_buffer_rsrc((void*)out, 0, S_LEN * DM * 2, 0x00020000);
    __builtin_amdgcn_raw_buffer_store_b128(o, outr, ((t0 + cw * 16 + (lane >> 2)) * DM + n0 + kh * 32 + (lane & 3) * 8) * 2, 0, OUT_AUX);
  }
  STAMP(8);
  STAMP_FLUSH(1);
}

extern "C" __global__ void k9_end() {}

extern "C" void kernel_launch(void* const* d_in, const int* in_sizes, int n_in, void* d_out, int out_size,
                              void* d_ws, size_t ws_size, hipStream_t stream) {
  const u16* x = (const u16*)d_in[0];
  const u16* wq = (const u16*)d_in[1];
  const u16* wk = (const u16*)d_in[2];
  const u16* wv = (const u16*)d_in[3];
  const u16* wo = (const u16*)d_in[4];
  const u16* bk = (const u16*)d_in[6];
  const u16* bv = (const u16*)d_in[7];
  const u16* shk = (const u16*)d_in[8];
  const u16* hk = (const u16*)d_in[9];
  const u16* dk = (const u16*)d_in[10];
  const u16* gw = (const u16*)d_in[11];
  char* ws = (char*)d_ws;
  k_main<<<256, 768, 0, stream>>>(x, wq, wk, wv, gw, bk, bv, shk, ws, hk, dk, wo, (u16*)d_out);
}
```

```cpp
#include <hip/hip_runtime.h>
#include <hip/hip_bf16.h>
#include <stdint.h>

typedef __attribute__((ext_vector_type(4))) __bf16 bf16x4;
typedef __attribute__((ext_vector_type(8))) __bf16 bf16x8;
typedef __attribute__((ext_vector_type(16))) __bf16 bf16x16;
typedef __attribute__((ext_vector_type(4))) short s16x4;
typedef __attribute__((ext_vector_type(4))) float f32x4;
typedef __attribute__((ext_vector_type(16))) float f32x16;
typedef __attribute__((ext_vector_type(2))) unsigned u32x2;
typedef __attribute__((ext_vector_type(4))) unsigned u32x4;
typedef unsigned short u16;
typedef __attribute__((ext_vector_type(4), aligned(4))) unsigned u32x4_a4;

#define LDS_AS __attribute__((address_space(3)))
#define GLB_AS __attribute__((address_space(1)))

constexpr int S_LEN = 2048, DM = 576, DH = 18;
constexpr int PS_COPY = 2112;
constexpr int SG_K1 = 0, SG_K2 = 4096, SG_V = 4624, SG_BYTES = 9744;
constexpr int IMG_PIECES = 156;
constexpr int IMG_BYTES = IMG_PIECES * 1024;

constexpr size_t WS_CNT = 0;
constexpr size_t WS_FLAG = 8192;
constexpr int FL_E = 0, FL_Q = 128, FL_S = FL_Q + 16 * 128, FL_U = FL_S + 8 * 512;
constexpr size_t WS_Q = 16384;
constexpr size_t WS_IMG = WS_Q + (size_t)S_LEN * DM * 2;
constexpr size_t WS_UR = WS_IMG + IMG_BYTES;
constexpr size_t WS_PS = WS_UR + (size_t)DM * S_LEN * 2;
constexpr size_t WS_G = WS_PS + (size_t)DM * 4 * PS_COPY * 2;
constexpr size_t WS_O = WS_G + S_LEN * 4;
constexpr size_t O_BYTES = (size_t)S_LEN * DM * 2;
constexpr size_t WS_Z = WS_O + O_BYTES;
constexpr size_t Z_BYTES = (size_t)S_LEN * DM * 2;
constexpr int CNT_Z = 0, CNT_F = 18;
constexpr unsigned O_EMPTY = 0xffffffffu;

#define STAMP_DECL do {} while (0)
#define STAMP_ARG
#define STAMP_PASS
#define STAMP(i) do {} while (0)
#define CSTAMP(i) do {} while (0)
#define STAMP_FLUSH(kern) do {} while (0)
#define WAIT_LGKM0() do { asm volatile("" ::: "memory"); __builtin_amdgcn_s_waitcnt(0xC07F); asm volatile("" ::: "memory"); } while (0)
#define RAW_BARRIER() do { WAIT_LGKM0(); __builtin_amdgcn_s_barrier(); asm volatile("" ::: "memory"); } while (0)
template <int N> __device__ __forceinline__ void wait_vm() {
  asm volatile("s_waitcnt vmcnt(%0)" :: "i"(N) : "memory");
  if (N == 0) __builtin_amdgcn_s_waitcnt(0x0070);
}

__device__ __forceinline__ float bflo(unsigned w) { return __uint_as_float(w << 16); }
__device__ __forceinline__ float bfhi(unsigned w) { return __uint_as_float(w & 0xffff0000u); }
__device__ __forceinline__ unsigned pack2(float lo, float hi) {
  typedef __attribute__((ext_vector_type(2))) __bf16 bf16x2;
  bf16x2 v; v[0] = (__bf16)lo; v[1] = (__bf16)hi;
  return __builtin_bit_cast(unsigned, v);
}
__device__ __forceinline__ float rbf(float x) { return (float)(__bf16)x; }
__device__ __forceinline__ void glds16(const void* g, void* lds) {
  __builtin_amdgcn_global_load_lds((const GLB_AS unsigned*)g, (LDS_AS unsigned*)lds, 16, 0, 0);
}
__device__ __forceinline__ bf16x4 tr_read(const char* p) {
  return __builtin_amdgcn_ds_read_tr16_b64_v4bf16((LDS_AS bf16x4*)p);
}
__device__ __forceinline__ int vpos(int d) {
  const int a = d - (d > 11 && d < 16);
  return d > 18 ? 15 : (d == 11 ? 19 : a);
}
#define MFMA16(a, b, c) __builtin_amdgcn_mfma_f32_16x16x32_bf16(a, b, c, 0, 0, 0)

__device__ __forceinline__ int wsw(int k) { return ((k >> 1) & 1) | ((k >> 2) & 2); }
__device__ __forceinline__ bf16x8 a_frag(const char* sA, int lane, int rg, int s) {
  const int row = rg * 16 + (lane & 15);
  return *(const bf16x8*)(sA + row * 128 + (((s * 4 + (lane >> 4)) ^ ((row >> 1) & 7)) << 4));
}
__device__ __forceinline__ bf16x8 w_frag(const char* sW, int lane, int s, int nb) {
  const int k = s * 32 + (lane >> 4) * 8 + ((lane & 15) >> 2);
  const char* b = sW + k * 128 + ((nb ^ wsw(k)) << 5) + (lane & 3) * 8;
  const bf16x4 lo = tr_read(b), hi = tr_read(b + 512);
  return __builtin_shufflevector(lo, hi, 0, 1, 2, 3, 4, 5, 6, 7);
}

template <int NP, int NSLOT, int SLOT, int DEPTH>
__device__ __forceinline__ void ring_loader(const char* const (&base)[NP], const int (&step)[NP], char* const (&dst)[NP], int rot) {
  auto issue = [&](int j) {
    int ch = j + rot; ch = ch >= 9 ? ch - 9 : ch;
#pragma unroll
    for (int i = 0; i < NP; ++i) glds16(base[i] + ch * step[i], dst[i] + ((j + 3) % NSLOT) * SLOT);
  };
#pragma unroll
  for (int j = 0; j < DEPTH; ++j) issue(j);
#define RL_STEP(j) do { wait_vm<NP * ((8 - (j)) < (DEPTH - 1) ? (8 - (j)) : (DEPTH - 1))>(); __builtin_amdgcn_s_barrier(); \
    asm volatile("" ::: "memory"); if ((j) + DEPTH < 9) issue((j) + DEPTH); } while (0)
  RL_STEP(0); RL_STEP(1); RL_STEP(2); RL_STEP(3); RL_STEP(4); RL_STEP(5); RL_STEP(6); RL_STEP(7); RL_STEP(8);
#undef RL_STEP
}

#define K1_DEPTH 3
__device__ __forceinline__ void lds_wait(volatile LDS_AS unsigned* p, unsigned need) {
#pragma unroll 1
  for (int spin = 0; spin < (1 << 24); ++spin) {
    if (__builtin_amdgcn_readfirstlane(*p) >= need) break;
    __builtin_amdgcn_s_sleep(2);
  }
  asm volatile("" ::: "memory");
}

typedef unsigned long long u64;
__device__ __forceinline__ void flag_set(char* ws, int off, u64 tag) {
  __hip_atomic_store((GLB_AS u64*)(ws + WS_FLAG + off), tag, __ATOMIC_RELAXED, __HIP_MEMORY_SCOPE_AGENT);
}
template <int SLP>
__device__ __forceinline__ void flag_wait(char* ws, int off, u64 tag) {
  GLB_AS u64* fp = (GLB_AS u64*)(ws + WS_FLAG + off);
#pragma unroll 1
  for (int spin = 0; spin < (1 << 22); ++spin) {
    const u64 v = __hip_atomic_load(fp, __ATOMIC_RELAXED, __HIP_MEMORY_SCOPE_AGENT);
    if (__all(v == tag)) break;
    __builtin_amdgcn_s_sleep(SLP);
  }
  asm volatile("" ::: "memory");
}
__device__ __forceinline__ void code_prefetch(bool all, int lane) {
  u64 pc; unsigned d;
  asm volatile("s_getpc_b64 %0\nL_pf_%=:\n s_mov_b32 %1, k9_end-L_pf_%=" : "=s"(pc), "=s"(d));
  unsigned o[4], t[4];
#pragma unroll
  for (int i = 0; i < 4; ++i) o[i] = min((unsigned)(lane * 128 + i * 8192), all ? d - 4u : 12288u);
  asm volatile("global_load_dword %0, %4, %8\n global_load_dword %1, %5, %8\n global_load_dword %2, %6, %8\n"
               " global_load_dword %3, %7, %8\n s_waitcnt vmcnt(0)"
               : "=&v"(t[0]), "=&v"(t[1]), "=&v"(t[2]), "=&v"(t[3])
               : "v"(o[0]), "v"(o[1]), "v"(o[2]), "v"(o[3]), "s"(pc) : "memory");
}
constexpr int T_SYNC = 163840 - 64;
__device__ __forceinline__ u64 lds_tag(char* sm) { return *(volatile LDS_AS u64*)(sm + T_SYNC + 56); }

__device__ __forceinline__ void q_tile(const u16* __restrict__ x, const u16* __restrict__ wq, char* ws, char* sm,
                                       int grp, int loc, int wave, int lane STAMP_ARG) {
  const int g = lane >> 4, tl = lane & 15;
  const int mh = loc >= 9 ? 1 : 0, nt = loc - mh * 9;
  const int t0 = (grp * 2 + mh) * 128, n0 = nt * 64;
  constexpr int SLOT = 24576, NSLOT = 6;
  const __amdgpu_buffer_rsrc_t qr = __builtin_amdgcn_make_buffer_rsrc((void*)(ws + WS_Q), 0, S_LEN * DM * 2, 0x00020000);
  if (wave < 6) {
    int rot = nt + 4 * mh + grp; rot = rot >= 18 ? rot - 18 : (rot >= 9 ? rot - 9 : rot);
    const char* base[4]; int step[4]; char* dst[4];
#pragma unroll
    for (int i = 0; i < 4; ++i) {
      const int pc = wave + 6 * i;
      const bool isW = pc < 8;
      const int row = (isW ? pc : pc - 8) * 8 + (lane >> 3);
      const int sw = isW ? wsw(row) << 1 : (row >> 1) & 7;
      base[i] = (isW ? (const char*)(wq + n0) : (const char*)(x + (size_t)t0 * DM)) + (unsigned)row * (DM * 2) + (((lane & 7) ^ sw) << 4);
      step[i] = isW ? 64 * DM * 2 : 128;
      dst[i] = sm + pc * 1024;
    }
    ring_loader<4, NSLOT, SLOT, K1_DEPTH>(base, step, dst, rot);
  } else if (wave < 8) {
    RAW_BARRIER();
    if (wave == 7) {
#pragma unroll
      for (int i = 0; i < 4; ++i) {
#pragma unroll
        for (int k = 0; k < 4; ++k)
          __builtin_amdgcn_raw_buffer_store_b128(u32x4{O_EMPTY, O_EMPTY, O_EMPTY, O_EMPTY}, qr, ((t0 + (i * 4 + k) * 8 + (lane >> 3)) * DM + n0 + (lane & 7) * 8) * 2, 0, 16);
        RAW_BARRIER();
      }
      RAW_BARRIER(); RAW_BARRIER(); RAW_BARRIER();
      wait_vm<0>();
      if (lane == 0) flag_set(ws, FL_Q + (grp * 2 + mh) * 128 + nt * 8, lds_tag(sm));
      RAW_BARRIER();
    } else {
#pragma unroll
      for (int j = 1; j < 9; ++j) RAW_BARRIER();
    }
  } else {
    const int cwv = wave - 8;
    f32x4 acc[2][4];
#pragma unroll
    for (int i = 0; i < 2; ++i)
#pragma unroll
      for (int nb = 0; nb < 4; ++nb) acc[i][nb] = f32x4{0.f, 0.f, 0.f, 0.f};
    bf16x8 af[2][4], wf[2][8];
    auto rd = [&](int j, bf16x8 (&a)[4], bf16x8 (&w)[8]) {
      const char* sl = sm + (j >= 3 ? j - 3 : j + 3) * SLOT;
#pragma unroll
      for (int s = 0; s < 2; ++s) {
#pragma unroll
        for (int i = 0; i < 2; ++i) a[s * 2 + i] = a_frag(sl + 8192, lane, cwv * 2 + i, s);
#pragma unroll
        for (int nb = 0; nb < 4; ++nb) w[s * 4 + nb] = w_frag(sl, lane, s, nb);
      }
    };
    auto mm = [&](bf16x8 (&a)[4], bf16x8 (&w)[8]) {
#pragma unroll
      for (int s = 0; s < 2; ++s)
#pragma unroll
        for (int nb = 0; nb < 4; ++nb)
#pragma unroll
          for (int i = 0; i < 2; ++i) acc[i][nb] = MFMA16(w[s * 4 + nb], a[s * 2 + i], acc[i][nb]);
    };
    auto mix = [&]() {
#pragma unroll
      for (int i = 0; i < 10; ++i) { __builtin_amdgcn_sched_group_barrier(0x8, 1, 0); __builtin_amdgcn_sched_group_barrier(0x100, 2, 0); }
      __builtin_amdgcn_sched_group_barrier(0x8, 6, 0);
      __builtin_amdgcn_sched_barrier(0);
    };
    RAW_BARRIER();
    STAMP(12);
    rd(0, af[0], wf[0]);
#pragma unroll 1
    for (int j = 1; j < 9; j += 2) {
      RAW_BARRIER(); __builtin_amdgcn_sched_barrier(0); rd(j, af[1], wf[1]); mm(af[0], wf[0]); mix();
      RAW_BARRIER(); __builtin_amdgcn_sched_barrier(0); rd(j + 1, af[0], wf[0]); mm(af[1], wf[1]); mix();
    }
    mm(af[0], wf[0]);
    const float qscale = 0.23570226039551584f * 1.4426950408889634f;
    char* sE = sm + 4 * SLOT + cwv * 4608;
#pragma unroll
    for (int i = 0; i < 2; ++i)
#pragma unroll
      for (int nb = 0; nb < 4; ++nb) {
        const f32x4 a = acc[i][nb] * qscale;
        *(u32x2*)(sE + (i * 16 + tl) * 144 + nb * 32 + g * 8) =
            u32x2{min(pack2(a[0], a[1]), O_EMPTY - 1u), min(pack2(a[2], a[3]), O_EMPTY - 1u)};
      }
#pragma unroll
    for (int i = 0; i < 4; ++i) {
      const int row = i * 8 + (lane >> 3);
      const u32x4 v = *(const u32x4*)(sE + row * 144 + (lane & 7) * 16);
      __builtin_amdgcn_raw_buffer_store_b128(v, qr, ((t0 + cwv * 32 + row) * DM + n0 + (lane & 7) * 8) * 2, 0, 16);
    }
  }
}

__device__ __forceinline__ void kvg_tile(const u16* __restrict__ x, const u16* __restrict__ wk, const u16* __restrict__ wv,
                                         const u16* __restrict__ gw, char* ws, char* sm, int si, int tid, int wave, int lane) {
  const int g = lane >> 4, tl = lane & 15;
  const int t0 = si * 32;
  char* sW = sm + 36864;
  if (wave != 6 && wave != 7) {
    const int k = min(wave < 6 ? tid : tid - 128, 575);
    const u32x4 k0 = *(const u32x4_a4*)(wk + k * DH), k1 = *(const u32x4_a4*)(wk + k * DH + 8);
    const u32x4 v0 = *(const u32x4_a4*)(wv + k * DH), v1 = *(const u32x4_a4*)(wv + k * DH + 8);
    const unsigned k2 = *(const unsigned*)(wk + k * DH + 16), v2 = *(const unsigned*)(wv + k * DH + 16);
    const unsigned gk = gw[k];
    if (wave < 4) {
      const int row = wave * 8 + (lane >> 3);
      const char* xs = (const char*)x + (unsigned)(t0 + row) * (DM * 2) + (((lane & 7) ^ ((row >> 1) & 7)) << 4);
#pragma unroll
      for (int ch = 0; ch < 9; ++ch) glds16(xs + ch * 128, sm + ch * 4096 + wave * 1024);
    }
    char* d = sW + k * 128;
    const int sw = wsw(k);
    *(u32x4*)(d + (sw << 5)) = k0; *(u32x4*)(d + (sw << 5) + 16) = k1;
    *(u32x4*)(d + ((1 ^ sw) << 5)) = v0; *(u32x4*)(d + ((1 ^ sw) << 5) + 16) = v1;
    *(u32x4*)(d + ((2 ^ sw) << 5)) = u32x4{k2, gk, v2, 0u}; *(u32x4*)(d + ((2 ^ sw) << 5) + 16) = u32x4{0u, 0u, 0u, 0u};
  } else if (si == 0 && wave == 7) {
    __hip_atomic_store((GLB_AS unsigned*)(ws + WS_CNT + lane * 128), 0u, __ATOMIC_RELAXED, __HIP_MEMORY_SCOPE_AGENT);
  }
  wait_vm<0>();
  RAW_BARRIER();
  const int rg = wave & 1, kh = (wave >> 1) & 1, sub = wave >> 2;
  f32x4 acc = {0.f, 0.f, 0.f, 0.f};
  {
    bf16x8 fa[9], fw[9];
#pragma unroll
    for (int q = 0; q < 9; ++q) { fa[q] = a_frag(sm + q * 4096, lane, rg, kh); fw[q] = w_frag(sW + q * 8192, lane, kh, sub); }
    __builtin_amdgcn_sched_barrier(0);
    if (sub == 1) {
#pragma unroll
      for (int q = 0; q < 9; ++q) acc = MFMA16(fa[q], fw[q], acc);
    } else {
#pragma unroll
      for (int q = 0; q < 9; ++q) acc = MFMA16(fw[q], fa[q], acc);
    }
  }
  f32x4* ex = (f32x4*)(sm + 114688 + (sub * 2 + rg) * 1024) + lane;
  if (kh) *ex = acc;
  RAW_BARRIER();
  if (!kh) {
    acc += *ex;
    const int tw = t0 + rg * 16, t = tw + tl;
    const __amdgpu_buffer_rsrc_t ir = __builtin_amdgcn_make_buffer_rsrc((void*)(ws + WS_IMG), 0, IMG_BYTES, 0x00020000);
    const int img = (tw >> 7) * SG_BYTES, vimg = img + SG_V + ((tw >> 4) & 7) * 640;
    const int tile = (tw >> 5) & 3, r = t & 31;
    const unsigned p01 = pack2(acc[0], acc[1]), p23 = pack2(acc[2], acc[3]);
    if (sub == 0) {
      __builtin_amdgcn_raw_buffer_store_b64(u32x2{p01, p23}, ir, img + SG_K1 + tile * 1024 + (r + 32 * (g >> 1)) * 16 + (g & 1) * 8, 0, 16);
      if (lane < 5) {
        const unsigned val = (tl & 1) ? 0x3f803f80u : 0u;
        const u32x4 cv = lane < 4 ? u32x4{val, val, val, val} : u32x4{0x3f80u, 0u, 0x3f80u, 0u};
        __builtin_amdgcn_raw_buffer_store_b128(cv, ir, lane < 4 ? vimg + (tl >> 1) * 320 + ((tl & 1) ? 18 : 15) * 16 : img + SG_K2 + 512, 0, 16);
      }
    } else if (sub == 1) {
      __builtin_amdgcn_raw_buffer_store_b64(u32x2{p01, p23}, ir, vimg + (g >> 1) * 320 + (g & 1) * 8 + vpos(tl) * 16, 0, 16);
    } else if (g == 0) {
      __builtin_amdgcn_raw_buffer_store_b32(p01, ir, img + SG_K2 + r * 16 + tile * 4, 0, 16);
      const float lg = rbf(acc[2]);
      __hip_atomic_store((GLB_AS float*)(ws + WS_G) + t, rbf(__builtin_amdgcn_rcpf(1.0f + __expf(-lg))), __ATOMIC_RELAXED, __HIP_MEMORY_SCOPE_AGENT);
    } else if (g == 1) {
      const int d = vimg + (tl >> 3) * 320 + (tl & 7) * 2;
      __builtin_amdgcn_raw_buffer_store_b16((u16)p01, ir, d + 16 * 16, 0, 16);
      __builtin_amdgcn_raw_buffer_store_b16((u16)(p01 >> 16), ir, d + 17 * 16, 0, 16);
    }
    wait_vm<0>();
  }
  RAW_BARRIER();
  if (tid < 8) flag_set(ws, FL_S + tid * 512 + si * 8, lds_tag(sm));
}

__device__ __forceinline__ void u_tile(const u16* __restrict__ bk, const u16* __restrict__ bv, const u16* __restrict__ shk,
                                       char* ws, char* sm, int grp, int j, int tid, int wave, int lane) {
  constexpr int UP = 776, UB = 98304;
  const int tA = grp * 256, tB = tA + (j & 1) * 128, cB = 6 + (j >> 1);
  {
    const bool sec = wave >= 8;
    const int cg = tid & 7, st = (tid >> 3) & 63;
    const int c0 = (sec ? cB : j) * 64 + cg * 8, ts = (sec ? tB : tA) + st * 4;
    u32x4 kr[7], vr[4], sk[4];
#pragma unroll
    for (int i = 0; i < 7; ++i) kr[i] = *(const u32x4*)(bk + (size_t)max(ts - 3 + i, 0) * DM + c0);
#pragma unroll
    for (int i = 0; i < 4; ++i) vr[i] = *(const u32x4*)(bv + (size_t)(ts + i) * DM + c0);
#pragma unroll
    for (int i = 0; i < 4; ++i) sk[i] = *(const u32x4*)(shk + c0 * 4 + i * 8);
    if (ts == 0) { kr[0] = u32x4{0u, 0u, 0u, 0u}; kr[1] = kr[0]; kr[2] = kr[0]; }
#pragma unroll
    for (int c = 0; c < 8; ++c) {
      const unsigned w01 = sk[c >> 1][(c & 1) * 2], w23 = sk[c >> 1][(c & 1) * 2 + 1];
      const float w0 = bflo(w01), w1 = bfhi(w01), w2 = bflo(w23), w3 = bfhi(w23);
      float kk[7], o[4];
#pragma unroll
      for (int i = 0; i < 7; ++i) kk[i] = (c & 1) ? bfhi(kr[i][c >> 1]) : bflo(kr[i][c >> 1]);
#pragma unroll
      for (int i = 0; i < 4; ++i) {
        const float a = kk[i] * w3 + kk[i + 1] * w2 + kk[i + 2] * w1 + kk[i + 3] * w0;
        o[i] = a * ((c & 1) ? bfhi(vr[i][c >> 1]) : bflo(vr[i][c >> 1]));
      }
      *(u32x2*)(sm + UB + (cg * 8 + c) * UP + ((sec ? 380 : 252) - st * 4) * 2) = u32x2{pack2(o[3], o[2]), pack2(o[1], o[0])};
    }
  }
  RAW_BARRIER();
}
__device__ __forceinline__ void u_store(char* ws, char* sm, int grp, int j, int wave, int lane) {
  constexpr int UP = 776, UB = 98304;
  const int tA = grp * 256, tB = tA + (j & 1) * 128, cB = 6 + (j >> 1);
  const __amdgpu_buffer_rsrc_t ur = __builtin_amdgcn_make_buffer_rsrc((void*)(ws + WS_UR), 0, DM * S_LEN * 2, 0x00020000);
  u32x4 v[12];
#pragma unroll
  for (int i = 0; i < 12; ++i) {
    const int row = (wave - 8) * 16 + (i < 8 ? i * 2 + (lane >> 5) : (i - 8) * 4 + (lane >> 4));
    const char* sp = sm + UB + row * UP + (i < 8 ? (lane & 31) * 16 : 512 + (lane & 15) * 16);
    const u32x2 lo = *(const u32x2*)sp, hi = *(const u32x2*)(sp + 8);
    v[i] = u32x4{lo[0], lo[1], hi[0], hi[1]};
  }
#pragma unroll
  for (int i = 0; i < 12; ++i) {
    const int row = (wave - 8) * 16 + (i < 8 ? i * 2 + (lane >> 5) : (i - 8) * 4 + (lane >> 4));
    const int off = i < 8 ? (j * 64 + row) * S_LEN + (S_LEN - 256 - tA) + (lane & 31) * 8 : (cB * 64 + row) * S_LEN + (S_LEN - 128 - tB) + (lane & 15) * 8;
    __builtin_amdgcn_raw_buffer_store_b128(v[i], ur, off * 2, 0, 16);
  }
}
__device__ __forceinline__ void u_publish(char* ws, char* sm, int grp, int j, int lane) {
  wait_vm<0>();
  unsigned n = 0;
  if (lane == 0) n = __hip_atomic_fetch_add((LDS_AS unsigned*)(sm + T_SYNC) + 9, 1u, __ATOMIC_RELAXED, __HIP_MEMORY_SCOPE_WORKGROUP);
  if (__builtin_amdgcn_readfirstlane(n) == 3u && lane < 3)
    flag_set(ws, FL_U + (lane < 2 ? j * 128 + (grp * 2 + lane) * 8 : (6 + (j >> 1)) * 128 + (grp * 2 + (j & 1)) * 8), lds_tag(sm));
}

__device__ __forceinline__ void cnt_add(char* ws, int byte_off) {
  __hip_atomic_fetch_add((GLB_AS unsigned*)(ws + WS_CNT + byte_off), 1u, __ATOMIC_RELAXED, __HIP_MEMORY_SCOPE_AGENT);
}
template <int SLP>
__device__ __forceinline__ void cnt_wait(char* ws, int byte_off, unsigned need) {
  GLB_AS unsigned* cp = (GLB_AS unsigned*)(ws + WS_CNT + byte_off);
#pragma unroll 1
  for (int spin = 0; spin < (1 << 22); ++spin) {
    const unsigned v = __hip_atomic_load(cp, __ATOMIC_RELAXED, __HIP_MEMORY_SCOPE_AGENT);
    if (__all(v >= need)) break;
    __builtin_amdgcn_s_sleep(SLP);
  }
  asm volatile("" ::: "memory");
}

#define SSM_HI 0
#define SSM_PRIO 1
#define LATE_PRIO 0
#define PS_AUX 16
#define SSM_PACE() do {} while (0)
template <int N0>
__device__ __forceinline__ void ssm_unit(int c, const u16* __restrict__ hk, const u16* __restrict__ dk,
                                         char* ws, int lane) {
  constexpr int NM = N0 + 4;
  const int i = lane & 15, g = lane >> 4;
  const __amdgpu_buffer_rsrc_t ur = __builtin_amdgcn_make_buffer_rsrc((void*)(ws + WS_PS + (size_t)(c * 2 + (N0 ? 1 : 0)) * (PS_COPY * 4)), 0,
                                                                      PS_COPY * 4, 0x00020000);
  int ao = (((i & 8) ? 0 : PS_COPY) + 2030 - 256 * (NM - 1) - 2 * (i & 7) + 8 * g) * 2;
  const __amdgpu_buffer_rsrc_t hr = __builtin_amdgcn_make_buffer_rsrc((void*)(hk + (size_t)c * S_LEN), 0, S_LEN * 2, 0x00020000);
  int bo = (16 * i - 16 + 8 * g) * 2, bo1 = bo + 512;
  asm volatile("" : "+v"(bo1), "+v"(ao));
  f32x4 acc[4];
#pragma unroll
  for (int n = 0; n < 4; ++n) acc[n] = f32x4{0.f, 0.f, 0.f, 0.f};
  u32x4 A[NM], B[NM];
  auto ldA = [&](int m) { A[m] = __builtin_amdgcn_raw_buffer_load_b128(ur, ao, 512 * (NM - 1 - m), 0); };
  auto ldB = [&](int j) {
    if (j == 0) B[0] = __builtin_amdgcn_raw_buffer_load_b128(hr, bo, 0, 0);
    else B[j] = __builtin_amdgcn_raw_buffer_load_b128(hr, bo1, 512 * (j - 1), 0);
  };
#pragma unroll
  for (int j = 0; j < NM; ++j) ldB(j);
  const float dd = bflo(dk[c]);
  wait_vm<0>();
  const unsigned tch = __builtin_amdgcn_raw_buffer_load_b32(ur, (lane >> 5) * (PS_COPY * 2) + (N0 ? (lane & 31) * 128 : 2048 + (lane & 31) * 64), 0, 0);
#pragma unroll
  for (int m = 0; m < NM; ++m) ldA(m);
  u32x2 u[4];
#pragma unroll
  for (int n = 0; n < 4; ++n) {
    int uo = (2044 - (256 * (N0 + n) + 16 * i + 8 * (g & 1) + 4 * (g >> 1))) * 2;
    asm volatile("" : "+v"(uo));
    u[n] = __builtin_amdgcn_raw_buffer_load_b64(ur, uo, 0, 0);
  }
  asm volatile("" :: "v"(tch));
  if (N0) {
#pragma unroll 1
    for (int rho = 0; rho < 8; ++rho) {
      if (rho == SSM_HI) __builtin_amdgcn_s_setprio(SSM_PRIO);
      if (rho < 7) { ao -= 64; bo -= 64; bo1 -= 64; asm volatile("" : "+v"(ao), "+v"(bo), "+v"(bo1)); }
#pragma unroll
      for (int m = 0; m < NM; ++m) {
#pragma unroll
        for (int n = 3; n >= 0; --n)
          if (N0 + n - m >= 0) {
            acc[n] = MFMA16(__builtin_bit_cast(bf16x8, A[m]), __builtin_bit_cast(bf16x8, B[N0 + n - m]), acc[n]);
            SSM_PACE();
          }
        __builtin_amdgcn_sched_barrier(0);
        ldA(m); ldB(NM - 1 - m);
        __builtin_amdgcn_sched_barrier(0);
      }
    }
  } else {
    u32x4 A2[NM], B2[NM];
    auto round = [&](u32x4 (&Ac)[NM], u32x4 (&Bc)[NM], u32x4 (&An)[NM], u32x4 (&Bn)[NM]) {
#pragma unroll
      for (int m = 0; m < NM; ++m) {
        An[m] = __builtin_amdgcn_raw_buffer_load_b128(ur, ao, 512 * (NM - 1 - m), 0);
        Bn[m] = m == 0 ? __builtin_amdgcn_raw_buffer_load_b128(hr, bo, 0, 0) : __builtin_amdgcn_raw_buffer_load_b128(hr, bo1, 512 * (m - 1), 0);
      }
      __builtin_amdgcn_sched_barrier(0);
#pragma unroll
      for (int m = 0; m < NM; ++m)
#pragma unroll
        for (int n = 3; n >= m; --n) {
          acc[n] = MFMA16(__builtin_bit_cast(bf16x8, Ac[m]), __builtin_bit_cast(bf16x8, Bc[n - m]), acc[n]);
          SSM_PACE();
        }
      __builtin_amdgcn_sched_barrier(0);
    };
#pragma unroll 1
    for (int rho = 0; rho < 8; rho += 2) {
      if (rho == SSM_HI) __builtin_amdgcn_s_setprio(SSM_PRIO);
      ao -= 64; bo -= 64; bo1 -= 64; asm volatile("" : "+v"(ao), "+v"(bo), "+v"(bo1));
      round(A, B, A2, B2);
      if (rho < 6) { ao -= 64; bo -= 64; bo1 -= 64; asm volatile("" : "+v"(ao), "+v"(bo), "+v"(bo1)); }
      round(A2, B2, A, B);
    }
  }
  __builtin_amdgcn_s_setprio(3);
  const __amdgpu_buffer_rsrc_t zr = __builtin_amdgcn_make_buffer_rsrc((void*)(ws + WS_Z + (size_t)c * (S_LEN * 2)), 0, S_LEN * 2, 0x00020000);
#pragma unroll
  for (int n = 0; n < 4; ++n) {
    const int tq = 256 * (N0 + n) + 16 * i + 8 * (g & 1) + 4 * (g >> 1);
    const auto s02 = __builtin_amdgcn_permlane32_swap(__float_as_uint(acc[n][0]), __float_as_uint(acc[n][2]), false, false);
    const auto s13 = __builtin_amdgcn_permlane32_swap(__float_as_uint(acc[n][1]), __float_as_uint(acc[n][3]), false, false);
    const float z0 = __uint_as_float(s02[0]) + bfhi(u[n][1]) * dd;
    const float z1 = __uint_as_float(s02[1]) + bflo(u[n][1]) * dd;
    const float z2 = __uint_as_float(s13[0]) + bfhi(u[n][0]) * dd;
    const float z3 = __uint_as_float(s13[1]) + bflo(u[n][0]) * dd;
    __builtin_amdgcn_raw_buffer_store_b64(u32x2{pack2(z0, z1), pack2(z2, z3)}, zr, tq * 2, 0, 16);
  }
  wait_vm<0>();
  if (lane == 0) cnt_add(ws, (CNT_Z + (c >> 6) * 2 + (N0 ? 1 : 0)) * 128);
}

__device__ __forceinline__ f32x16 qk(bf16x8 k1, s16x4 k2, bf16x8 q1, s16x4 q2) {
  f32x16 z;
#pragma unroll
  for (int i = 0; i < 16; ++i) z[i] = 0.f;
  f32x16 s = __builtin_amdgcn_mfma_f32_32x32x16_bf16(k1, q1, z, 0, 0, 0);
  return __builtin_amdgcn_mfma_f32_32x32x8bf16_1k(k2, q2, s, 0, 0, 0);
}
__device__ __forceinline__ float max16(f32x16 s) {
  float m = fmaxf(s[0], s[1]);
#pragma unroll
  for (int i = 2; i < 16; ++i) m = fmaxf(m, s[i]);
  return m;
}
__device__ __forceinline__ s16x4 mk2(unsigned a, unsigned b) { return __builtin_bit_cast(s16x4, u32x2{a, b}); }
__device__ __forceinline__ float swap32_max(float m) {
  const auto s = __builtin_amdgcn_permlane32_swap(__float_as_uint(m), __float_as_uint(m), false, false);
  return fmaxf(__uint_as_float(s[0]), __uint_as_float(s[1]));
}

#define ATTN_CLOB "v96","v97","v98","v99","v100","v101","v102","v103","v104","v105","v106","v107","v108","v109","v110","v111", \
  "v112","v113","v114","v115","v116","v117","v118","v119","v120","v121","v122","v123","v124","v125","v126","v127", \
  "v128","v129","v130","v131","v132","v133","v134","v135","v136","v137","v138","v139","v140","v141","v142","v143"
#define EX1(a) "v_exp_f32 v" #a ", v" #a "\n"
#define EX2(a, b) EX1(a) EX1(b)
#define EX3(a, b, c) EX1(a) EX1(b) EX1(c)
#define EX4(a, b, c, d) EX1(a) EX1(b) EX1(c) EX1(d)
#define ATILE(OVHI, OK1N, K2C, QB2, K2N, QA2, OVLON, X3, X4, X6, X7) \
  EX2(96, 97) \
  "v_smfmac_f32_32x32x32_bf16 %[ob], v[140:143], v[120:127], %[idx]\n" \
  "ds_read_b128 v[140:143], %[av] offset:" OVHI "\n" \
  EX3(98, 99, 100) \
  "v_mfma_f32_32x32x8_bf16 v[112:127], " K2C ", %[" QB2 "], 0\n" \
  "s_waitcnt lgkmcnt(1)\n" \
  EX4(101, 102, 103, 104) \
  "v_mfma_f32_32x32x16_bf16 v[112:127], v[128:131], %[qb1], v[112:127]\n" \
  "ds_read_b128 v[128:131], %[ak1] offset:" OK1N "\n" \
  X3 \
  EX4(105, 106, 107, 108) \
  "v_smfmac_f32_32x32x32_bf16 %[oa], v[136:139], v[96:103], %[idx]\n" \
  X4 \
  EX3(109, 110, 111) \
  EX2(112, 113) \
  "v_smfmac_f32_32x32x32_bf16 %[oa], v[140:143], v[104:111], %[idx]\n" \
  "s_waitcnt lgkmcnt(0)\n" \
  EX3(114, 115, 116) \
  "v_mfma_f32_32x32x8_bf16 v[96:111], " K2N ", %[" QA2 "], 0\n" \
  X6 \
  EX4(117, 118, 119, 120) \
  "v_mfma_f32_32x32x16_bf16 v[96:111], v[128:131], %[qa1], v[96:111]\n" \
  X7 \
  EX4(121, 122, 123, 124) \
  "v_smfmac_f32_32x32x32_bf16 %[ob], v[136:139], v[112:119], %[idx]\n" \
  "ds_read_b128 v[136:139], %[av] offset:" OVLON "\n" \
  EX3(125, 126, 127)
#define ITSTAMP
#define ITCLOB
#define WL2 "s_waitcnt lgkmcnt(2)\n"
#define WL1 "s_waitcnt lgkmcnt(1)\n"
#define SPIN(T) "s_cbranch_scc0 L_attn_go" T "_%=\n L_attn_spin" T "_%=:\n ds_read_b32 %[tmp], %[syn]\n s_add_i32 %[chk], %[chk], 16\n s_waitcnt lgkmcnt(0)\n" \
  " v_readfirstlane_b32 %[stmp], %[tmp]\n s_cmp_gt_u32 %[chk], 0x10000000\n s_cbranch_scc1 L_attn_go" T "_%=\n s_cmp_lt_u32 %[stmp], 4\n" \
  " s_cbranch_scc1 L_attn_spin" T "_%=\n L_attn_go" T "_%=:\n"
#define ASG(O, A6, A7, B6, B7) \
  ATILE(O "+640", O "+1024", "v[132:133]", "qb2e", "v[132:133]", "qa2o", O "+1280", "", WL1, "", "") \
  ATILE(O "+1920", O "+2048", "v[132:133]", "qb2o", "v[134:135]", "qa2e", O "+2560", "", WL1, "", "") \
  ATILE(O "+3200", O "+3072", "v[134:135]", "qb2e", "v[134:135]", "qa2o", O "+3840", "", WL1, A6, A7) \
  ATILE(O "+4480", O "+9744", "v[134:135]", "qb2o", "v[132:133]", "qa2e", O "+9744", \
        "ds_read_b128 v[132:135], %[ak2] offset:" O "+9744\n", WL2, B6, B7)
#define ATTN_LOOP_ASM \
  "ds_read_b128 v[128:131], %[ak1]\n" \
  "ds_read_b128 v[132:135], %[ak2]\n" \
  "ds_read_b128 v[136:139], %[av]\n" \
  "v_mov_b32 v140, 0\n v_mov_b32 v141, 0\n v_mov_b32 v142, 0\n v_mov_b32 v143, 0\n" \
  "v_mov_b32 v120, 0\n v_mov_b32 v121, 0\n v_mov_b32 v122, 0\n v_mov_b32 v123, 0\n" \
  "v_mov_b32 v124, 0\n v_mov_b32 v125, 0\n v_mov_b32 v126, 0\n v_mov_b32 v127, 0\n" \
  "s_waitcnt lgkmcnt(1)\n" \
  "v_mfma_f32_32x32x16_bf16 v[96:111], v[128:131], %[qa1], 0\n" \
  "v_mfma_f32_32x32x8_bf16 v[96:111], v[132:133], %[qa2e], v[96:111]\n" \
  "s_nop 11\n" \
  "L_attn_loop_%=:\n" ITSTAMP \
  ASG("0", "s_cmp_eq_u32 %[it], %[chk]\n", SPIN("a"), "s_add_i32 %[it], %[it], 1\n", "") \
  ASG("9744", "s_cmp_eq_u32 %[it], %[chk]\n", SPIN("b"), "s_add_i32 %[it], %[it], 1\n", "s_cmp_lt_i32 %[it], %[nit]\n") \
  "v_add_u32 %[ak1], 19488, %[ak1]\n" \
  "v_add_u32 %[ak2], 19488, %[ak2]\n" \
  "v_add_u32 %[av], 19488, %[av]\n" \
  "s_cbranch_scc1 L_attn_loop_%=\n" \
  "v_smfmac_f32_32x32x32_bf16 %[ob], v[140:143], v[120:127], %[idx]\n" \
  "s_waitcnt lgkmcnt(0)\n" \
  "s_nop 15\n"

__device__ __forceinline__ f32x16 attn_exact(__amdgpu_buffer_rsrc_t ir, int offK1, int offK2, int offV, int hh, bf16x8 q1, unsigned q2) {
  const s16x4 x2 = mk2(hh ? 0u : q2, 0u);
  float m = -INFINITY;
#pragma unroll 1
  for (int tile = 0; tile < 64; ++tile) {
    const int base = (tile >> 2) * SG_BYTES;
    const s16x4 kk = mk2(__builtin_amdgcn_raw_buffer_load_b32(ir, base + offK2 + (hh ? 0 : (tile & 3) * 4), 0, 16), 0u);
    const bf16x8 k1 = __builtin_bit_cast(bf16x8, __builtin_amdgcn_raw_buffer_load_b128(ir, base + offK1 + (tile & 3) * 1024, 0, 16));
    m = fmaxf(m, max16(qk(k1, kk, q1, x2)));
  }
  m = swap32_max(m);
  f32x16 O;
#pragma unroll
  for (int i = 0; i < 16; ++i) O[i] = 0.f;
#pragma unroll 1
  for (int tile = 0; tile < 64; ++tile) {
    const int base = (tile >> 2) * SG_BYTES;
    const s16x4 kk = mk2(__builtin_amdgcn_raw_buffer_load_b32(ir, base + offK2 + (hh ? 0 : (tile & 3) * 4), 0, 16), 0u);
    const bf16x8 k1 = __builtin_bit_cast(bf16x8, __builtin_amdgcn_raw_buffer_load_b128(ir, base + offK1 + (tile & 3) * 1024, 0, 16));
    const bf16x8 vlo = __builtin_bit_cast(bf16x8, __builtin_amdgcn_raw_buffer_load_b128(ir, base + offV + (tile & 3) * 1280, 0, 16));
    const bf16x8 vhi = __builtin_bit_cast(bf16x8, __builtin_amdgcn_raw_buffer_load_b128(ir, base + offV + (tile & 3) * 1280 + 640, 0, 16));
    f32x16 S = qk(k1, kk, q1, x2);
#pragma unroll
    for (int i = 0; i < 16; ++i) S[i] = __builtin_amdgcn_exp2f(S[i] - m);
    O = __builtin_amdgcn_smfmac_f32_32x32x32_bf16(vlo, __builtin_bit_cast(bf16x16, __builtin_shufflevector(S, S, 0, 1, 2, 3, 4, 5, 6, 7)), O, 0xDDDDDDDD, 0, 0);
    O = __builtin_amdgcn_smfmac_f32_32x32x32_bf16(vhi, __builtin_bit_cast(bf16x16, __builtin_shufflevector(S, S, 8, 9, 10, 11, 12, 13, 14, 15)), O, 0xDDDDDDDD, 0, 0);
  }
  return O;
}

#define EARLY_POST_PRIO 2
#define OUT_AUX 16
#define Q_SLEEP 4
#define KV_DELAY 0
#define O_SLEEP 4
#define NLATE 4
constexpr int T_QX = 16384;
constexpr int T_WO = 58624;
constexpr int T_XCH = T_WO + 73728;
constexpr int T_Z = 0;
constexpr int T_PART = 10240;
constexpr int T_EPI = 30720;
static_assert(NLATE % 2 == 0 && T_WO >= NLATE * SG_BYTES && T_XCH + 24576 <= T_SYNC && T_EPI + 12800 <= T_WO, "lds");
constexpr int P_EARLY = NLATE * SG_BYTES / 1024;
constexpr int NG_HI = (153 - (P_EARLY + 20) + 3) / 4;
static_assert(P_EARLY + 20 + NG_HI * 4 <= IMG_PIECES && 12 + (32 - NG_HI) * 4 >= P_EARLY && (P_EARLY + 20) * 1024 >= (NLATE + 2) * SG_BYTES, "dma plan");
static_assert(NG_HI >= 16 && T_QX >= 12288 && T_QX + 12288 <= P_EARLY * 1024, "q hand-over area");

__device__ __forceinline__ void img_group(const char* src, char* sm, int p) {
  const GLB_AS unsigned* g = (const GLB_AS unsigned*)(src + p * 1024);
  LDS_AS unsigned* l = (LDS_AS unsigned*)(sm + p * 1024);
  __builtin_amdgcn_global_load_lds(g, l, 16, 0, 16);
  __builtin_amdgcn_global_load_lds(g, l, 16, 1024, 16);
  __builtin_amdgcn_global_load_lds(g, l, 16, 2048, 16);
  __builtin_amdgcn_global_load_lds(g, l, 16, 3072, 16);
}

extern "C" __global__ void __launch_bounds__(768) k_main(const u16* __restrict__ x, const u16* __restrict__ wq,
                                               const u16* __restrict__ wk, const u16* __restrict__ wv,
                                               const u16* __restrict__ gw, const u16* __restrict__ bk,
                                               const u16* __restrict__ bv, const u16* __restrict__ shk, char* ws,
                                               const u16* __restrict__ hk, const u16* __restrict__ dk,
                                               const u16* __restrict__ wo, u16* __restrict__ out) {
  __shared__ __attribute__((aligned(256))) char sm[163840];
  const int tid = threadIdx.x, lane = tid & 63;
  const int wave = __builtin_amdgcn_readfirstlane(tid >> 6);
  const int b = blockIdx.x;
  const int grp = b & 7, loc = b >> 3;
  volatile LDS_AS unsigned* sync = (volatile LDS_AS unsigned*)(sm + T_SYNC);
  STAMP_DECL;
  STAMP(0);
  if (wave == 6 && loc < 26) code_prefetch(loc >= 18, lane);
  if (wave == 7) {
    const u64 e = __hip_atomic_load((GLB_AS u64*)(ws + WS_FLAG + FL_E), __ATOMIC_RELAXED, __HIP_MEMORY_SCOPE_AGENT) + 1u;
    if (lane < 16) sync[lane] = lane < 14 ? 0u : (lane == 14 ? (unsigned)e : (unsigned)(e >> 32));
  }
  if (loc < 18) q_tile(x, wq, ws, sm, grp, loc, wave, lane STAMP_PASS);
  else if (loc < 26) kvg_tile(x, wk, wv, gw, ws, sm, grp * 8 + loc - 18, tid, wave, lane);
  else { __builtin_amdgcn_s_sleep(KV_DELAY); u_tile(bk, bv, shk, ws, sm, grp, loc - 26, tid, wave, lane); }
  STAMP(13);

  const bool has_tile = loc < (grp < 2 ? 30 : 29);
  const int qid = has_tile ? grp * 29 + (grp < 2 ? grp : 2) + loc : 0;
  const int mt = qid / 9, nt = qid - mt * 9;
  const int t0 = mt * 80, n0 = nt * 64;
  const __amdgpu_buffer_rsrc_t orsrc = __builtin_amdgcn_make_buffer_rsrc((void*)(ws + WS_O), 0, (int)O_BYTES, 0x00020000);
  const u64 tag = lds_tag(sm);
  const int qb = ((grp + 4) & 7) * 32 + loc;
  const int cwi = wave - 2;
  const int kh = cwi >= 5 ? 1 : 0, cw = cwi - kh * 5;
  const int g = lane >> 4, tl = lane & 15;
  u32x2 xv[2]; float gt;
  auto load_xg = [&]() {
    const int te = min(t0 + max(cw, 0) * 16 + tl, S_LEN - 1);
#pragma unroll
    for (int h = 0; h < 2; ++h) xv[h] = *(const u32x2*)(x + (size_t)te * DM + n0 + kh * 32 + h * 16 + g * 4);
    gt = __uint_as_float(__builtin_amdgcn_raw_buffer_load_b32(__builtin_amdgcn_make_buffer_rsrc((void*)(ws + WS_G), 0, S_LEN * 4, 0x00020000), te * 4, 0, 16));
    asm volatile("" ::: "memory");
  };

  if (wave < 4) {
    __builtin_amdgcn_s_setprio(3);
    const int W = b < 208 ? b + 208 * wave : 4 * b + wave;
    if (wave == 0) { flag_wait<2>(ws, FL_S + grp * 512 + (lane < 8 ? lane : (NLATE - 1) * 4 + (lane & 15)) * 8, tag); if (lane == 0) sync[7] = 1u; }
    else lds_wait(sync + 7, 1u);
    STAMP(14);
    const char* src = ws + WS_IMG + lane * 16;
    img_group(src, sm, P_EARLY + wave * 4);
    img_group(src, sm, wave == 0 ? P_EARLY + 16 : wave * 4 - 4);
    if (wave == 0) flag_wait<2>(ws, FL_S + grp * 512 + lane * 8, tag);
    wait_vm<0>();
    RAW_BARRIER();
#pragma unroll
    for (int i = 0; i < 8; ++i) {
      if (i == 4) lds_wait(sync + 10, 4u);
      int p = P_EARLY + 20 + (wave + 4 * i) * 4;
      p = p > 152 ? p - (P_EARLY + 20 + NG_HI * 4) + 12 : p;
      img_group(src, sm, p);
    }
    wait_vm<0>();
    if (lane == 0) __hip_atomic_fetch_add((LDS_AS unsigned*)sync + 5, 1u, __ATOMIC_RELAXED, __HIP_MEMORY_SCOPE_WORKGROUP);
    {
      const int c1 = W < 576 ? W : W - 576, c2 = (W >= 576 && W < 576 + 128) ? W - 128 : c1;
      flag_wait<8>(ws, FL_U + (((lane & 16) ? c2 : c1) >> 6) * 128 + (lane & 15) * 8, tag);
      const __amdgpu_buffer_rsrc_t us = __builtin_amdgcn_make_buffer_rsrc((void*)(ws + WS_UR), 0, DM * S_LEN * 2, 0x00020000);
      const __amdgpu_buffer_rsrc_t up = __builtin_amdgcn_make_buffer_rsrc((void*)(ws + WS_PS), 0, (int)(WS_G - WS_PS), 0x00020000);
      u32x4 ud[4]; unsigned un[4]; int upo[4];
#pragma unroll
      for (int q = 0; q < 4; ++q) {
        const int cc = q < 2 ? c1 : c2, blk = W < 576 ? q : 2 + (q & 1);
        const int uo = cc * (S_LEN * 2) + blk * 1024 + lane * 16;
        upo[q] = (cc * 2 + (W < 576 ? 1 : 0)) * (PS_COPY * 4) + blk * 1024 + lane * 16;
        ud[q] = __builtin_amdgcn_raw_buffer_load_b128(us, uo, 0, 16);
        un[q] = __builtin_amdgcn_raw_buffer_load_b32(us, uo + 16, 0, 16);
        if (blk == 3 && lane == 63) un[q] = 0u;
      }
#pragma unroll
      for (int q = 0; q < 4; ++q) {
        const u32x4 sh = {__builtin_amdgcn_alignbit(ud[q][1], ud[q][0], 16), __builtin_amdgcn_alignbit(ud[q][2], ud[q][1], 16),
                          __builtin_amdgcn_alignbit(ud[q][3], ud[q][2], 16), __builtin_amdgcn_alignbit(un[q], ud[q][3], 16)};
        __builtin_amdgcn_raw_buffer_store_b128(ud[q], up, upo[q], 0, PS_AUX);
        __builtin_amdgcn_raw_buffer_store_b128(sh, up, upo[q] + PS_COPY * 2, 0, PS_AUX);
        if ((q & 1) && lane < 4 && (q == 3 || W >= 576))
          __builtin_amdgcn_raw_buffer_store_b128(u32x4{0u, 0u, 0u, 0u}, up, upo[q] + 1024 + (lane >> 1) * (PS_COPY * 2 - 32), 0, PS_AUX);
      }
    }
    STAMP(1);
    if (wave == 3) {
#pragma unroll
      for (int i = 0; i < 9; ++i)
        __builtin_amdgcn_raw_buffer_store_b128(u32x4{O_EMPTY, O_EMPTY, O_EMPTY, O_EMPTY}, orsrc,
                                               ((qb >> 1) * 72 + i * 8 + (lane >> 3)) * 256 + (qb & 1) * 128 + (lane & 7) * 16, 0, 16);
      wait_vm<0>();
      if (lane == 0) { cnt_add(ws, (CNT_F + qb / 10) * 128); sync[6] = 1u; }
    }
    STAMP(9);
    if (W < 576) {
      ssm_unit<4>(W, hk, dk, ws, lane);
    } else {
      for (int c = W - 576; c < 576; c += 448) ssm_unit<0>(c, hk, dk, ws, lane);
    }
    if (b == 0 && wave == 1) {
      cnt_wait<8>(ws, (CNT_F + min(lane, 25)) * 128, lane >= 25 ? 6u : 10u);
      if (lane == 0) flag_set(ws, FL_E, tag + 1u);
    }
    load_xg();
    wait_vm<0>();
    STAMP(2);
  } else {
    const int pair = wave & 3;
    const bool early = wave < 8;
    __builtin_amdgcn_s_setprio(2);
    const int r = lane & 31, hh = lane >> 5;
    const int tq = qb * 8 + pair * 2;
    const int offK1 = SG_K1 + lane * 16;
    const int offK2 = SG_K2 + (hh ? 512 : r * 16);
    const int offV = SG_V + hh * 320 + vpos(r) * 16;
    const int idx = 0xDDDDDDDD;
    const unsigned ldsBase = (unsigned)(uintptr_t)(LDS_AS char*)sm + (early ? NLATE * SG_BYTES : 0);

    const __amdgpu_buffer_rsrc_t irsrc = __builtin_amdgcn_make_buffer_rsrc((void*)(ws + WS_IMG), 0, IMG_BYTES, 0x00020000);
    bf16x8 qa1, qb1; unsigned qa2, qb2;
    f32x16 Oa, Ob;
    {
#pragma unroll
      for (int i = 0; i < 16; ++i) { Oa[i] = 0.f; Ob[i] = 0.f; }
      unsigned aK1 = ldsBase + offK1, aK2 = ldsBase + offK2, aV = ldsBase + offV;
      asm volatile("" : "+v"(Oa), "+v"(Ob), "+v"(aK1), "+v"(aK2), "+v"(aV) :: "memory");
      const __amdgpu_buffer_rsrc_t qr = __builtin_amdgcn_make_buffer_rsrc((void*)(ws + WS_Q + (size_t)tq * (DM * 2)), 0, DM * 4, 0x00020000);
      auto load_q = [&]() {
#pragma unroll 1
        for (int spin = 0; spin < (1 << 22); ++spin) {
          asm volatile("" ::: "memory");
          const u32x4 ta = __builtin_amdgcn_raw_buffer_load_b128(qr, r * 36 + hh * 16, 0, 16);
          const u32x4 tb = __builtin_amdgcn_raw_buffer_load_b128(qr, r * 36 + hh * 16 + DM * 2, 0, 16);
          qa2 = __builtin_amdgcn_raw_buffer_load_b32(qr, r * 36 + 32, 0, 16);
          qb2 = __builtin_amdgcn_raw_buffer_load_b32(qr, r * 36 + 32 + DM * 2, 0, 16);
          qa1 = __builtin_bit_cast(bf16x8, ta); qb1 = __builtin_bit_cast(bf16x8, tb);
          const unsigned mq = max(max(max(max(ta[0], ta[1]), max(ta[2], ta[3])), max(max(tb[0], tb[1]), max(tb[2], tb[3]))), max(qa2, qb2));
          if (__all(mq != O_EMPTY)) break;
          __builtin_amdgcn_s_sleep(Q_SLEEP);
        }
      };
      u32x4* qx = (u32x4*)(sm + T_QX + pair * 3072) + lane;
      unsigned pm;
      if (early) {
        if (wave == 7) { flag_wait<4>(ws, lane < 9 ? FL_Q + (qb >> 4) * 128 + lane * 8 : FL_S + grp * 512, tag); if (lane == 0) sync[8] = 1u; }
        else lds_wait(sync + 8, 1u);
        const bf16x8 k1 = __builtin_bit_cast(bf16x8, __builtin_amdgcn_raw_buffer_load_b128(irsrc, offK1, 0, 16));
        const unsigned k2 = hh ? 0u : __builtin_amdgcn_raw_buffer_load_b32(irsrc, offK2, 0, 16);
        STAMP(14);
        load_q();
        const float ma = swap32_max(max16(qk(k1, mk2(k2, 0u), qa1, mk2(hh ? 0u : qa2, 0u))));
        const float mb = swap32_max(max16(qk(k1, mk2(k2, 0u), qb1, mk2(hh ? 0u : qb2, 0u))));
        pm = pack2(-ma, -mb);
        qx[0] = __builtin_bit_cast(u32x4, qa1); qx[64] = __builtin_bit_cast(u32x4, qb1); qx[128] = u32x4{qa2, qb2, pm, 0u};
        STAMP(9);
        RAW_BARRIER();
      } else {
        __builtin_amdgcn_s_setprio(3);
        if (loc >= 26) { u_store(ws, sm, grp, loc - 26, wave, lane); u_publish(ws, sm, grp, loc - 26, lane); }
        RAW_BARRIER();
        qa1 = __builtin_bit_cast(bf16x8, qx[0]); qb1 = __builtin_bit_cast(bf16x8, qx[64]);
        const u32x4 e = qx[128];
        qa2 = e[0]; qb2 = e[1]; pm = e[2];
        WAIT_LGKM0();
        if (lane == 0) __hip_atomic_fetch_add((LDS_AS unsigned*)sync + 10, 1u, __ATOMIC_RELAXED, __HIP_MEMORY_SCOPE_WORKGROUP);
        __builtin_amdgcn_s_setprio(LATE_PRIO);
      }
      STAMP(1); CSTAMP(10);
      const unsigned xa = hh ? 0u : qa2, xb = hh ? 0u : qb2;
      const unsigned nma = pm & 0xffffu, nmb = pm >> 16;
      const s16x4 qa2e = mk2(hh ? nma : qa2, 0u), qa2o = mk2(hh ? nma : 0u, xa);
      const s16x4 qb2e = mk2(hh ? nmb : qb2, 0u), qb2o = mk2(hh ? nmb : 0u, xb);
      int it = 0;
      const int nit = early ? 16 - NLATE : NLATE;
      int chk = 2 - (wave >> 2), stmp;
      unsigned tmp;
      const unsigned syn = (unsigned)(uintptr_t)(sync + 5);
      asm volatile(ATTN_LOOP_ASM
                   : [oa] "+v"(Oa), [ob] "+v"(Ob), [ak1] "+v"(aK1), [ak2] "+v"(aK2), [av] "+v"(aV), [it] "+s"(it),
                     [chk] "+s"(chk), [stmp] "=&s"(stmp), [tmp] "=&v"(tmp)
                   : [qa1] "v"(qa1), [qb1] "v"(qb1), [qa2e] "v"(qa2e), [qa2o] "v"(qa2o), [qb2e] "v"(qb2e), [qb2o] "v"(qb2o),
                     [idx] "v"(idx), [nit] "s"(nit), [syn] "v"(syn)
                   : "memory", "scc", ATTN_CLOB ITCLOB);
    }
    STAMP(2); CSTAMP(11);
    load_xg();
    f32x4* xch = (f32x4*)(sm + T_XCH + pair * 6144) + lane;
    if (early) {
      __builtin_amdgcn_s_setprio(EARLY_POST_PRIO);
      if (lane == 0) __hip_atomic_fetch_add((LDS_AS unsigned*)sync, 1u, __ATOMIC_RELAXED, __HIP_MEMORY_SCOPE_WORKGROUP);
      lds_wait(sync, 4u);
      lds_wait(sync + 6, 1u);
#pragma unroll
      for (int i = 0; i < 3; ++i) {
        xch[i * 64] = f32x4{Oa[4 * i], Oa[4 * i + 1], Oa[4 * i + 2], Oa[4 * i + 3]};
        xch[(3 + i) * 64] = f32x4{Ob[4 * i], Ob[4 * i + 1], Ob[4 * i + 2], Ob[4 * i + 3]};
      }
      WAIT_LGKM0();
      if (lane == 0) sync[1 + pair] = 1u;
      const char* wsrc = (const char*)(wo + n0) + (size_t)(pair * 144 + (lane >> 3)) * (DM * 2);
      const int c0 = ((lane & 7) ^ (wsw(lane >> 3) << 1)) << 4;
#pragma unroll
      for (int i = 0; i < 18; ++i) glds16(wsrc + i * 8 * DM * 2 + (c0 ^ ((i & 1) << 6)), sm + T_WO + pair * 18432 + i * 1024);
      if (wave == 4) cnt_wait<8>(ws, (CNT_F + mt) * 128, mt == 25 ? 6u : 10u);
      wait_vm<0>();
    } else {
      __builtin_amdgcn_s_setprio(3);
      lds_wait(sync + 1 + pair, 1u);
      float big = 0.f;
#pragma unroll
      for (int i = 0; i < 3; ++i) {
        const f32x4 pa = xch[i * 64], pb = xch[(3 + i) * 64];
#pragma unroll
        for (int e = 0; e < 4; ++e) if (4 * i + e < 11) {
          Oa[4 * i + e] += pa[e]; Ob[4 * i + e] += pb[e];
          big += fabsf(Oa[4 * i + e]) + fabsf(Ob[4 * i + e]);
        }
      }
      if (__any(!(big < 1e30f) || (!hh && !(fminf(Oa[10], Ob[10]) > 1e-12f)))) {
        Oa = attn_exact(irsrc, offK1, offK2, offV, hh, qa1, qa2);
        Ob = attn_exact(irsrc, offK1, offK2, offV, hh, qb1, qb2);
      }
      STAMP(3);
      const auto la = __builtin_amdgcn_permlane32_swap(__float_as_uint(Oa[10]), __float_as_uint(Oa[10]), false, false);
      const auto lb = __builtin_amdgcn_permlane32_swap(__float_as_uint(Ob[10]), __float_as_uint(Ob[10]), false, false);
      const float ia = __builtin_amdgcn_rcpf(__uint_as_float(la[0])), ib = __builtin_amdgcn_rcpf(__uint_as_float(lb[0]));
      char* ost = sm + T_XCH + pair * 6144;
      char* od = ost + r * 36 + hh * 8;
      *(unsigned*)(od) = pack2(Oa[0] * ia, Oa[1] * ia);        *(unsigned*)(od + 1168) = pack2(Ob[0] * ib, Ob[1] * ib);
      *(unsigned*)(od + 4) = pack2(Oa[2] * ia, Oa[3] * ia);    *(unsigned*)(od + 1172) = pack2(Ob[2] * ib, Ob[3] * ib);
      *(unsigned*)(od + 16) = pack2(Oa[4] * ia, Oa[5] * ia);   *(unsigned*)(od + 1184) = pack2(Ob[4] * ib, Ob[5] * ib);
      *(unsigned*)(od + 20) = pack2(Oa[6] * ia, Oa[7] * ia);   *(unsigned*)(od + 1188) = pack2(Ob[6] * ib, Ob[7] * ib);
      if (!hh) { *(unsigned*)(od + 32) = pack2(Oa[8] * ia, Oa[9] * ia); *(unsigned*)(od + 1200) = pack2(Ob[8] * ib, Ob[9] * ib); }
      u32x4 ov[3];
#pragma unroll
      for (int i = 0; i < 3; ++i) ov[i] = *(const u32x4*)(ost + (lane & 1) * 1168 + (i * 32 + (lane >> 1)) * 16);
#pragma unroll
      for (int i = 0; i < 3; ++i) {
#pragma unroll
        for (int e = 0; e < 4; ++e) ov[i][e] = min(ov[i][e], O_EMPTY - 1u);
        if (i < 2 || lane < 16)
          __builtin_amdgcn_raw_buffer_store_b128(ov[i], orsrc, (((tq >> 4) * 72 + i * 32 + (lane >> 1)) * 16 + (tq & 15) + (lane & 1)) * 16, 0, 16);
      }
    }
  }
  STAMP(4);
  if (!has_tile) { STAMP_FLUSH(1); return; }

  RAW_BARRIER();
  STAMP(5);
  if (wave < 2) {
    cnt_wait<2>(ws, (CNT_Z + nt * 2 + (((lane & 1) ? t0 : t0 + 79) < 1024 ? 0 : 1)) * 128, 64u);
    const __amdgpu_buffer_rsrc_t zrsrc = __builtin_amdgcn_make_buffer_rsrc((void*)(ws + WS_Z), 0, (int)Z_BYTES, 0x00020000);
    u32x4 zv[5];
#pragma unroll
    for (int i = 0; i < 5; ++i) {
      const int ci = (wave * 5 + i) * 64 + lane, c = ci / 10, k = ci - c * 10;
      zv[i] = __builtin_amdgcn_raw_buffer_load_b128(zrsrc, ((n0 + c) * S_LEN + t0 + k * 8) * 2, 0, 16);
    }
#pragma unroll
    for (int i = 0; i < 5; ++i) *(u32x4*)(sm + T_Z + ((wave * 5 + i) * 64 + lane) * 16) = zv[i];
    WAIT_LGKM0();
  } else {
    float bx[2][4];
    const float omg = 1.0f - gt;
#pragma unroll
    for (int h = 0; h < 2; ++h) {
      bx[h][0] = omg * bflo(xv[h][0]); bx[h][1] = omg * bfhi(xv[h][0]);
      bx[h][2] = omg * bflo(xv[h][1]); bx[h][3] = omg * bfhi(xv[h][1]);
    }
    const int rgp = mt * 5 + cw;
    const int obase = ((rgp * 18 + kh * 9) * 64 + lane) * 16;
    const char* wl = sm + T_WO + kh * 9 * 4096;
    constexpr int NPRE = 5;
    bf16x8 wpre[NPRE][4];
    int so[9];
#pragma unroll
    for (int s = 0; s < 9; ++s) so[s] = s + nt >= 9 ? s + nt - 9 : s + nt;
#pragma unroll
    for (int s = 0; s < NPRE; ++s)
#pragma unroll
      for (int nb = 0; nb < 4; ++nb) wpre[s][nb] = w_frag(wl + so[9 - NPRE + s] * 4096, lane, 0, nb);
    unsigned wq4[4];
#pragma unroll
    for (int nb = 0; nb < 4; ++nb) {
      const int k = g * 8 + (tl >> 2);
      wq4[nb] = (unsigned)(uintptr_t)(LDS_AS char*)sm + T_WO + kh * 9 * 4096 + k * 128 + ((nb ^ wsw(k)) << 5) + (lane & 3) * 8;
    }
    STAMP(6);
    f32x4 acc[4];
#pragma unroll 1
    for (int spin = 0; spin < (1 << 20); ++spin) {
      asm volatile("" : "+v"(wq4[0]), "+v"(wq4[1]), "+v"(wq4[2]), "+v"(wq4[3]) :: "memory");
      u32x4 of[9];
#pragma unroll
      for (int s = 0; s < 9; ++s) of[s] = __builtin_amdgcn_raw_buffer_load_b128(orsrc, obase, so[s] * 1024, 16);
      __builtin_amdgcn_sched_barrier(0);
#pragma unroll
      for (int nb = 0; nb < 4; ++nb) acc[nb] = f32x4{0.f, 0.f, 0.f, 0.f};
      unsigned mx = 0;
#pragma unroll
      for (int s = 0; s < 9; ++s) {
#pragma unroll
        for (int nb = 0; nb < 4; ++nb) {
          bf16x8 wf;
          if (s >= 9 - NPRE) wf = wpre[s - (9 - NPRE)][nb];
          else {
            LDS_AS bf16x4* wp = (LDS_AS bf16x4*)(uintptr_t)(wq4[nb] + so[s] * 4096);
            wf = __builtin_shufflevector(__builtin_amdgcn_ds_read_tr16_b64_v4bf16(wp), __builtin_amdgcn_ds_read_tr16_b64_v4bf16(wp + 64), 0, 1, 2, 3, 4, 5, 6, 7);
          }
          acc[nb] = MFMA16(wf, __builtin_bit_cast(bf16x8, of[s]), acc[nb]);
        }
        mx = max(max(mx, max(of[s][0], of[s][1])), max(of[s][2], of[s][3]));
      }
      if (__all(mx != O_EMPTY)) break;
      __builtin_amdgcn_s_sleep(O_SLEEP);
    }
    f32x4* pd = (f32x4*)(sm + T_PART + cwi * 2048) + lane;
    pd[0] = kh ? acc[0] : acc[2];
    pd[64] = kh ? acc[1] : acc[3];
    STAMP(7);
    RAW_BARRIER();
    const f32x4* ps = (const f32x4*)(sm + T_PART + (kh ? cw : cw + 5) * 2048) + lane;
    char* sE = sm + T_EPI + cwi * 1280;
    f32x4 pa[2]; u32x2 z[2];
#pragma unroll
    for (int h = 0; h < 2; ++h) {
      pa[h] = ps[h * 64];
      z[h] = __builtin_bit_cast(u32x2, tr_read(sm + T_Z + ((kh * 2 + h) * 16 + g * 4 + (tl >> 2)) * 160 + (cw * 16 + (tl & 3) * 4) * 2));
    }
    asm volatile("" ::: "memory");
#pragma unroll
    for (int h = 0; h < 2; ++h) {
      const f32x4 a = (kh ? acc[2 + h] : acc[h]) + pa[h];
      *(u32x2*)(sE + tl * 80 + h * 32 + g * 8) =
          u32x2{pack2(gt * a[0] + bx[h][0] * bflo(z[h][0]), gt * a[1] + bx[h][1] * bfhi(z[h][0])),
                pack2(gt * a[2] + bx[h][2] * bflo(z[h][1]), gt * a[3] + bx[h][3] * bfhi(z[h][1]))};
    }
    const u32x4 o = *(const u32x4*)(sE + (lane >> 2) * 80 + (lane & 3) * 16);
    const __amdgpu_buffer_rsrc_t outr = __builtin_amdgcn_make_buffer_rsrc((void*)out, 0, S_LEN * DM * 2, 0x00020000);
    __builtin_amdgcn_raw_buffer_store_b128(o, outr, ((t0 + cw * 16 + (lane >> 2)) * DM + n0 + kh * 32 + (lane & 3) * 8) * 2, 0, OUT_AUX);
  }
  STAMP(8);
  STAMP_FLUSH(1);
}

extern "C" __global__ void k9_end() {}

extern "C" void kernel_launch(void* const* d_in, const int* in_sizes, int n_in, void* d_out, int out_size,
                              void* d_ws, size_t ws_size, hipStream_t stream) {
  const u16* x = (const u16*)d_in[0];
  const u16* wq = (const u16*)d_in[1];
  const u16* wk = (const u16*)d_in[2];
  const u16* wv = (const u16*)d_in[3];
  const u16* wo = (const u16*)d_in[4];
  const u16* bk = (const u16*)d_in[6];
  const u16* bv = (const u16*)d_in[7];
  const u16* shk = (const u16*)d_in[8];
  const u16* hk = (const u16*)d_in[9];
  const u16* dk = (const u16*)d_in[10];
  const u16* gw = (const u16*)d_in[11];
  char* ws = (char*)d_ws;
  k_main<<<256, 768, 0, stream>>>(x, wq, wk, wv, gw, bk, bv, shk, ws, hk, dk, wo, (u16*)d_out);
}
```

```cpp
#include <hip/hip_runtime.h>
#include <hip/hip_bf16.h>
#include <stdint.h>

typedef __attribute__((ext_vector_type(4))) __bf16 bf16x4;
typedef __attribute__((ext_vector_type(8))) __bf16 bf16x8;
typedef __attribute__((ext_vector_type(16))) __bf16 bf16x16;
typedef __attribute__((ext_vector_type(4))) short s16x4;
typedef __attribute__((ext_vector_type(4))) float f32x4;
typedef __attribute__((ext_vector_type(16))) float f32x16;
typedef __attribute__((ext_vector_type(2))) unsigned u32x2;
typedef __attribute__((ext_vector_type(4))) unsigned u32x4;
typedef unsigned short u16;
typedef __attribute__((ext_vector_type(4), aligned(4))) unsigned u32x4_a4;

#define LDS_AS __attribute__((address_space(3)))
#define GLB_AS __attribute__((address_space(1)))

constexpr int S_LEN = 2048, DM = 576, DH = 18;
constexpr int PS_COPY = 2112;
constexpr int SG_K1 = 0, SG_K2 = 4096, SG_V = 4624, SG_BYTES = 9744;
constexpr int IMG_PIECES = 156;
constexpr int IMG_BYTES = IMG_PIECES * 1024;

constexpr size_t WS_CNT = 0;
constexpr size_t WS_FLAG = 8192;
constexpr int FL_E = 0, FL_Q = 128, FL_S = FL_Q + 16 * 128, FL_M = FL_S + 8 * 512, FL_U = FL_M + 8 * 512;
constexpr size_t WS_Q = 24576;
constexpr size_t WS_IMG = WS_Q + (size_t)S_LEN * DM * 2;
constexpr size_t WS_UR = WS_IMG + IMG_BYTES;
constexpr size_t WS_PS = WS_UR + (size_t)DM * S_LEN * 2;
constexpr size_t WS_G = WS_PS + (size_t)DM * 4 * PS_COPY * 2;
constexpr size_t WS_O = WS_G + S_LEN * 4;
constexpr size_t O_BYTES = (size_t)S_LEN * DM * 2;
constexpr size_t WS_Z = WS_O + O_BYTES;
constexpr size_t Z_BYTES = (size_t)S_LEN * DM * 2;
constexpr int CNT_Z = 0, CNT_F = 18;
constexpr unsigned O_EMPTY = 0xffffffffu;

#define STAMP_DECL do {} while (0)
#define STAMP_ARG
#define STAMP_PASS
#define STAMP(i) do {} while (0)
#define CSTAMP(i) do {} while (0)
#define STAMP_FLUSH(kern) do {} while (0)
#define WAIT_LGKM0() do { asm volatile("" ::: "memory"); __builtin_amdgcn_s_waitcnt(0xC07F); asm volatile("" ::: "memory"); } while (0)
#define RAW_BARRIER() do { WAIT_LGKM0(); __builtin_amdgcn_s_barrier(); asm volatile("" ::: "memory"); } while (0)
template <int N> __device__ __forceinline__ void wait_vm() {
  asm volatile("s_waitcnt vmcnt(%0)" :: "i"(N) : "memory");
  if (N == 0) __builtin_amdgcn_s_waitcnt(0x0070);
}

__device__ __forceinline__ float bflo(unsigned w) { return __uint_as_float(w << 16); }
__device__ __forceinline__ float bfhi(unsigned w) { return __uint_as_float(w & 0xffff0000u); }
__device__ __forceinline__ unsigned pack2(float lo, float hi) {
  typedef __attribute__((ext_vector_type(2))) __bf16 bf16x2;
  bf16x2 v; v[0] = (__bf16)lo; v[1] = (__bf16)hi;
  return __builtin_bit_cast(unsigned, v);
}
__device__ __forceinline__ float rbf(float x) { return (float)(__bf16)x; }
__device__ __forceinline__ void glds16(const void* g, void* lds) {
  __builtin_amdgcn_global_load_lds((const GLB_AS unsigned*)g, (LDS_AS unsigned*)lds, 16, 0, 0);
}
__device__ __forceinline__ bf16x4 tr_read(const char* p) {
  return __builtin_amdgcn_ds_read_tr16_b64_v4bf16((LDS_AS bf16x4*)p);
}
__device__ __forceinline__ int vpos(int d) {
  const int a = d - (d > 11 && d < 16);
  return d > 18 ? 15 : (d == 11 ? 19 : a);
}
#define MFMA16(a, b, c) __builtin_amdgcn_mfma_f32_16x16x32_bf16(a, b, c, 0, 0, 0)

__device__ __forceinline__ int wsw(int k) { return ((k >> 1) & 1) | ((k >> 2) & 2); }
__device__ __forceinline__ bf16x8 a_frag(const char* sA, int lane, int rg, int s) {
  const int row = rg * 16 + (lane & 15);
  return *(const bf16x8*)(sA + row * 128 + (((s * 4 + (lane >> 4)) ^ ((row >> 1) & 7)) << 4));
}
__device__ __forceinline__ bf16x8 w_frag(const char* sW, int lane, int s, int nb) {
  const int k = s * 32 + (lane >> 4) * 8 + ((lane & 15) >> 2);
  const char* b = sW + k * 128 + ((nb ^ wsw(k)) << 5) + (lane & 3) * 8;
  const bf16x4 lo = tr_read(b), hi = tr_read(b + 512);
  return __builtin_shufflevector(lo, hi, 0, 1, 2, 3, 4, 5, 6, 7);
}

template <int NP, int NSLOT, int SLOT, int DEPTH>
__device__ __forceinline__ void ring_loader(const char* const (&base)[NP], const int (&step)[NP], char* const (&dst)[NP], int rot) {
  auto issue = [&](int j) {
    int ch = j + rot; ch = ch >= 9 ? ch - 9 : ch;
#pragma unroll
    for (int i = 0; i < NP; ++i) glds16(base[i] + ch * step[i], dst[i] + ((j + 3) % NSLOT) * SLOT);
  };
#pragma unroll
  for (int j = 0; j < DEPTH; ++j) issue(j);
#define RL_STEP(j) do { wait_vm<NP * ((8 - (j)) < (DEPTH - 1) ? (8 - (j)) : (DEPTH - 1))>(); __builtin_amdgcn_s_barrier(); \
    asm volatile("" ::: "memory"); if ((j) + DEPTH < 9) issue((j) + DEPTH); } while (0)
  RL_STEP(0); RL_STEP(1); RL_STEP(2); RL_STEP(3); RL_STEP(4); RL_STEP(5); RL_STEP(6); RL_STEP(7); RL_STEP(8);
#undef RL_STEP
}

#define K1_DEPTH 3
__device__ __forceinline__ void lds_wait(volatile LDS_AS unsigned* p, unsigned need) {
#pragma unroll 1
  for (int spin = 0; spin < (1 << 24); ++spin) {
    if (__builtin_amdgcn_readfirstlane(*p) >= need) break;
    __builtin_amdgcn_s_sleep(2);
  }
  asm volatile("" ::: "memory");
}

typedef unsigned long long u64;
__device__ __forceinline__ void flag_set(char* ws, int off, u64 tag) {
  __hip_atomic_store((GLB_AS u64*)(ws + WS_FLAG + off), tag, __ATOMIC_RELAXED, __HIP_MEMORY_SCOPE_AGENT);
}
template <int SLP>
__device__ __forceinline__ void flag_wait(char* ws, int off, u64 tag) {
  GLB_AS u64* fp = (GLB_AS u64*)(ws + WS_FLAG + off);
#pragma unroll 1
  for (int spin = 0; spin < (1 << 22); ++spin) {
    const u64 v = __hip_atomic_load(fp, __ATOMIC_RELAXED, __HIP_MEMORY_SCOPE_AGENT);
    if (__all(v == tag)) break;
    __builtin_amdgcn_s_sleep(SLP);
  }
  asm volatile("" ::: "memory");
}
__device__ __forceinline__ void code_prefetch(bool all, int lane) {
  u64 pc; unsigned d;
  asm volatile("s_getpc_b64 %0\nL_pf_%=:\n s_mov_b32 %1, k9_end-L_pf_%=" : "=s"(pc), "=s"(d));
  unsigned o[4], t[4];
#pragma unroll
  for (int i = 0; i < 4; ++i) o[i] = min((unsigned)(lane * 128 + i * 8192), all ? d - 4u : 12288u);
  asm volatile("global_load_dword %0, %4, %8\n global_load_dword %1, %5, %8\n global_load_dword %2, %6, %8\n"
               " global_load_dword %3, %7, %8\n s_waitcnt vmcnt(0)"
               : "=&v"(t[0]), "=&v"(t[1]), "=&v"(t[2]), "=&v"(t[3])
               : "v"(o[0]), "v"(o[1]), "v"(o[2]), "v"(o[3]), "s"(pc) : "memory");
}
constexpr int T_SYNC = 163840 - 64;
__device__ __forceinline__ u64 lds_tag(char* sm) { return *(volatile LDS_AS u64*)(sm + T_SYNC + 56); }

__device__ __forceinline__ void q_tile(const u16* __restrict__ x, const u16* __restrict__ wq, char* ws, char* sm,
                                       int grp, int loc, int wave, int lane STAMP_ARG) {
  const int g = lane >> 4, tl = lane & 15;
  const int mh = loc >= 9 ? 1 : 0, nt = loc - mh * 9;
  const int t0 = (grp * 2 + mh) * 128, n0 = nt * 64;
  constexpr int SLOT = 24576, NSLOT = 6;
  const __amdgpu_buffer_rsrc_t qr = __builtin_amdgcn_make_buffer_rsrc((void*)(ws + WS_Q), 0, S_LEN * DM * 2, 0x00020000);
  if (wave < 6) {
    int rot = nt + 4 * mh + grp; rot = rot >= 18 ? rot - 18 : (rot >= 9 ? rot - 9 : rot);
    const char* base[4]; int step[4]; char* dst[4];
#pragma unroll
    for (int i = 0; i < 4; ++i) {
      const int pc = wave + 6 * i;
      const bool isW = pc < 8;
      const int row = (isW ? pc : pc - 8) * 8 + (lane >> 3);
      const int sw = isW ? wsw(row) << 1 : (row >> 1) & 7;
      base[i] = (isW ? (const char*)(wq + n0) : (const char*)(x + (size_t)t0 * DM)) + (unsigned)row * (DM * 2) + (((lane & 7) ^ sw) << 4);
      step[i] = isW ? 64 * DM * 2 : 128;
      dst[i] = sm + pc * 1024;
    }
    ring_loader<4, NSLOT, SLOT, K1_DEPTH>(base, step, dst, rot);
  } else if (wave < 8) {
    RAW_BARRIER();
    if (wave == 7) {
#pragma unroll
      for (int i = 0; i < 4; ++i) {
#pragma unroll
        for (int k = 0; k < 4; ++k)
          __builtin_amdgcn_raw_buffer_store_b128(u32x4{O_EMPTY, O_EMPTY, O_EMPTY, O_EMPTY}, qr, ((t0 + (i * 4 + k) * 8 + (lane >> 3)) * DM + n0 + (lane & 7) * 8) * 2, 0, 16);
        RAW_BARRIER();
      }
      RAW_BARRIER(); RAW_BARRIER(); RAW_BARRIER();
      wait_vm<0>();
      if (lane == 0) flag_set(ws, FL_Q + (grp * 2 + mh) * 128 + nt * 8, lds_tag(sm));
      RAW_BARRIER();
    } else {
#pragma unroll
      for (int j = 1; j < 9; ++j) RAW_BARRIER();
    }
  } else {
    const int cwv = wave - 8;
    __builtin_amdgcn_s_setprio(3);
    f32x4 acc[2][4];
#pragma unroll
    for (int i = 0; i < 2; ++i)
#pragma unroll
      for (int nb = 0; nb < 4; ++nb) acc[i][nb] = f32x4{0.f, 0.f, 0.f, 0.f};
    bf16x8 af[2][4], wf[2][8];
    auto rd = [&](int j, bf16x8 (&a)[4], bf16x8 (&w)[8]) {
      const char* sl = sm + (j >= 3 ? j - 3 : j + 3) * SLOT;
#pragma unroll
      for (int s = 0; s < 2; ++s) {
#pragma unroll
        for (int i = 0; i < 2; ++i) a[s * 2 + i] = a_frag(sl + 8192, lane, cwv * 2 + i, s);
#pragma unroll
        for (int nb = 0; nb < 4; ++nb) w[s * 4 + nb] = w_frag(sl, lane, s, nb);
      }
    };
    auto mm = [&](bf16x8 (&a)[4], bf16x8 (&w)[8]) {
#pragma unroll
      for (int s = 0; s < 2; ++s)
#pragma unroll
        for (int nb = 0; nb < 4; ++nb)
#pragma unroll
          for (int i = 0; i < 2; ++i) acc[i][nb] = MFMA16(w[s * 4 + nb], a[s * 2 + i], acc[i][nb]);
    };
    auto mix = [&]() {
#pragma unroll
      for (int i = 0; i < 10; ++i) { __builtin_amdgcn_sched_group_barrier(0x8, 1, 0); __builtin_amdgcn_sched_group_barrier(0x100, 2, 0); }
      __builtin_amdgcn_sched_group_barrier(0x8, 6, 0);
      __builtin_amdgcn_sched_barrier(0);
    };
    RAW_BARRIER();
    STAMP(12);
    rd(0, af[0], wf[0]);
#pragma unroll 1
    for (int j = 1; j < 9; j += 2) {
      RAW_BARRIER(); __builtin_amdgcn_sched_barrier(0); rd(j, af[1], wf[1]); mm(af[0], wf[0]); mix();
      RAW_BARRIER(); __builtin_amdgcn_sched_barrier(0); rd(j + 1, af[0], wf[0]); mm(af[1], wf[1]); mix();
    }
    mm(af[0], wf[0]);
    const float qscale = 0.23570226039551584f * 1.4426950408889634f;
    char* sE = sm + 4 * SLOT + cwv * 4608;
#pragma unroll
    for (int i = 0; i < 2; ++i)
#pragma unroll
      for (int nb = 0; nb < 4; ++nb) {
        const f32x4 a = acc[i][nb] * qscale;
        *(u32x2*)(sE + (i * 16 + tl) * 144 + nb * 32 + g * 8) =
            u32x2{min(pack2(a[0], a[1]), O_EMPTY - 1u), min(pack2(a[2], a[3]), O_EMPTY - 1u)};
      }
#pragma unroll
    for (int i = 0; i < 4; ++i) {
      const int row = i * 8 + (lane >> 3);
      const u32x4 v = *(const u32x4*)(sE + row * 144 + (lane & 7) * 16);
      __builtin_amdgcn_raw_buffer_store_b128(v, qr, ((t0 + cwv * 32 + row) * DM + n0 + (lane & 7) * 8) * 2, 0, 16);
    }
  }
}

__device__ __forceinline__ void kvg_tile(const u16* __restrict__ x, const u16* __restrict__ wk, const u16* __restrict__ wv,
                                         const u16* __restrict__ gw, char* ws, char* sm, int si, int tid, int wave, int lane) {
  const int g = lane >> 4, tl = lane & 15;
  const int t0 = si * 32;
  const __amdgpu_buffer_rsrc_t ir = __builtin_amdgcn_make_buffer_rsrc((void*)(ws + WS_IMG), 0, IMG_BYTES, 0x00020000);
  char* sW = sm + 36864;
  if (wave != 6 && wave != 7) {
    const int k = min(wave < 6 ? tid : tid - 128, 575);
    const u32x4 k0 = *(const u32x4_a4*)(wk + k * DH), k1 = *(const u32x4_a4*)(wk + k * DH + 8);
    const u32x4 v0 = *(const u32x4_a4*)(wv + k * DH), v1 = *(const u32x4_a4*)(wv + k * DH + 8);
    const unsigned k2 = *(const unsigned*)(wk + k * DH + 16), v2 = *(const unsigned*)(wv + k * DH + 16);
    const unsigned gk = gw[k];
    if (wave < 4) {
      const int row = wave * 8 + (lane >> 3);
      const char* xs = (const char*)x + (unsigned)(t0 + row) * (DM * 2) + (((lane & 7) ^ ((row >> 1) & 7)) << 4);
#pragma unroll
      for (int ch = 0; ch < 9; ++ch) glds16(xs + ch * 128, sm + ch * 4096 + wave * 1024);
    }
    char* d = sW + k * 128;
    const int sw = wsw(k);
    *(u32x4*)(d + (sw << 5)) = k0; *(u32x4*)(d + (sw << 5) + 16) = k1;
    *(u32x4*)(d + ((1 ^ sw) << 5)) = v0; *(u32x4*)(d + ((1 ^ sw) << 5) + 16) = v1;
    *(u32x4*)(d + ((2 ^ sw) << 5)) = u32x4{k2, gk, v2, 0u}; *(u32x4*)(d + ((2 ^ sw) << 5) + 16) = u32x4{0u, 0u, 0u, 0u};
  } else if (wave == 7) {
    const int img = (si >> 2) * SG_BYTES, tile = si & 3;
    __builtin_amdgcn_raw_buffer_store_b128(u32x4{O_EMPTY, O_EMPTY, O_EMPTY, O_EMPTY}, ir, img + SG_K1 + tile * 1024 + lane * 16, 0, 16);
#pragma unroll
    for (int i = 0; i < 2; ++i) {
      const int ch = i * 64 + lane, pos = ch % 20;
      const unsigned val = pos == 15 ? 0u : (pos == 18 ? 0x3f803f80u : O_EMPTY);
      if (ch < 80) __builtin_amdgcn_raw_buffer_store_b128(u32x4{val, val, val, val}, ir, img + SG_V + tile * 1280 + ch * 16, 0, 16);
    }
    if (lane < 33) __builtin_amdgcn_raw_buffer_store_b32(lane < 32 ? O_EMPTY : ((tile & 1) ? 0u : 0x3f80u), ir, img + SG_K2 + lane * 16 + tile * 4, 0, 16);
    if (si == 0) __hip_atomic_store((GLB_AS unsigned*)(ws + WS_CNT + lane * 128), 0u, __ATOMIC_RELAXED, __HIP_MEMORY_SCOPE_AGENT);
    wait_vm<0>();
    if (lane < 8) flag_set(ws, FL_M + lane * 512 + si * 8, lds_tag(sm));
  }
  if (wave != 7) wait_vm<0>();
  RAW_BARRIER();
  const int rg = wave & 1, kh = (wave >> 1) & 1, sub = wave >> 2;
  f32x4 acc = {0.f, 0.f, 0.f, 0.f};
  {
    bf16x8 fa[9], fw[9];
#pragma unroll
    for (int q = 0; q < 9; ++q) { fa[q] = a_frag(sm + q * 4096, lane, rg, kh); fw[q] = w_frag(sW + q * 8192, lane, kh, sub); }
    __builtin_amdgcn_sched_barrier(0);
    if (sub == 1) {
#pragma unroll
      for (int q = 0; q < 9; ++q) acc = MFMA16(fa[q], fw[q], acc);
    } else {
#pragma unroll
      for (int q = 0; q < 9; ++q) acc = MFMA16(fw[q], fa[q], acc);
    }
  }
  f32x4* ex = (f32x4*)(sm + 114688 + (sub * 2 + rg) * 1024) + lane;
  if (kh) *ex = acc;
  RAW_BARRIER();
  if (!kh) {
    acc += *ex;
    const int tw = t0 + rg * 16, t = tw + tl;
    const int img = (tw >> 7) * SG_BYTES, vimg = img + SG_V + ((tw >> 4) & 7) * 640;
    const int tile = (tw >> 5) & 3, r = t & 31;
    const unsigned p01 = min(pack2(acc[0], acc[1]), O_EMPTY - 1u), p23 = min(pack2(acc[2], acc[3]), O_EMPTY - 1u);
    if (sub == 0) {
      __builtin_amdgcn_raw_buffer_store_b64(u32x2{p01, p23}, ir, img + SG_K1 + tile * 1024 + (r + 32 * (g >> 1)) * 16 + (g & 1) * 8, 0, 16);
    } else if (sub == 1) {
      __builtin_amdgcn_raw_buffer_store_b64(u32x2{p01, p23}, ir, vimg + (g >> 1) * 320 + (g & 1) * 8 + vpos(tl) * 16, 0, 16);
    } else {
      const unsigned nb = __shfl_xor(p01, 1);
      const unsigned pv = min((tl & 1) ? (nb >> 16) | (p01 & 0xffff0000u) : (p01 & 0xffffu) | (nb << 16), O_EMPTY - 1u);
      if (g < 2)
        __builtin_amdgcn_raw_buffer_store_b32(g ? pv : p01, ir,
            g ? vimg + (tl >> 3) * 320 + (16 + (tl & 1)) * 16 + (tl & 6) * 2 : img + SG_K2 + r * 16 + tile * 4, 0, 16);
      if (g == 0) {
        const float lg = rbf(acc[2]);
        __hip_atomic_store((GLB_AS float*)(ws + WS_G) + t, rbf(__builtin_amdgcn_rcpf(1.0f + __expf(-lg))), __ATOMIC_RELAXED, __HIP_MEMORY_SCOPE_AGENT);
      }
    }
    wait_vm<0>();
  }
  RAW_BARRIER();
  if (tid < 8) flag_set(ws, FL_S + tid * 512 + si * 8, lds_tag(sm));
}

__device__ __forceinline__ void u_tile(const u16* __restrict__ bk, const u16* __restrict__ bv, const u16* __restrict__ shk,
                                       char* ws, char* sm, int grp, int j, int tid, int wave, int lane) {
  constexpr int UP = 776, UB = 98304;
  const int tA = grp * 256, tB = tA + (j & 1) * 128, cB = 6 + (j >> 1);
  {
    const bool sec = wave >= 8;
    const int cg = tid & 7, st = (tid >> 3) & 63;
    const int c0 = (sec ? cB : j) * 64 + cg * 8, ts = (sec ? tB : tA) + st * 4;
    u32x4 kr[7], vr[4], sk[4];
#pragma unroll
    for (int i = 0; i < 7; ++i) kr[i] = *(const u32x4*)(bk + (size_t)max(ts - 3 + i, 0) * DM + c0);
#pragma unroll
    for (int i = 0; i < 4; ++i) vr[i] = *(const u32x4*)(bv + (size_t)(ts + i) * DM + c0);
#pragma unroll
    for (int i = 0; i < 4; ++i) sk[i] = *(const u32x4*)(shk + c0 * 4 + i * 8);
    if (ts == 0) { kr[0] = u32x4{0u, 0u, 0u, 0u}; kr[1] = kr[0]; kr[2] = kr[0]; }
#pragma unroll
    for (int c = 0; c < 8; ++c) {
      const unsigned w01 = sk[c >> 1][(c & 1) * 2], w23 = sk[c >> 1][(c & 1) * 2 + 1];
      const float w0 = bflo(w01), w1 = bfhi(w01), w2 = bflo(w23), w3 = bfhi(w23);
      float kk[7], o[4];
#pragma unroll
      for (int i = 0; i < 7; ++i) kk[i] = (c & 1) ? bfhi(kr[i][c >> 1]) : bflo(kr[i][c >> 1]);
#pragma unroll
      for (int i = 0; i < 4; ++i) {
        const float a = kk[i] * w3 + kk[i + 1] * w2 + kk[i + 2] * w1 + kk[i + 3] * w0;
        o[i] = a * ((c & 1) ? bfhi(vr[i][c >> 1]) : bflo(vr[i][c >> 1]));
      }
      *(u32x2*)(sm + UB + (cg * 8 + c) * UP + ((sec ? 380 : 252) - st * 4) * 2) = u32x2{pack2(o[3], o[2]), pack2(o[1], o[0])};
    }
  }
  RAW_BARRIER();
}
__device__ __forceinline__ void u_store(char* ws, char* sm, int grp, int j, int wave, int lane) {
  constexpr int UP = 776, UB = 98304;
  const int tA = grp * 256, tB = tA + (j & 1) * 128, cB = 6 + (j >> 1);
  const __amdgpu_buffer_rsrc_t ur = __builtin_amdgcn_make_buffer_rsrc((void*)(ws + WS_UR), 0, DM * S_LEN * 2, 0x00020000);
  u32x4 v[12];
#pragma unroll
  for (int i = 0; i < 12; ++i) {
    const int row = (wave - 8) * 16 + (i < 8 ? i * 2 + (lane >> 5) : (i - 8) * 4 + (lane >> 4));
    const char* sp = sm + UB + row * UP + (i < 8 ? (lane & 31) * 16 : 512 + (lane & 15) * 16);
    const u32x2 lo = *(const u32x2*)sp, hi = *(const u32x2*)(sp + 8);
    v[i] = u32x4{lo[0], lo[1], hi[0], hi[1]};
  }
#pragma unroll
  for (int i = 0; i < 12; ++i) {
    const int row = (wave - 8) * 16 + (i < 8 ? i * 2 + (lane >> 5) : (i - 8) * 4 + (lane >> 4));
    const int off = i < 8 ? (j * 64 + row) * S_LEN + (S_LEN - 256 - tA) + (lane & 31) * 8 : (cB * 64 + row) * S_LEN + (S_LEN - 128 - tB) + (lane & 15) * 8;
    __builtin_amdgcn_raw_buffer_store_b128(v[i], ur, off * 2, 0, 16);
  }
}
__device__ __forceinline__ void u_publish(char* ws, char* sm, int grp, int j, int lane) {
  wait_vm<0>();
  unsigned n = 0;
  if (lane == 0) n = __hip_atomic_fetch_add((LDS_AS unsigned*)(sm + T_SYNC) + 9, 1u, __ATOMIC_RELAXED, __HIP_MEMORY_SCOPE_WORKGROUP);
  if (__builtin_amdgcn_readfirstlane(n) == 3u && lane < 3)
    flag_set(ws, FL_U + (lane < 2 ? j * 128 + (grp * 2 + lane) * 8 : (6 + (j >> 1)) * 128 + (grp * 2 + (j & 1)) * 8), lds_tag(sm));
}

__device__ __forceinline__ void cnt_add(char* ws, int byte_off) {
  __hip_atomic_fetch_add((GLB_AS unsigned*)(ws + WS_CNT + byte_off), 1u, __ATOMIC_RELAXED, __HIP_MEMORY_SCOPE_AGENT);
}
template <int SLP>
__device__ __forceinline__ void cnt_wait(char* ws, int byte_off, unsigned need) {
  GLB_AS unsigned* cp = (GLB_AS unsigned*)(ws + WS_CNT + byte_off);
#pragma unroll 1
  for (int spin = 0; spin < (1 << 22); ++spin) {
    const unsigned v = __hip_atomic_load(cp, __ATOMIC_RELAXED, __HIP_MEMORY_SCOPE_AGENT);
    if (__all(v >= need)) break;
    __builtin_amdgcn_s_sleep(SLP);
  }
  asm volatile("" ::: "memory");
}

#define SSM_HI 0
#define SSM_PRIO 1
#define LATE_PRIO 0
#define PS_AUX 16
#define SSM_PACE() do {} while (0)
template <int N0>
__device__ __forceinline__ void ssm_unit(int c, const u16* __restrict__ hk, const u16* __restrict__ dk,
                                         char* ws, int lane) {
  constexpr int NM = N0 + 4;
  const int i = lane & 15, g = lane >> 4;
  const __amdgpu_buffer_rsrc_t ur = __builtin_amdgcn_make_buffer_rsrc((void*)(ws + WS_PS + (size_t)(c * 2 + (N0 ? 1 : 0)) * (PS_COPY * 4)), 0,
                                                                      PS_COPY * 4, 0x00020000);
  int ao = (((i & 8) ? 0 : PS_COPY) + 2030 - 256 * (NM - 1) - 2 * (i & 7) + 8 * g) * 2;
  const __amdgpu_buffer_rsrc_t hr = __builtin_amdgcn_make_buffer_rsrc((void*)(hk + (size_t)c * S_LEN), 0, S_LEN * 2, 0x00020000);
  int bo = (16 * i - 16 + 8 * g) * 2, bo1 = bo + 512;
  asm volatile("" : "+v"(bo1), "+v"(ao));
  f32x4 acc[4];
#pragma unroll
  for (int n = 0; n < 4; ++n) acc[n] = f32x4{0.f, 0.f, 0.f, 0.f};
  u32x4 A[NM], B[NM];
  auto ldA = [&](int m) { A[m] = __builtin_amdgcn_raw_buffer_load_b128(ur, ao, 512 * (NM - 1 - m), 0); };
  auto ldB = [&](int j) {
    if (j == 0) B[0] = __builtin_amdgcn_raw_buffer_load_b128(hr, bo, 0, 0);
    else B[j] = __builtin_amdgcn_raw_buffer_load_b128(hr, bo1, 512 * (j - 1), 0);
  };
#pragma unroll
  for (int j = 0; j < NM; ++j) ldB(j);
  const float dd = bflo(dk[c]);
  wait_vm<0>();
  const unsigned tch = __builtin_amdgcn_raw_buffer_load_b32(ur, (lane >> 5) * (PS_COPY * 2) + (N0 ? (lane & 31) * 128 : 2048 + (lane & 31) * 64), 0, 0);
#pragma unroll
  for (int m = 0; m < NM; ++m) ldA(m);
  u32x2 u[4];
#pragma unroll
  for (int n = 0; n < 4; ++n) {
    int uo = (2044 - (256 * (N0 + n) + 16 * i + 8 * (g & 1) + 4 * (g >> 1))) * 2;
    asm volatile("" : "+v"(uo));
    u[n] = __builtin_amdgcn_raw_buffer_load_b64(ur, uo, 0, 0);
  }
  asm volatile("" :: "v"(tch));
  if (N0) {
#pragma unroll 1
    for (int rho = 0; rho < 8; ++rho) {
      if (rho == SSM_HI) __builtin_amdgcn_s_setprio(SSM_PRIO);
      if (rho < 7) { ao -= 64; bo -= 64; bo1 -= 64; asm volatile("" : "+v"(ao), "+v"(bo), "+v"(bo1)); }
#pragma unroll
      for (int m = 0; m < NM; ++m) {
#pragma unroll
        for (int n = 3; n >= 0; --n)
          if (N0 + n - m >= 0) {
            acc[n] = MFMA16(__builtin_bit_cast(bf16x8, A[m]), __builtin_bit_cast(bf16x8, B[N0 + n - m]), acc[n]);
            SSM_PACE();
          }
        __builtin_amdgcn_sched_barrier(0);
        ldA(m); ldB(NM - 1 - m);
        __builtin_amdgcn_sched_barrier(0);
      }
    }
  } else {
    u32x4 A2[NM], B2[NM];
    auto round = [&](u32x4 (&Ac)[NM], u32x4 (&Bc)[NM], u32x4 (&An)[NM], u32x4 (&Bn)[NM]) {
#pragma unroll
      for (int m = 0; m < NM; ++m) {
        An[m] = __builtin_amdgcn_raw_buffer_load_b128(ur, ao, 512 * (NM - 1 - m), 0);
        Bn[m] = m == 0 ? __builtin_amdgcn_raw_buffer_load_b128(hr, bo, 0, 0) : __builtin_amdgcn_raw_buffer_load_b128(hr, bo1, 512 * (m - 1), 0);
      }
      __builtin_amdgcn_sched_barrier(0);
#pragma unroll
      for (int m = 0; m < NM; ++m)
#pragma unroll
        for (int n = 3; n >= m; --n) {
          acc[n] = MFMA16(__builtin_bit_cast(bf16x8, Ac[m]), __builtin_bit_cast(bf16x8, Bc[n - m]), acc[n]);
          SSM_PACE();
        }
      __builtin_amdgcn_sched_barrier(0);
    };
#pragma unroll 1
    for (int rho = 0; rho < 8; rho += 2) {
      if (rho == SSM_HI) __builtin_amdgcn_s_setprio(SSM_PRIO);
      ao -= 64; bo -= 64; bo1 -= 64; asm volatile("" : "+v"(ao), "+v"(bo), "+v"(bo1));
      round(A, B, A2, B2);
      if (rho < 6) { ao -= 64; bo -= 64; bo1 -= 64; asm volatile("" : "+v"(ao), "+v"(bo), "+v"(bo1)); }
      round(A2, B2, A, B);
    }
  }
  __builtin_amdgcn_s_setprio(3);
  const __amdgpu_buffer_rsrc_t zr = __builtin_amdgcn_make_buffer_rsrc((void*)(ws + WS_Z + (size_t)c * (S_LEN * 2)), 0, S_LEN * 2, 0x00020000);
#pragma unroll
  for (int n = 0; n < 4; ++n) {
    const int tq = 256 * (N0 + n) + 16 * i + 8 * (g & 1) + 4 * (g >> 1);
    const auto s02 = __builtin_amdgcn_permlane32_swap(__float_as_uint(acc[n][0]), __float_as_uint(acc[n][2]), false, false);
    const auto s13 = __builtin_amdgcn_permlane32_swap(__float_as_uint(acc[n][1]), __float_as_uint(acc[n][3]), false, false);
    const float z0 = __uint_as_float(s02[0]) + bfhi(u[n][1]) * dd;
    const float z1 = __uint_as_float(s02[1]) + bflo(u[n][1]) * dd;
    const float z2 = __uint_as_float(s13[0]) + bfhi(u[n][0]) * dd;
    const float z3 = __uint_as_float(s13[1]) + bflo(u[n][0]) * dd;
    __builtin_amdgcn_raw_buffer_store_b64(u32x2{pack2(z0, z1), pack2(z2, z3)}, zr, tq * 2, 0, 16);
  }
  wait_vm<0>();
  if (lane == 0) cnt_add(ws, (CNT_Z + (c >> 6) * 2 + (N0 ? 1 : 0)) * 128);
}

__device__ __forceinline__ f32x16 qk(bf16x8 k1, s16x4 k2, bf16x8 q1, s16x4 q2) {
  f32x16 z;
#pragma unroll
  for (int i = 0; i < 16; ++i) z[i] = 0.f;
  f32x16 s = __builtin_amdgcn_mfma_f32_32x32x16_bf16(k1, q1, z, 0, 0, 0);
  return __builtin_amdgcn_mfma_f32_32x32x8bf16_1k(k2, q2, s, 0, 0, 0);
}
__device__ __forceinline__ float max16(f32x16 s) {
  float m = fmaxf(s[0], s[1]);
#pragma unroll
  for (int i = 2; i < 16; ++i) m = fmaxf(m, s[i]);
  return m;
}
__device__ __forceinline__ s16x4 mk2(unsigned a, unsigned b) { return __builtin_bit_cast(s16x4, u32x2{a, b}); }
__device__ __forceinline__ float swap32_max(float m) {
  const auto s = __builtin_amdgcn_permlane32_swap(__float_as_uint(m), __float_as_uint(m), false, false);
  return fmaxf(__uint_as_float(s[0]), __uint_as_float(s[1]));
}

#define ATTN_CLOB "v96","v97","v98","v99","v100","v101","v102","v103","v104","v105","v106","v107","v108","v109","v110","v111", \
  "v112","v113","v114","v115","v116","v117","v118","v119","v120","v121","v122","v123","v124","v125","v126","v127", \
  "v128","v129","v130","v131","v132","v133","v134","v135","v136","v137","v138","v139","v140","v141","v142","v143"
#define EX1(a) "v_exp_f32 v" #a ", v" #a "\n"
#define EX2(a, b) EX1(a) EX1(b)
#define EX3(a, b, c) EX1(a) EX1(b) EX1(c)
#define EX4(a, b, c, d) EX1(a) EX1(b) EX1(c) EX1(d)
#define ATILE(OVHI, OK1N, K2C, QB2, K2N, QA2, OVLON, X3, X4, X6, X7) \
  EX2(96, 97) \
  "v_smfmac_f32_32x32x32_bf16 %[ob], v[140:143], v[120:127], %[idx]\n" \
  "ds_read_b128 v[140:143], %[av] offset:" OVHI "\n" \
  EX3(98, 99, 100) \
  "v_mfma_f32_32x32x8_bf16 v[112:127], " K2C ", %[" QB2 "], 0\n" \
  "s_waitcnt lgkmcnt(1)\n" \
  EX4(101, 102, 103, 104) \
  "v_mfma_f32_32x32x16_bf16 v[112:127], v[128:131], %[qb1], v[112:127]\n" \
  "ds_read_b128 v[128:131], %[ak1] offset:" OK1N "\n" \
  X3 \
  EX4(105, 106, 107, 108) \
  "v_smfmac_f32_32x32x32_bf16 %[oa], v[136:139], v[96:103], %[idx]\n" \
  X4 \
  EX3(109, 110, 111) \
  EX2(112, 113) \
  "v_smfmac_f32_32x32x32_bf16 %[oa], v[140:143], v[104:111], %[idx]\n" \
  "s_waitcnt lgkmcnt(0)\n" \
  EX3(114, 115, 116) \
  "v_mfma_f32_32x32x8_bf16 v[96:111], " K2N ", %[" QA2 "], 0\n" \
  X6 \
  EX4(117, 118, 119, 120) \
  "v_mfma_f32_32x32x16_bf16 v[96:111], v[128:131], %[qa1], v[96:111]\n" \
  X7 \
  EX4(121, 122, 123, 124) \
  "v_smfmac_f32_32x32x32_bf16 %[ob], v[136:139], v[112:119], %[idx]\n" \
  "ds_read_b128 v[136:139], %[av] offset:" OVLON "\n" \
  EX3(125, 126, 127)
#define ITSTAMP
#define ITCLOB
#define WL2 "s_waitcnt lgkmcnt(2)\n"
#define WL1 "s_waitcnt lgkmcnt(1)\n"
#define SPIN(T) "s_cbranch_scc0 L_attn_go" T "_%=\n L_attn_spin" T "_%=:\n ds_read_b32 %[tmp], %[syn]\n s_add_i32 %[chk], %[chk], 16\n s_waitcnt lgkmcnt(0)\n" \
  " v_readfirstlane_b32 %[stmp], %[tmp]\n s_cmp_gt_u32 %[chk], 0x10000000\n s_cbranch_scc1 L_attn_go" T "_%=\n s_cmp_lt_u32 %[stmp], 4\n" \
  " s_cbranch_scc1 L_attn_spin" T "_%=\n L_attn_go" T "_%=:\n"
#define ASG(O, A6, A7, B6, B7) \
  ATILE(O "+640", O "+1024", "v[132:133]", "qb2e", "v[132:133]", "qa2o", O "+1280", "", WL1, "", "") \
  ATILE(O "+1920", O "+2048", "v[132:133]", "qb2o", "v[134:135]", "qa2e", O "+2560", "", WL1, "", "") \
  ATILE(O "+3200", O "+3072", "v[134:135]", "qb2e", "v[134:135]", "qa2o", O "+3840", "", WL1, A6, A7) \
  ATILE(O "+4480", O "+9744", "v[134:135]", "qb2o", "v[132:133]", "qa2e", O "+9744", \
        "ds_read_b128 v[132:135], %[ak2] offset:" O "+9744\n", WL2, B6, B7)
#define ATTN_LOOP_ASM \
  "ds_read_b128 v[128:131], %[ak1]\n" \
  "ds_read_b128 v[132:135], %[ak2]\n" \
  "ds_read_b128 v[136:139], %[av]\n" \
  "v_mov_b32 v140, 0\n v_mov_b32 v141, 0\n v_mov_b32 v142, 0\n v_mov_b32 v143, 0\n" \
  "v_mov_b32 v120, 0\n v_mov_b32 v121, 0\n v_mov_b32 v122, 0\n v_mov_b32 v123, 0\n" \
  "v_mov_b32 v124, 0\n v_mov_b32 v125, 0\n v_mov_b32 v126, 0\n v_mov_b32 v127, 0\n" \
  "s_waitcnt lgkmcnt(1)\n" \
  "v_mfma_f32_32x32x16_bf16 v[96:111], v[128:131], %[qa1], 0\n" \
  "v_mfma_f32_32x32x8_bf16 v[96:111], v[132:133], %[qa2e], v[96:111]\n" \
  "s_nop 11\n" \
  "L_attn_loop_%=:\n" ITSTAMP \
  ASG("0", "s_cmp_eq_u32 %[it], %[chk]\n", SPIN("a"), "s_add_i32 %[it], %[it], 1\n", "") \
  ASG("9744", "s_cmp_eq_u32 %[it], %[chk]\n", SPIN("b"), "s_add_i32 %[it], %[it], 1\n", "s_cmp_lt_i32 %[it], %[nit]\n") \
  "v_add_u32 %[ak1], 19488, %[ak1]\n" \
  "v_add_u32 %[ak2], 19488, %[ak2]\n" \
  "v_add_u32 %[av], 19488, %[av]\n" \
  "s_cbranch_scc1 L_attn_loop_%=\n" \
  "v_smfmac_f32_32x32x32_bf16 %[ob], v[140:143], v[120:127], %[idx]\n" \
  "s_waitcnt lgkmcnt(0)\n" \
  "s_nop 15\n"

__device__ __forceinline__ f32x16 attn_exact(__amdgpu_buffer_rsrc_t ir, int offK1, int offK2, int offV, int hh, bf16x8 q1, unsigned q2) {
  const s16x4 x2 = mk2(hh ? 0u : q2, 0u);
  float m = -INFINITY;
#pragma unroll 1
  for (int tile = 0; tile < 64; ++tile) {
    const int base = (tile >> 2) * SG_BYTES;
    const s16x4 kk = mk2(__builtin_amdgcn_raw_buffer_load_b32(ir, base + offK2 + (hh ? 0 : (tile & 3) * 4), 0, 16), 0u);
    const bf16x8 k1 = __builtin_bit_cast(bf16x8, __builtin_amdgcn_raw_buffer_load_b128(ir, base + offK1 + (tile & 3) * 1024, 0, 16));
    m = fmaxf(m, max16(qk(k1, kk, q1, x2)));
  }
  m = swap32_max(m);
  f32x16 O;
#pragma unroll
  for (int i = 0; i < 16; ++i) O[i] = 0.f;
#pragma unroll 1
  for (int tile = 0; tile < 64; ++tile) {
    const int base = (tile >> 2) * SG_BYTES;
    const s16x4 kk = mk2(__builtin_amdgcn_raw_buffer_load_b32(ir, base + offK2 + (hh ? 0 : (tile & 3) * 4), 0, 16), 0u);
    const bf16x8 k1 = __builtin_bit_cast(bf16x8, __builtin_amdgcn_raw_buffer_load_b128(ir, base + offK1 + (tile & 3) * 1024, 0, 16));
    const bf16x8 vlo = __builtin_bit_cast(bf16x8, __builtin_amdgcn_raw_buffer_load_b128(ir, base + offV + (tile & 3) * 1280, 0, 16));
    const bf16x8 vhi = __builtin_bit_cast(bf16x8, __builtin_amdgcn_raw_buffer_load_b128(ir, base + offV + (tile & 3) * 1280 + 640, 0, 16));
    f32x16 S = qk(k1, kk, q1, x2);
#pragma unroll
    for (int i = 0; i < 16; ++i) S[i] = __builtin_amdgcn_exp2f(S[i] - m);
    O = __builtin_amdgcn_smfmac_f32_32x32x32_bf16(vlo, __builtin_bit_cast(bf16x16, __builtin_shufflevector(S, S, 0, 1, 2, 3, 4, 5, 6, 7)), O, 0xDDDDDDDD, 0, 0);
    O = __builtin_amdgcn_smfmac_f32_32x32x32_bf16(vhi, __builtin_bit_cast(bf16x16, __builtin_shufflevector(S, S, 8, 9, 10, 11, 12, 13, 14, 15)), O, 0xDDDDDDDD, 0, 0);
  }
  return O;
}

#define EARLY_POST_PRIO 2
#define OUT_AUX 16
#define Q_SLEEP 4
#define KV_DELAY 0
#define O_SLEEP 4
#define I_SLEEP 6
#define NLATE 4
constexpr int T_QX = 16384;
constexpr int T_WO = 58624;
constexpr int T_XCH = T_WO + 73728;
constexpr int T_Z = 0;
constexpr int T_PART = 10240;
constexpr int T_EPI = 30720;
static_assert(NLATE % 2 == 0 && T_WO >= NLATE * SG_BYTES && T_XCH + 24576 <= T_SYNC && T_EPI + 12800 <= T_WO, "lds");
constexpr int P_EARLY = NLATE * SG_BYTES / 1024;
constexpr int NG_HI = (153 - (P_EARLY + 20) + 3) / 4;
static_assert(P_EARLY + 20 + NG_HI * 4 <= IMG_PIECES && 12 + (32 - NG_HI) * 4 >= P_EARLY && (P_EARLY + 20) * 1024 >= (NLATE + 2) * SG_BYTES, "dma plan");
static_assert(NG_HI >= 16 && T_QX >= 12288 && T_QX + 12288 <= P_EARLY * 1024, "q hand-over area");

__device__ __forceinline__ void img_group(const char* src, char* sm, int p) {
  const GLB_AS unsigned* g = (const GLB_AS unsigned*)(src + p * 1024);
  LDS_AS unsigned* l = (LDS_AS unsigned*)(sm + p * 1024);
  __builtin_amdgcn_global_load_lds(g, l, 16, 0, 16);
  __builtin_amdgcn_global_load_lds(g, l, 16, 1024, 16);
  __builtin_amdgcn_global_load_lds(g, l, 16, 2048, 16);
  __builtin_amdgcn_global_load_lds(g, l, 16, 3072, 16);
}

extern "C" __global__ void __launch_bounds__(768) k_main(const u16* __restrict__ x, const u16* __restrict__ wq,
                                               const u16* __restrict__ wk, const u16* __restrict__ wv,
                                               const u16* __restrict__ gw, const u16* __restrict__ bk,
                                               const u16* __restrict__ bv, const u16* __restrict__ shk, char* ws,
                                               const u16* __restrict__ hk, const u16* __restrict__ dk,
                                               const u16* __restrict__ wo, u16* __restrict__ out) {
  __shared__ __attribute__((aligned(256))) char sm[163840];
  const int tid = threadIdx.x, lane = tid & 63;
  const int wave = __builtin_amdgcn_readfirstlane(tid >> 6);
  const int b = blockIdx.x;
  const int grp = b & 7, loc = b >> 3;
  volatile LDS_AS unsigned* sync = (volatile LDS_AS unsigned*)(sm + T_SYNC);
  STAMP_DECL;
  STAMP(0);
  if (wave == 6 && loc < 26) code_prefetch(loc >= 18, lane);
  if (wave == 7) {
    const u64 e = __hip_atomic_load((GLB_AS u64*)(ws + WS_FLAG + FL_E), __ATOMIC_RELAXED, __HIP_MEMORY_SCOPE_AGENT) + 1u;
    if (lane < 16) sync[lane] = lane < 14 ? 0u : (lane == 14 ? (unsigned)e : (unsigned)(e >> 32));
  }
  if (loc < 18) q_tile(x, wq, ws, sm, grp, loc, wave, lane STAMP_PASS);
  else if (loc < 26) kvg_tile(x, wk, wv, gw, ws, sm, grp * 8 + loc - 18, tid, wave, lane);
  else { __builtin_amdgcn_s_sleep(KV_DELAY); u_tile(bk, bv, shk, ws, sm, grp, loc - 26, tid, wave, lane); }
  STAMP(13);

  const bool has_tile = loc < (grp < 2 ? 30 : 29);
  const int qid = has_tile ? grp * 29 + (grp < 2 ? grp : 2) + loc : 0;
  const int mt = qid / 9, nt = qid - mt * 9;
  const int t0 = mt * 80, n0 = nt * 64;
  const __amdgpu_buffer_rsrc_t orsrc = __builtin_amdgcn_make_buffer_rsrc((void*)(ws + WS_O), 0, (int)O_BYTES, 0x00020000);
  const u64 tag = lds_tag(sm);
  const int qb = ((grp + 4) & 7) * 32 + loc;
  const int cwi = wave - 2;
  const int kh = cwi >= 5 ? 1 : 0, cw = cwi - kh * 5;
  const int g = lane >> 4, tl = lane & 15;
  u32x2 xv[2]; float gt;
  auto load_xg = [&]() {
    const int te = min(t0 + max(cw, 0) * 16 + tl, S_LEN - 1);
#pragma unroll
    for (int h = 0; h < 2; ++h) xv[h] = *(const u32x2*)(x + (size_t)te * DM + n0 + kh * 32 + h * 16 + g * 4);
    gt = __uint_as_float(__builtin_amdgcn_raw_buffer_load_b32(__builtin_amdgcn_make_buffer_rsrc((void*)(ws + WS_G), 0, S_LEN * 4, 0x00020000), te * 4, 0, 16));
    asm volatile("" ::: "memory");
  };

  if (wave < 4) {
    __builtin_amdgcn_s_setprio(2);
    const int W = b < 208 ? b + 208 * wave : 4 * b + wave;
    lds_wait(sync + 8, 1u);
    STAMP(14);
    const char* src = ws + WS_IMG + lane * 16;
    {
      const int pa = P_EARLY + wave * 4, pb = wave == 0 ? P_EARLY + 16 : wave * 4 - 4;
#pragma unroll 1
      for (int spin = 0; spin < (1 << 20); ++spin) {
        img_group(src, sm, pa);
        img_group(src, sm, pb);
        wait_vm<0>();
        unsigned mx = 0;
#pragma unroll
        for (int i = 0; i < 8; ++i) {
          const u32x4 v = *(const u32x4*)(sm + ((i < 4 ? pa : pb - 4) + i) * 1024 + lane * 16);
          mx = max(max(mx, max(v[0], v[1])), max(v[2], v[3]));
        }
        if (__all(mx != O_EMPTY)) break;
        __builtin_amdgcn_s_sleep(I_SLEEP);
      }
    }
    __builtin_amdgcn_s_setprio(3);
    RAW_BARRIER();
    if (wave == 0) { flag_wait<2>(ws, FL_S + grp * 512 + lane * 8, tag); if (lane == 0) sync[7] = 1u; }
    else lds_wait(sync + 7, 1u);
#pragma unroll
    for (int i = 0; i < 8; ++i) {
      if (i == 4) lds_wait(sync + 10, 4u);
      int p = P_EARLY + 20 + (wave + 4 * i) * 4;
      p = p > 152 ? p - (P_EARLY + 20 + NG_HI * 4) + 12 : p;
      img_group(src, sm, p);
    }
    wait_vm<0>();
    if (lane == 0) __hip_atomic_fetch_add((LDS_AS unsigned*)sync + 5, 1u, __ATOMIC_RELAXED, __HIP_MEMORY_SCOPE_WORKGROUP);
    {
      const int c1 = W < 576 ? W : W - 576, c2 = (W >= 576 && W < 576 + 128) ? W - 128 : c1;
      flag_wait<8>(ws, FL_U + (((lane & 16) ? c2 : c1) >> 6) * 128 + (lane & 15) * 8, tag);
      const __amdgpu_buffer_rsrc_t us = __builtin_amdgcn_make_buffer_rsrc((void*)(ws + WS_UR), 0, DM * S_LEN * 2, 0x00020000);
      const __amdgpu_buffer_rsrc_t up = __builtin_amdgcn_make_buffer_rsrc((void*)(ws + WS_PS), 0, (int)(WS_G - WS_PS), 0x00020000);
      u32x4 ud[4]; unsigned un[4]; int upo[4];
#pragma unroll
      for (int q = 0; q < 4; ++q) {
        const int cc = q < 2 ? c1 : c2, blk = W < 576 ? q : 2 + (q & 1);
        const int uo = cc * (S_LEN * 2) + blk * 1024 + lane * 16;
        upo[q] = (cc * 2 + (W < 576 ? 1 : 0)) * (PS_COPY * 4) + blk * 1024 + lane * 16;
        ud[q] = __builtin_amdgcn_raw_buffer_load_b128(us, uo, 0, 16);
        un[q] = __builtin_amdgcn_raw_buffer_load_b32(us, uo + 16, 0, 16);
        if (blk == 3 && lane == 63) un[q] = 0u;
      }
#pragma unroll
      for (int q = 0; q < 4; ++q) {
        const u32x4 sh = {__builtin_amdgcn_alignbit(ud[q][1], ud[q][0], 16), __builtin_amdgcn_alignbit(ud[q][2], ud[q][1], 16),
                          __builtin_amdgcn_alignbit(ud[q][3], ud[q][2], 16), __builtin_amdgcn_alignbit(un[q], ud[q][3], 16)};
        __builtin_amdgcn_raw_buffer_store_b128(ud[q], up, upo[q], 0, PS_AUX);
        __builtin_amdgcn_raw_buffer_store_b128(sh, up, upo[q] + PS_COPY * 2, 0, PS_AUX);
        if ((q & 1) && lane < 4 && (q == 3 || W >= 576))
          __builtin_amdgcn_raw_buffer_store_b128(u32x4{0u, 0u, 0u, 0u}, up, upo[q] + 1024 + (lane >> 1) * (PS_COPY * 2 - 32), 0, PS_AUX);
      }
    }
    STAMP(1);
    if (wave == 3) {
#pragma unroll
      for (int i = 0; i < 9; ++i)
        __builtin_amdgcn_raw_buffer_store_b128(u32x4{O_EMPTY, O_EMPTY, O_EMPTY, O_EMPTY}, orsrc,
                                               ((qb >> 1) * 72 + i * 8 + (lane >> 3)) * 256 + (qb & 1) * 128 + (lane & 7) * 16, 0, 16);
      wait_vm<0>();
      if (lane == 0) { cnt_add(ws, (CNT_F + qb / 10) * 128); sync[6] = 1u; }
    }
    STAMP(9);
    if (W < 576) {
      ssm_unit<4>(W, hk, dk, ws, lane);
    } else {
      for (int c = W - 576; c < 576; c += 448) ssm_unit<0>(c, hk, dk, ws, lane);
    }
    if (b == 0 && wave == 1) {
      cnt_wait<8>(ws, (CNT_F + min(lane, 25)) * 128, lane >= 25 ? 6u : 10u);
      if (lane == 0) flag_set(ws, FL_E, tag + 1u);
    }
    load_xg();
    wait_vm<0>();
    STAMP(2);
  } else {
    const int pair = wave & 3;
    const bool early = wave < 8;
    __builtin_amdgcn_s_setprio(2);
    const int r = lane & 31, hh = lane >> 5;
    const int tq = qb * 8 + pair * 2;
    const int offK1 = SG_K1 + lane * 16;
    const int offK2 = SG_K2 + (hh ? 512 : r * 16);
    const int offV = SG_V + hh * 320 + vpos(r) * 16;
    const int idx = 0xDDDDDDDD;
    const unsigned ldsBase = (unsigned)(uintptr_t)(LDS_AS char*)sm + (early ? NLATE * SG_BYTES : 0);

    const __amdgpu_buffer_rsrc_t irsrc = __builtin_amdgcn_make_buffer_rsrc((void*)(ws + WS_IMG), 0, IMG_BYTES, 0x00020000);
    bf16x8 qa1, qb1; unsigned qa2, qb2;
    f32x16 Oa, Ob;
    {
#pragma unroll
      for (int i = 0; i < 16; ++i) { Oa[i] = 0.f; Ob[i] = 0.f; }
      unsigned aK1 = ldsBase + offK1, aK2 = ldsBase + offK2, aV = ldsBase + offV;
      asm volatile("" : "+v"(Oa), "+v"(Ob), "+v"(aK1), "+v"(aK2), "+v"(aV) :: "memory");
      const __amdgpu_buffer_rsrc_t qr = __builtin_amdgcn_make_buffer_rsrc((void*)(ws + WS_Q + (size_t)tq * (DM * 2)), 0, DM * 4, 0x00020000);
      bf16x8 k1; unsigned k2;
      auto load_q = [&]() {
#pragma unroll 1
        for (int spin = 0; spin < (1 << 22); ++spin) {
          asm volatile("" ::: "memory");
          const u32x4 ta = __builtin_amdgcn_raw_buffer_load_b128(qr, r * 36 + hh * 16, 0, 16);
          const u32x4 tb = __builtin_amdgcn_raw_buffer_load_b128(qr, r * 36 + hh * 16 + DM * 2, 0, 16);
          qa2 = __builtin_amdgcn_raw_buffer_load_b32(qr, r * 36 + 32, 0, 16);
          qb2 = __builtin_amdgcn_raw_buffer_load_b32(qr, r * 36 + 32 + DM * 2, 0, 16);
          const u32x4 tk = __builtin_amdgcn_raw_buffer_load_b128(irsrc, offK1, 0, 16);
          k2 = __builtin_amdgcn_raw_buffer_load_b32(irsrc, offK2, 0, 16);
          qa1 = __builtin_bit_cast(bf16x8, ta); qb1 = __builtin_bit_cast(bf16x8, tb); k1 = __builtin_bit_cast(bf16x8, tk);
          const unsigned mq = max(max(max(max(tk[0], tk[1]), max(tk[2], tk[3])), k2), max(max(max(max(ta[0], ta[1]), max(ta[2], ta[3])), max(max(tb[0], tb[1]), max(tb[2], tb[3]))), max(qa2, qb2)));
          if (__all(mq != O_EMPTY)) break;
          __builtin_amdgcn_s_sleep(Q_SLEEP);
        }
      };
      u32x4* qx = (u32x4*)(sm + T_QX + pair * 3072) + lane;
      unsigned pm;
      if (early) {
        if (wave >= 6) {
          flag_wait<4>(ws, wave == 6 ? FL_M + grp * 512 + lane * 8 : FL_Q + (qb >> 4) * 128 + min(lane, 8) * 8, tag);
          if (lane == 0) sync[wave == 6 ? 8 : 11] = 1u;
        }
        lds_wait(sync + 8, 1u);
        lds_wait(sync + 11, 1u);
        STAMP(14);
        load_q();
        const float ma = swap32_max(max16(qk(k1, mk2(k2, 0u), qa1, mk2(hh ? 0u : qa2, 0u))));
        const float mb = swap32_max(max16(qk(k1, mk2(k2, 0u), qb1, mk2(hh ? 0u : qb2, 0u))));
        pm = pack2(-ma, -mb);
        qx[0] = __builtin_bit_cast(u32x4, qa1); qx[64] = __builtin_bit_cast(u32x4, qb1); qx[128] = u32x4{qa2, qb2, pm, 0u};
        STAMP(9);
        RAW_BARRIER();
      } else {
        __builtin_amdgcn_s_setprio(3);
        if (loc >= 26) { u_store(ws, sm, grp, loc - 26, wave, lane); u_publish(ws, sm, grp, loc - 26, lane); }
        RAW_BARRIER();
        qa1 = __builtin_bit_cast(bf16x8, qx[0]); qb1 = __builtin_bit_cast(bf16x8, qx[64]);
        const u32x4 e = qx[128];
        qa2 = e[0]; qb2 = e[1]; pm = e[2];
        WAIT_LGKM0();
        if (lane == 0) __hip_atomic_fetch_add((LDS_AS unsigned*)sync + 10, 1u, __ATOMIC_RELAXED, __HIP_MEMORY_SCOPE_WORKGROUP);
        __builtin_amdgcn_s_setprio(LATE_PRIO);
      }
      STAMP(1); CSTAMP(10);
      const unsigned xa = hh ? 0u : qa2, xb = hh ? 0u : qb2;
      const unsigned nma = pm & 0xffffu, nmb = pm >> 16;
      const s16x4 qa2e = mk2(hh ? nma : qa2, 0u), qa2o = mk2(hh ? nma : 0u, xa);
      const s16x4 qb2e = mk2(hh ? nmb : qb2, 0u), qb2o = mk2(hh ? nmb : 0u, xb);
      int it = 0;
      const int nit = early ? 16 - NLATE : NLATE;
      int chk = 2 - (wave >> 2), stmp;
      unsigned tmp;
      const unsigned syn = (unsigned)(uintptr_t)(sync + 5);
      asm volatile(ATTN_LOOP_ASM
                   : [oa] "+v"(Oa), [ob] "+v"(Ob), [ak1] "+v"(aK1), [ak2] "+v"(aK2), [av] "+v"(aV), [it] "+s"(it),
                     [chk] "+s"(chk), [stmp] "=&s"(stmp), [tmp] "=&v"(tmp)
                   : [qa1] "v"(qa1), [qb1] "v"(qb1), [qa2e] "v"(qa2e), [qa2o] "v"(qa2o), [qb2e] "v"(qb2e), [qb2o] "v"(qb2o),
                     [idx] "v"(idx), [nit] "s"(nit), [syn] "v"(syn)
                   : "memory", "scc", ATTN_CLOB ITCLOB);
    }
    STAMP(2); CSTAMP(11);
    load_xg();
    f32x4* xch = (f32x4*)(sm + T_XCH + pair * 6144) + lane;
    if (early) {
      __builtin_amdgcn_s_setprio(EARLY_POST_PRIO);
      if (lane == 0) __hip_atomic_fetch_add((LDS_AS unsigned*)sync, 1u, __ATOMIC_RELAXED, __HIP_MEMORY_SCOPE_WORKGROUP);
      lds_wait(sync, 4u);
      lds_wait(sync + 6, 1u);
#pragma unroll
      for (int i = 0; i < 3; ++i) {
        xch[i * 64] = f32x4{Oa[4 * i], Oa[4 * i + 1], Oa[4 * i + 2], Oa[4 * i + 3]};
        xch[(3 + i) * 64] = f32x4{Ob[4 * i], Ob[4 * i + 1], Ob[4 * i + 2], Ob[4 * i + 3]};
      }
      WAIT_LGKM0();
      if (lane == 0) sync[1 + pair] = 1u;
      const char* wsrc = (const char*)(wo + n0) + (size_t)(pair * 144 + (lane >> 3)) * (DM * 2);
      const int c0 = ((lane & 7) ^ (wsw(lane >> 3) << 1)) << 4;
#pragma unroll
      for (int i = 0; i < 18; ++i) glds16(wsrc + i * 8 * DM * 2 + (c0 ^ ((i & 1) << 6)), sm + T_WO + pair * 18432 + i * 1024);
      if (wave == 4) cnt_wait<8>(ws, (CNT_F + mt) * 128, mt == 25 ? 6u : 10u);
      wait_vm<0>();
    } else {
      __builtin_amdgcn_s_setprio(3);
      lds_wait(sync + 1 + pair, 1u);
      float big = 0.f;
#pragma unroll
      for (int i = 0; i < 3; ++i) {
        const f32x4 pa = xch[i * 64], pb = xch[(3 + i) * 64];
#pragma unroll
        for (int e = 0; e < 4; ++e) if (4 * i + e < 11) {
          Oa[4 * i + e] += pa[e]; Ob[4 * i + e] += pb[e];
          big += fabsf(Oa[4 * i + e]) + fabsf(Ob[4 * i + e]);
        }
      }
      if (__any(!(big < 1e30f) || (!hh && !(fminf(Oa[10], Ob[10]) > 1e-12f)))) {
        Oa = attn_exact(irsrc, offK1, offK2, offV, hh, qa1, qa2);
        Ob = attn_exact(irsrc, offK1, offK2, offV, hh, qb1, qb2);
      }
      STAMP(3);
      const auto la = __builtin_amdgcn_permlane32_swap(__float_as_uint(Oa[10]), __float_as_uint(Oa[10]), false, false);
      const auto lb = __builtin_amdgcn_permlane32_swap(__float_as_uint(Ob[10]), __float_as_uint(Ob[10]), false, false);
      const float ia = __builtin_amdgcn_rcpf(__uint_as_float(la[0])), ib = __builtin_amdgcn_rcpf(__uint_as_float(lb[0]));
      char* ost = sm + T_XCH + pair * 6144;
      char* od = ost + r * 36 + hh * 8;
      *(unsigned*)(od) = pack2(Oa[0] * ia, Oa[1] * ia);        *(unsigned*)(od + 1168) = pack2(Ob[0] * ib, Ob[1] * ib);
      *(unsigned*)(od + 4) = pack2(Oa[2] * ia, Oa[3] * ia);    *(unsigned*)(od + 1172) = pack2(Ob[2] * ib, Ob[3] * ib);
      *(unsigned*)(od + 16) = pack2(Oa[4] * ia, Oa[5] * ia);   *(unsigned*)(od + 1184) = pack2(Ob[4] * ib, Ob[5] * ib);
      *(unsigned*)(od + 20) = pack2(Oa[6] * ia, Oa[7] * ia);   *(unsigned*)(od + 1188) = pack2(Ob[6] * ib, Ob[7] * ib);
      if (!hh) { *(unsigned*)(od + 32) = pack2(Oa[8] * ia, Oa[9] * ia); *(unsigned*)(od + 1200) = pack2(Ob[8] * ib, Ob[9] * ib); }
      u32x4 ov[3];
#pragma unroll
      for (int i = 0; i < 3; ++i) ov[i] = *(const u32x4*)(ost + (lane & 1) * 1168 + (i * 32 + (lane >> 1)) * 16);
#pragma unroll
      for (int i = 0; i < 3; ++i) {
#pragma unroll
        for (int e = 0; e < 4; ++e) ov[i][e] = min(ov[i][e], O_EMPTY - 1u);
        if (i < 2 || lane < 16)
          __builtin_amdgcn_raw_buffer_store_b128(ov[i], orsrc, (((tq >> 4) * 72 + i * 32 + (lane >> 1)) * 16 + (tq & 15) + (lane & 1)) * 16, 0, 16);
      }
    }
  }
  STAMP(4);
  if (!has_tile) { STAMP_FLUSH(1); return; }

  RAW_BARRIER();
  STAMP(5);
  if (wave < 2) {
    cnt_wait<2>(ws, (CNT_Z + nt * 2 + (((lane & 1) ? t0 : t0 + 79) < 1024 ? 0 : 1)) * 128, 64u);
    const __amdgpu_buffer_rsrc_t zrsrc = __builtin_amdgcn_make_buffer_rsrc((void*)(ws + WS_Z), 0, (int)Z_BYTES, 0x00020000);
    u32x4 zv[5];
#pragma unroll
    for (int i = 0; i < 5; ++i) {
      const int ci = (wave * 5 + i) * 64 + lane, c = ci / 10, k = ci - c * 10;
      zv[i] = __builtin_amdgcn_raw_buffer_load_b128(zrsrc, ((n0 + c) * S_LEN + t0 + k * 8) * 2, 0, 16);
    }
#pragma unroll
    for (int i = 0; i < 5; ++i) *(u32x4*)(sm + T_Z + ((wave * 5 + i) * 64 + lane) * 16) = zv[i];
    WAIT_LGKM0();
  } else {
    float bx[2][4];
    const float omg = 1.0f - gt;
#pragma unroll
    for (int h = 0; h < 2; ++h) {
      bx[h][0] = omg * bflo(xv[h][0]); bx[h][1] = omg * bfhi(xv[h][0]);
      bx[h][2] = omg * bflo(xv[h][1]); bx[h][3] = omg * bfhi(xv[h][1]);
    }
    const int rgp = mt * 5 + cw;
    const int obase = ((rgp * 18 + kh * 9) * 64 + lane) * 16;
    const char* wl = sm + T_WO + kh * 9 * 4096;
    constexpr int NPRE = 5;
    bf16x8 wpre[NPRE][4];
    int so[9];
#pragma unroll
    for (int s = 0; s < 9; ++s) so[s] = s + nt >= 9 ? s + nt - 9 : s + nt;
#pragma unroll
    for (int s = 0; s < NPRE; ++s)
#pragma unroll
      for (int nb = 0; nb < 4; ++nb) wpre[s][nb] = w_frag(wl + so[9 - NPRE + s] * 4096, lane, 0, nb);
    unsigned wq4[4];
#pragma unroll
    for (int nb = 0; nb < 4; ++nb) {
      const int k = g * 8 + (tl >> 2);
      wq4[nb] = (unsigned)(uintptr_t)(LDS_AS char*)sm + T_WO + kh * 9 * 4096 + k * 128 + ((nb ^ wsw(k)) << 5) + (lane & 3) * 8;
    }
    STAMP(6);
    f32x4 acc[4];
#pragma unroll 1
    for (int spin = 0; spin < (1 << 20); ++spin) {
      asm volatile("" : "+v"(wq4[0]), "+v"(wq4[1]), "+v"(wq4[2]), "+v"(wq4[3]) :: "memory");
      u32x4 of[9];
#pragma unroll
      for (int s = 0; s < 9; ++s) of[s] = __builtin_amdgcn_raw_buffer_load_b128(orsrc, obase, so[s] * 1024, 16);
      __builtin_amdgcn_sched_barrier(0);
#pragma unroll
      for (int nb = 0; nb < 4; ++nb) acc[nb] = f32x4{0.f, 0.f, 0.f, 0.f};
      unsigned mx = 0;
#pragma unroll
      for (int s = 0; s < 9; ++s) {
#pragma unroll
        for (int nb = 0; nb < 4; ++nb) {
          bf16x8 wf;
          if (s >= 9 - NPRE) wf = wpre[s - (9 - NPRE)][nb];
          else {
            LDS_AS bf16x4* wp = (LDS_AS bf16x4*)(uintptr_t)(wq4[nb] + so[s] * 4096);
            wf = __builtin_shufflevector(__builtin_amdgcn_ds_read_tr16_b64_v4bf16(wp), __builtin_amdgcn_ds_read_tr16_b64_v4bf16(wp + 64), 0, 1, 2, 3, 4, 5, 6, 7);
          }
          acc[nb] = MFMA16(wf, __builtin_bit_cast(bf16x8, of[s]), acc[nb]);
        }
        mx = max(max(mx, max(of[s][0], of[s][1])), max(of[s][2], of[s][3]));
      }
      if (__all(mx != O_EMPTY)) break;
      __builtin_amdgcn_s_sleep(O_SLEEP);
    }
    f32x4* pd = (f32x4*)(sm + T_PART + cwi * 2048) + lane;
    pd[0] = kh ? acc[0] : acc[2];
    pd[64] = kh ? acc[1] : acc[3];
    STAMP(7);
    RAW_BARRIER();
    const f32x4* ps = (const f32x4*)(sm + T_PART + (kh ? cw : cw + 5) * 2048) + lane;
    char* sE = sm + T_EPI + cwi * 1280;
    f32x4 pa[2]; u32x2 z[2];
#pragma unroll
    for (int h = 0; h < 2; ++h) {
      pa[h] = ps[h * 64];
      z[h] = __builtin_bit_cast(u32x2, tr_read(sm + T_Z + ((kh * 2 + h) * 16 + g * 4 + (tl >> 2)) * 160 + (cw * 16 + (tl & 3) * 4) * 2));
    }
    asm volatile("" ::: "memory");
#pragma unroll
    for (int h = 0; h < 2; ++h) {
      const f32x4 a = (kh ? acc[2 + h] : acc[h]) + pa[h];
      *(u32x2*)(sE + tl * 80 + h * 32 + g * 8) =
          u32x2{pack2(gt * a[0] + bx[h][0] * bflo(z[h][0]), gt * a[1] + bx[h][1] * bfhi(z[h][0])),
                pack2(gt * a[2] + bx[h][2] * bflo(z[h][1]), gt * a[3] + bx[h][3] * bfhi(z[h][1]))};
    }
    const u32x4 o = *(const u32x4*)(sE + (lane >> 2) * 80 + (lane & 3) * 16);
    const __amdgpu_buffer_rsrc_t outr = __builtin_amdgcn_make_buffer_rsrc((void*)out, 0, S_LEN * DM * 2, 0x00020000);
    __builtin_amdgcn_raw_buffer_store_b128(o, outr, ((t0 + cw * 16 + (lane >> 2)) * DM + n0 + kh * 32 + (lane & 3) * 8) * 2, 0, OUT_AUX);
  }
  STAMP(8);
  STAMP_FLUSH(1);
}

extern "C" __global__ void k9_end() {}

extern "C" void kernel_launch(void* const* d_in, const int* in_sizes, int n_in, void* d_out, int out_size,
                              void* d_ws, size_t ws_size, hipStream_t stream) {
  const u16* x = (const u16*)d_in[0];
  const u16* wq = (const u16*)d_in[1];
  const u16* wk = (const u16*)d_in[2];
  const u16* wv = (const u16*)d_in[3];
  const u16* wo = (const u16*)d_in[4];
  const u16* bk = (const u16*)d_in[6];
  const u16* bv = (const u16*)d_in[7];
  const u16* shk = (const u16*)d_in[8];
  const u16* hk = (const u16*)d_in[9];
  const u16* dk = (const u16*)d_in[10];
  const u16* gw = (const u16*)d_in[11];
  char* ws = (char*)d_ws;
  k_main<<<256, 768, 0, stream>>>(x, wq, wk, wv, gw, bk, bv, shk, ws, hk, dk, wo, (u16*)d_out);
}
```

```cpp
#include <hip/hip_runtime.h>
#include <hip/hip_bf16.h>
#include <stdint.h>

typedef __attribute__((ext_vector_type(4))) __bf16 bf16x4;
typedef __attribute__((ext_vector_type(8))) __bf16 bf16x8;
typedef __attribute__((ext_vector_type(16))) __bf16 bf16x16;
typedef __attribute__((ext_vector_type(4))) short s16x4;
typedef __attribute__((ext_vector_type(4))) float f32x4;
typedef __attribute__((ext_vector_type(16))) float f32x16;
typedef __attribute__((ext_vector_type(2))) unsigned u32x2;
typedef __attribute__((ext_vector_type(4))) unsigned u32x4;
typedef unsigned short u16;
typedef __attribute__((ext_vector_type(4), aligned(4))) unsigned u32x4_a4;

#define LDS_AS __attribute__((address_space(3)))
#define GLB_AS __attribute__((address_space(1)))

constexpr int S_LEN = 2048, DM = 576, DH = 18;
constexpr int PS_COPY = 2112;
constexpr int SG_K1 = 0, SG_K2 = 4096, SG_V = 4624, SG_BYTES = 9744;
constexpr int IMG_PIECES = 156;
constexpr int IMG_BYTES = IMG_PIECES * 1024;

constexpr size_t WS_CNT = 0;
constexpr size_t WS_FLAG = 8192;
constexpr int FL_E = 0, FL_Q = 128, FL_S = FL_Q + 16 * 128, FL_M = FL_S + 8 * 512, FL_U = FL_M + 8 * 512;
constexpr size_t WS_Q = 24576;
constexpr size_t WS_IMG = WS_Q + (size_t)S_LEN * DM * 2;
constexpr size_t WS_UR = WS_IMG + IMG_BYTES;
constexpr size_t WS_PS = WS_UR + (size_t)DM * S_LEN * 2;
constexpr size_t WS_G = WS_PS + (size_t)DM * 4 * PS_COPY * 2;
constexpr size_t WS_O = WS_G + S_LEN * 4;
constexpr size_t O_BYTES = (size_t)S_LEN * DM * 2;
constexpr size_t WS_Z = WS_O + O_BYTES;
constexpr size_t Z_BYTES = (size_t)S_LEN * DM * 2;
constexpr int CNT_Z = 0, CNT_F = 18;
constexpr unsigned O_EMPTY = 0xffffffffu;

#define STAMP_DECL do {} while (0)
#define STAMP_ARG
#define STAMP_PASS
#define STAMP(i) do {} while (0)
#define CSTAMP(i) do {} while (0)
#define STAMP_FLUSH(kern) do {} while (0)
#define WAIT_LGKM0() do { asm volatile("" ::: "memory"); __builtin_amdgcn_s_waitcnt(0xC07F); asm volatile("" ::: "memory"); } while (0)
#define RAW_BARRIER() do { WAIT_LGKM0(); __builtin_amdgcn_s_barrier(); asm volatile("" ::: "memory"); } while (0)
template <int N> __device__ __forceinline__ void wait_vm() {
  asm volatile("s_waitcnt vmcnt(%0)" :: "i"(N) : "memory");
  if (N == 0) __builtin_amdgcn_s_waitcnt(0x0070);
}

__device__ __forceinline__ float bflo(unsigned w) { return __uint_as_float(w << 16); }
__device__ __forceinline__ float bfhi(unsigned w) { return __uint_as_float(w & 0xffff0000u); }
__device__ __forceinline__ unsigned pack2(float lo, float hi) {
  typedef __attribute__((ext_vector_type(2))) __bf16 bf16x2;
  bf16x2 v; v[0] = (__bf16)lo; v[1] = (__bf16)hi;
  return __builtin_bit_cast(unsigned, v);
}
__device__ __forceinline__ float rbf(float x) { return (float)(__bf16)x; }
__device__ __forceinline__ void glds16(const void* g, void* lds) {
  __builtin_amdgcn_global_load_lds((const GLB_AS unsigned*)g, (LDS_AS unsigned*)lds, 16, 0, 0);
}
__device__ __forceinline__ bf16x4 tr_read(const char* p) {
  return __builtin_amdgcn_ds_read_tr16_b64_v4bf16((LDS_AS bf16x4*)p);
}
__device__ __forceinline__ int vpos(int d) {
  const int a = d - (d > 11 && d < 16);
  return d > 18 ? 15 : (d == 11 ? 19 : a);
}
#define MFMA16(a, b, c) __builtin_amdgcn_mfma_f32_16x16x32_bf16(a, b, c, 0, 0, 0)

__device__ __forceinline__ int wsw(int k) { return ((k >> 1) & 1) | ((k >> 2) & 2); }
__device__ __forceinline__ bf16x8 a_frag(const char* sA, int lane, int rg, int s) {
  const int row = rg * 16 + (lane & 15);
  return *(const bf16x8*)(sA + row * 128 + (((s * 4 + (lane >> 4)) ^ ((row >> 1) & 7)) << 4));
}
__device__ __forceinline__ bf16x8 w_frag(const char* sW, int lane, int s, int nb) {
  const int k = s * 32 + (lane >> 4) * 8 + ((lane & 15) >> 2);
  const char* b = sW + k * 128 + ((nb ^ wsw(k)) << 5) + (lane & 3) * 8;
  const bf16x4 lo = tr_read(b), hi = tr_read(b + 512);
  return __builtin_shufflevector(lo, hi, 0, 1, 2, 3, 4, 5, 6, 7);
}

template <int NP, int NSLOT, int SLOT, int DEPTH>
__device__ __forceinline__ void ring_loader(const char* const (&base)[NP], const int (&step)[NP], char* const (&dst)[NP], int rot) {
  auto issue = [&](int j) {
    int ch = j + rot; ch = ch >= 9 ? ch - 9 : ch;
#pragma unroll
    for (int i = 0; i < NP; ++i) glds16(base[i] + ch * step[i], dst[i] + ((j + 3) % NSLOT) * SLOT);
  };
#pragma unroll
  for (int j = 0; j < DEPTH; ++j) issue(j);
#define RL_STEP(j) do { wait_vm<NP * ((8 - (j)) < (DEPTH - 1) ? (8 - (j)) : (DEPTH - 1))>(); __builtin_amdgcn_s_barrier(); \
    asm volatile("" ::: "memory"); if ((j) + DEPTH < 9) issue((j) + DEPTH); } while (0)
  RL_STEP(0); RL_STEP(1); RL_STEP(2); RL_STEP(3); RL_STEP(4); RL_STEP(5); RL_STEP(6); RL_STEP(7); RL_STEP(8);
#undef RL_STEP
}

#define K1_DEPTH 3
__device__ __forceinline__ void lds_wait(volatile LDS_AS unsigned* p, unsigned need) {
#pragma unroll 1
  for (int spin = 0; spin < (1 << 24); ++spin) {
    if (__builtin_amdgcn_readfirstlane(*p) >= need) break;
    __builtin_amdgcn_s_sleep(2);
  }
  asm volatile("" ::: "memory");
}

typedef unsigned long long u64;
__device__ __forceinline__ void flag_set(char* ws, int off, u64 tag) {
  __hip_atomic_store((GLB_AS u64*)(ws + WS_FLAG + off), tag, __ATOMIC_RELAXED, __HIP_MEMORY_SCOPE_AGENT);
}
template <int SLP>
__device__ __forceinline__ void flag_wait(char* ws, int off, u64 tag) {
  GLB_AS u64* fp = (GLB_AS u64*)(ws + WS_FLAG + off);
#pragma unroll 1
  for (int spin = 0; spin < (1 << 22); ++spin) {
    const u64 v = __hip_atomic_load(fp, __ATOMIC_RELAXED, __HIP_MEMORY_SCOPE_AGENT);
    if (__all(v == tag)) break;
    __builtin_amdgcn_s_sleep(SLP);
  }
  asm volatile("" ::: "memory");
}
__device__ __forceinline__ void code_prefetch(bool all, int lane) {
  u64 pc; unsigned d;
  asm volatile("s_getpc_b64 %0\nL_pf_%=:\n s_mov_b32 %1, k9_end-L_pf_%=" : "=s"(pc), "=s"(d));
  unsigned o[4], t[4];
#pragma unroll
  for (int i = 0; i < 4; ++i) o[i] = min((unsigned)(lane * 128 + i * 8192), all ? d - 4u : 12288u);
  asm volatile("global_load_dword %0, %4, %8\n global_load_dword %1, %5, %8\n global_load_dword %2, %6, %8\n"
               " global_load_dword %3, %7, %8\n s_waitcnt vmcnt(0)"
               : "=&v"(t[0]), "=&v"(t[1]), "=&v"(t[2]), "=&v"(t[3])
               : "v"(o[0]), "v"(o[1]), "v"(o[2]), "v"(o[3]), "s"(pc) : "memory");
}
constexpr int T_SYNC = 163840 - 64;
__device__ __forceinline__ u64 lds_tag(char* sm) { return *(volatile LDS_AS u64*)(sm + T_SYNC + 56); }

__device__ __forceinline__ void q_tile(const u16* __restrict__ x, const u16* __restrict__ wq, char* ws, char* sm,
                                       int grp, int loc, int wave, int lane STAMP_ARG) {
  const int g = lane >> 4, tl = lane & 15;
  const int mh = loc >= 9 ? 1 : 0, nt = loc - mh * 9;
  const int t0 = (grp * 2 + mh) * 128, n0 = nt * 64;
  constexpr int SLOT = 24576, NSLOT = 6;
  const __amdgpu_buffer_rsrc_t qr = __builtin_amdgcn_make_buffer_rsrc((void*)(ws + WS_Q), 0, S_LEN * DM * 2, 0x00020000);
  if (wave < 6) {
    int rot = nt + 4 * mh + grp; rot = rot >= 18 ? rot - 18 : (rot >= 9 ? rot - 9 : rot);
    const char* base[4]; int step[4]; char* dst[4];
#pragma unroll
    for (int i = 0; i < 4; ++i) {
      const int pc = wave + 6 * i;
      const bool isW = pc < 8;
      const int row = (isW ? pc : pc - 8) * 8 + (lane >> 3);
      const int sw = isW ? wsw(row) << 1 : (row >> 1) & 7;
      base[i] = (isW ? (const char*)(wq + n0) : (const char*)(x + (size_t)t0 * DM)) + (unsigned)row * (DM * 2) + (((lane & 7) ^ sw) << 4);
      step[i] = isW ? 64 * DM * 2 : 128;
      dst[i] = sm + pc * 1024;
    }
    ring_loader<4, NSLOT, SLOT, K1_DEPTH>(base, step, dst, rot);
  } else if (wave < 8) {
    RAW_BARRIER();
    if (wave == 7) {
#pragma unroll
      for (int i = 0; i < 4; ++i) {
#pragma unroll
        for (int k = 0; k < 4; ++k)
          __builtin_amdgcn_raw_buffer_store_b128(u32x4{O_EMPTY, O_EMPTY, O_EMPTY, O_EMPTY}, qr, ((t0 + (i * 4 + k) * 8 + (lane >> 3)) * DM + n0 + (lane & 7) * 8) * 2, 0, 16);
        RAW_BARRIER();
      }
      RAW_BARRIER(); RAW_BARRIER(); RAW_BARRIER();
      wait_vm<0>();
      if (lane == 0) flag_set(ws, FL_Q + (grp * 2 + mh) * 128 + nt * 8, lds_tag(sm));
      RAW_BARRIER();
    } else {
#pragma unroll
      for (int j = 1; j < 9; ++j) RAW_BARRIER();
    }
  } else {
    const int cwv = wave - 8;
    __builtin_amdgcn_s_setprio(3);
    f32x4 acc[2][4];
#pragma unroll
    for (int i = 0; i < 2; ++i)
#pragma unroll
      for (int nb = 0; nb < 4; ++nb) acc[i][nb] = f32x4{0.f, 0.f, 0.f, 0.f};
    bf16x8 af[2][4], wf[2][8];
    auto rd = [&](int j, bf16x8 (&a)[4], bf16x8 (&w)[8]) {
      const char* sl = sm + (j >= 3 ? j - 3 : j + 3) * SLOT;
#pragma unroll
      for (int s = 0; s < 2; ++s) {
#pragma unroll
        for (int i = 0; i < 2; ++i) a[s * 2 + i] = a_frag(sl + 8192, lane, cwv * 2 + i, s);
#pragma unroll
        for (int nb = 0; nb < 4; ++nb) w[s * 4 + nb] = w_frag(sl, lane, s, nb);
      }
    };
    auto mm = [&](bf16x8 (&a)[4], bf16x8 (&w)[8]) {
#pragma unroll
      for (int s = 0; s < 2; ++s)
#pragma unroll
        for (int nb = 0; nb < 4; ++nb)
#pragma unroll
          for (int i = 0; i < 2; ++i) acc[i][nb] = MFMA16(w[s * 4 + nb], a[s * 2 + i], acc[i][nb]);
    };
    auto mix = [&]() {
#pragma unroll
      for (int i = 0; i < 10; ++i) { __builtin_amdgcn_sched_group_barrier(0x8, 1, 0); __builtin_amdgcn_sched_group_barrier(0x100, 2, 0); }
      __builtin_amdgcn_sched_group_barrier(0x8, 6, 0);
      __builtin_amdgcn_sched_barrier(0);
    };
    RAW_BARRIER();
    STAMP(12);
    rd(0, af[0], wf[0]);
#pragma unroll 1
    for (int j = 1; j < 9; j += 2) {
      RAW_BARRIER(); __builtin_amdgcn_sched_barrier(0); rd(j, af[1], wf[1]); mm(af[0], wf[0]); mix();
      RAW_BARRIER(); __builtin_amdgcn_sched_barrier(0); rd(j + 1, af[0], wf[0]); mm(af[1], wf[1]); mix();
    }
    mm(af[0], wf[0]);
    const float qscale = 0.23570226039551584f * 1.4426950408889634f;
    char* sE = sm + 4 * SLOT + cwv * 4608;
#pragma unroll
    for (int i = 0; i < 2; ++i)
#pragma unroll
      for (int nb = 0; nb < 4; ++nb) {
        const f32x4 a = acc[i][nb] * qscale;
        *(u32x2*)(sE + (i * 16 + tl) * 144 + nb * 32 + g * 8) =
            u32x2{min(pack2(a[0], a[1]), O_EMPTY - 1u), min(pack2(a[2], a[3]), O_EMPTY - 1u)};
      }
#pragma unroll
    for (int i = 0; i < 4; ++i) {
      const int row = i * 8 + (lane >> 3);
      const u32x4 v = *(const u32x4*)(sE + row * 144 + (lane & 7) * 16);
      __builtin_amdgcn_raw_buffer_store_b128(v, qr, ((t0 + cwv * 32 + row) * DM + n0 + (lane & 7) * 8) * 2, 0, 16);
    }
  }
}

__device__ __forceinline__ void kvg_tile(const u16* __restrict__ x, const u16* __restrict__ wk, const u16* __restrict__ wv,
                                         const u16* __restrict__ gw, char* ws, char* sm, int si, int tid, int wave, int lane) {
  const int g = lane >> 4, tl = lane & 15;
  const int t0 = si * 32;
  const __amdgpu_buffer_rsrc_t ir = __builtin_amdgcn_make_buffer_rsrc((void*)(ws + WS_IMG), 0, IMG_BYTES, 0x00020000);
  char* sW = sm + 36864;
  if (wave != 6 && wave != 7) {
    const int k = min(wave < 6 ? tid : tid - 128, 575);
    const u32x4 k0 = *(const u32x4_a4*)(wk + k * DH), k1 = *(const u32x4_a4*)(wk + k * DH + 8);
    const u32x4 v0 = *(const u32x4_a4*)(wv + k * DH), v1 = *(const u32x4_a4*)(wv + k * DH + 8);
    const unsigned k2 = *(const unsigned*)(wk + k * DH + 16), v2 = *(const unsigned*)(wv + k * DH + 16);
    const unsigned gk = gw[k];
    if (wave < 4) {
      const int row = wave * 8 + (lane >> 3);
      const char* xs = (const char*)x + (unsigned)(t0 + row) * (DM * 2) + (((lane & 7) ^ ((row >> 1) & 7)) << 4);
#pragma unroll
      for (int ch = 0; ch < 9; ++ch) glds16(xs + ch * 128, sm + ch * 4096 + wave * 1024);
    }
    char* d = sW + k * 128;
    const int sw = wsw(k);
    *(u32x4*)(d + (sw << 5)) = k0; *(u32x4*)(d + (sw << 5) + 16) = k1;
    *(u32x4*)(d + ((1 ^ sw) << 5)) = v0; *(u32x4*)(d + ((1 ^ sw) << 5) + 16) = v1;
    *(u32x4*)(d + ((2 ^ sw) << 5)) = u32x4{k2, gk, v2, 0u}; *(u32x4*)(d + ((2 ^ sw) << 5) + 16) = u32x4{0u, 0u, 0u, 0u};
  } else if (wave == 7) {
    const int img = (si >> 2) * SG_BYTES, tile = si & 3;
    __builtin_amdgcn_raw_buffer_store_b128(u32x4{O_EMPTY, O_EMPTY, O_EMPTY, O_EMPTY}, ir, img + SG_K1 + tile * 1024 + lane * 16, 0, 16);
#pragma unroll
    for (int i = 0; i < 2; ++i) {
      const int ch = i * 64 + lane, pos = ch % 20;
      const unsigned val = pos == 15 ? 0u : (pos == 18 ? 0x3f803f80u : O_EMPTY);
      if (ch < 80) __builtin_amdgcn_raw_buffer_store_b128(u32x4{val, val, val, val}, ir, img + SG_V + tile * 1280 + ch * 16, 0, 16);
    }
    if (lane < 33) __builtin_amdgcn_raw_buffer_store_b32(lane < 32 ? O_EMPTY : ((tile & 1) ? 0u : 0x3f80u), ir, img + SG_K2 + lane * 16 + tile * 4, 0, 16);
    if (si == 0) __hip_atomic_store((GLB_AS unsigned*)(ws + WS_CNT + lane * 128), 0u, __ATOMIC_RELAXED, __HIP_MEMORY_SCOPE_AGENT);
    wait_vm<0>();
    if (lane < 8) flag_set(ws, FL_M + lane * 512 + si * 8, lds_tag(sm));
  }
  if (wave != 7) wait_vm<0>();
  RAW_BARRIER();
  const int rg = wave & 1, kh = (wave >> 1) & 1, sub = wave >> 2;
  f32x4 acc = {0.f, 0.f, 0.f, 0.f};
  {
    bf16x8 fa[9], fw[9];
#pragma unroll
    for (int q = 0; q < 9; ++q) { fa[q] = a_frag(sm + q * 4096, lane, rg, kh); fw[q] = w_frag(sW + q * 8192, lane, kh, sub); }
    __builtin_amdgcn_sched_barrier(0);
    if (sub == 1) {
#pragma unroll
      for (int q = 0; q < 9; ++q) acc = MFMA16(fa[q], fw[q], acc);
    } else {
#pragma unroll
      for (int q = 0; q < 9; ++q) acc = MFMA16(fw[q], fa[q], acc);
    }
  }
  f32x4* ex = (f32x4*)(sm + 114688 + (sub * 2 + rg) * 1024) + lane;
  if (kh) *ex = acc;
  RAW_BARRIER();
  if (!kh) {
    acc += *ex;
    const int tw = t0 + rg * 16, t = tw + tl;
    const int img = (tw >> 7) * SG_BYTES, vimg = img + SG_V + ((tw >> 4) & 7) * 640;
    const int tile = (tw >> 5) & 3, r = t & 31;
    const unsigned p01 = min(pack2(acc[0], acc[1]), O_EMPTY - 1u), p23 = min(pack2(acc[2], acc[3]), O_EMPTY - 1u);
    if (sub == 0) {
      __builtin_amdgcn_raw_buffer_store_b64(u32x2{p01, p23}, ir, img + SG_K1 + tile * 1024 + (r + 32 * (g >> 1)) * 16 + (g & 1) * 8, 0, 16);
    } else if (sub == 1) {
      __builtin_amdgcn_raw_buffer_store_b64(u32x2{p01, p23}, ir, vimg + (g >> 1) * 320 + (g & 1) * 8 + vpos(tl) * 16, 0, 16);
    } else {
      const unsigned nb = __shfl_xor(p01, 1);
      const unsigned pv = min((tl & 1) ? (nb >> 16) | (p01 & 0xffff0000u) : (p01 & 0xffffu) | (nb << 16), O_EMPTY - 1u);
      if (g < 2)
        __builtin_amdgcn_raw_buffer_store_b32(g ? pv : p01, ir,
            g ? vimg + (tl >> 3) * 320 + (16 + (tl & 1)) * 16 + (tl & 6) * 2 : img + SG_K2 + r * 16 + tile * 4, 0, 16);
      if (g == 0) {
        const float lg = rbf(acc[2]);
        __hip_atomic_store((GLB_AS float*)(ws + WS_G) + t, rbf(__builtin_amdgcn_rcpf(1.0f + __expf(-lg))), __ATOMIC_RELAXED, __HIP_MEMORY_SCOPE_AGENT);
      }
    }
    wait_vm<0>();
  }
  RAW_BARRIER();
  if (tid < 8) flag_set(ws, FL_S + tid * 512 + si * 8, lds_tag(sm));
}

__device__ __forceinline__ void u_tile(const u16* __restrict__ bk, const u16* __restrict__ bv, const u16* __restrict__ shk,
                                       char* ws, char* sm, int grp, int j, int tid, int wave, int lane) {
  constexpr int UP = 776, UB = 98304;
  const int tA = grp * 256, tB = tA + (j & 1) * 128, cB = 6 + (j >> 1);
  {
    const bool sec = wave >= 8;
    const int cg = tid & 7, st = (tid >> 3) & 63;
    const int c0 = (sec ? cB : j) * 64 + cg * 8, ts = (sec ? tB : tA) + st * 4;
    u32x4 kr[7], vr[4], sk[4];
#pragma unroll
    for (int i = 0; i < 7; ++i) kr[i] = *(const u32x4*)(bk + (size_t)max(ts - 3 + i, 0) * DM + c0);
#pragma unroll
    for (int i = 0; i < 4; ++i) vr[i] = *(const u32x4*)(bv + (size_t)(ts + i) * DM + c0);
#pragma unroll
    for (int i = 0; i < 4; ++i) sk[i] = *(const u32x4*)(shk + c0 * 4 + i * 8);
    if (ts == 0) { kr[0] = u32x4{0u, 0u, 0u, 0u}; kr[1] = kr[0]; kr[2] = kr[0]; }
#pragma unroll
    for (int c = 0; c < 8; ++c) {
      const unsigned w01 = sk[c >> 1][(c & 1) * 2], w23 = sk[c >> 1][(c & 1) * 2 + 1];
      const float w0 = bflo(w01), w1 = bfhi(w01), w2 = bflo(w23), w3 = bfhi(w23);
      float kk[7], o[4];
#pragma unroll
      for (int i = 0; i < 7; ++i) kk[i] = (c & 1) ? bfhi(kr[i][c >> 1]) : bflo(kr[i][c >> 1]);
#pragma unroll
      for (int i = 0; i < 4; ++i) {
        const float a = kk[i] * w3 + kk[i + 1] * w2 + kk[i + 2] * w1 + kk[i + 3] * w0;
        o[i] = a * ((c & 1) ? bfhi(vr[i][c >> 1]) : bflo(vr[i][c >> 1]));
      }
      *(u32x2*)(sm + UB + (cg * 8 + c) * UP + ((sec ? 380 : 252) - st * 4) * 2) = u32x2{pack2(o[3], o[2]), pack2(o[1], o[0])};
    }
  }
  RAW_BARRIER();
}
__device__ __forceinline__ void u_store(char* ws, char* sm, int grp, int j, int wave, int lane) {
  constexpr int UP = 776, UB = 98304;
  const int tA = grp * 256, tB = tA + (j & 1) * 128, cB = 6 + (j >> 1);
  const __amdgpu_buffer_rsrc_t ur = __builtin_amdgcn_make_buffer_rsrc((void*)(ws + WS_UR), 0, DM * S_LEN * 2, 0x00020000);
  u32x4 v[12];
#pragma unroll
  for (int i = 0; i < 12; ++i) {
    const int row = (wave - 8) * 16 + (i < 8 ? i * 2 + (lane >> 5) : (i - 8) * 4 + (lane >> 4));
    const char* sp = sm + UB + row * UP + (i < 8 ? (lane & 31) * 16 : 512 + (lane & 15) * 16);
    const u32x2 lo = *(const u32x2*)sp, hi = *(const u32x2*)(sp + 8);
    v[i] = u32x4{lo[0], lo[1], hi[0], hi[1]};
  }
#pragma unroll
  for (int i = 0; i < 12; ++i) {
    const int row = (wave - 8) * 16 + (i < 8 ? i * 2 + (lane >> 5) : (i - 8) * 4 + (lane >> 4));
    const int off = i < 8 ? (j * 64 + row) * S_LEN + (S_LEN - 256 - tA) + (lane & 31) * 8 : (cB * 64 + row) * S_LEN + (S_LEN - 128 - tB) + (lane & 15) * 8;
    __builtin_amdgcn_raw_buffer_store_b128(v[i], ur, off * 2, 0, 16);
  }
}
__device__ __forceinline__ void u_publish(char* ws, char* sm, int grp, int j, int lane) {
  wait_vm<0>();
  unsigned n = 0;
  if (lane == 0) n = __hip_atomic_fetch_add((LDS_AS unsigned*)(sm + T_SYNC) + 9, 1u, __ATOMIC_RELAXED, __HIP_MEMORY_SCOPE_WORKGROUP);
  if (__builtin_amdgcn_readfirstlane(n) == 3u && lane < 3)
    flag_set(ws, FL_U + (lane < 2 ? j * 128 + (grp * 2 + lane) * 8 : (6 + (j >> 1)) * 128 + (grp * 2 + (j & 1)) * 8), lds_tag(sm));
}

__device__ __forceinline__ void cnt_add(char* ws, int byte_off) {
  __hip_atomic_fetch_add((GLB_AS unsigned*)(ws + WS_CNT + byte_off), 1u, __ATOMIC_RELAXED, __HIP_MEMORY_SCOPE_AGENT);
}
template <int SLP>
__device__ __forceinline__ void cnt_wait(char* ws, int byte_off, unsigned need) {
  GLB_AS unsigned* cp = (GLB_AS unsigned*)(ws + WS_CNT + byte_off);
#pragma unroll 1
  for (int spin = 0; spin < (1 << 22); ++spin) {
    const unsigned v = __hip_atomic_load(cp, __ATOMIC_RELAXED, __HIP_MEMORY_SCOPE_AGENT);
    if (__all(v >= need)) break;
    __builtin_amdgcn_s_sleep(SLP);
  }
  asm volatile("" ::: "memory");
}

#define SSM_HI 0
#define SSM_PRIO 1
#define LATE_PRIO 0
#define PS_AUX 16
#define SSM_PACE() do {} while (0)
template <int N0>
__device__ __forceinline__ void ssm_unit(int c, const u16* __restrict__ hk, const u16* __restrict__ dk,
                                         char* ws, int lane) {
  constexpr int NM = N0 + 4;
  const int i = lane & 15, g = lane >> 4;
  const __amdgpu_buffer_rsrc_t ur = __builtin_amdgcn_make_buffer_rsrc((void*)(ws + WS_PS + (size_t)(c * 2 + (N0 ? 1 : 0)) * (PS_COPY * 4)), 0,
                                                                      PS_COPY * 4, 0x00020000);
  int ao = (((i & 8) ? 0 : PS_COPY) + 2030 - 256 * (NM - 1) - 2 * (i & 7) + 8 * g) * 2;
  const __amdgpu_buffer_rsrc_t hr = __builtin_amdgcn_make_buffer_rsrc((void*)(hk + (size_t)c * S_LEN), 0, S_LEN * 2, 0x00020000);
  int bo = (16 * i - 16 + 8 * g) * 2, bo1 = bo + 512;
  asm volatile("" : "+v"(bo1), "+v"(ao));
  f32x4 acc[4];
#pragma unroll
  for (int n = 0; n < 4; ++n) acc[n] = f32x4{0.f, 0.f, 0.f, 0.f};
  u32x4 A[NM], B[NM];
  auto ldA = [&](int m) { A[m] = __builtin_amdgcn_raw_buffer_load_b128(ur, ao, 512 * (NM - 1 - m), 0); };
  auto ldB = [&](int j) {
    if (j == 0) B[0] = __builtin_amdgcn_raw_buffer_load_b128(hr, bo, 0, 0);
    else B[j] = __builtin_amdgcn_raw_buffer_load_b128(hr, bo1, 512 * (j - 1), 0);
  };
#pragma unroll
  for (int j = 0; j < NM; ++j) ldB(j);
  const float dd = bflo(dk[c]);
  wait_vm<0>();
  const unsigned tch = __builtin_amdgcn_raw_buffer_load_b32(ur, (lane >> 5) * (PS_COPY * 2) + (N0 ? (lane & 31) * 128 : 2048 + (lane & 31) * 64), 0, 0);
#pragma unroll
  for (int m = 0; m < NM; ++m) ldA(m);
  u32x2 u[4];
#pragma unroll
  for (int n = 0; n < 4; ++n) {
    int uo = (2044 - (256 * (N0 + n) + 16 * i + 8 * (g & 1) + 4 * (g >> 1))) * 2;
    asm volatile("" : "+v"(uo));
    u[n] = __builtin_amdgcn_raw_buffer_load_b64(ur, uo, 0, 0);
  }
  asm volatile("" :: "v"(tch));
  if (N0) {
#pragma unroll 1
    for (int rho = 0; rho < 8; ++rho) {
      if (rho == SSM_HI) __builtin_amdgcn_s_setprio(SSM_PRIO);
      if (rho < 7) { ao -= 64; bo -= 64; bo1 -= 64; asm volatile("" : "+v"(ao), "+v"(bo), "+v"(bo1)); }
#pragma unroll
      for (int m = 0; m < NM; ++m) {
#pragma unroll
        for (int n = 3; n >= 0; --n)
          if (N0 + n - m >= 0) {
            acc[n] = MFMA16(__builtin_bit_cast(bf16x8, A[m]), __builtin_bit_cast(bf16x8, B[N0 + n - m]), acc[n]);
            SSM_PACE();
          }
        __builtin_amdgcn_sched_barrier(0);
        ldA(m); ldB(NM - 1 - m);
        __builtin_amdgcn_sched_barrier(0);
      }
    }
  } else {
    u32x4 A2[NM], B2[NM];
    auto round = [&](u32x4 (&Ac)[NM], u32x4 (&Bc)[NM], u32x4 (&An)[NM], u32x4 (&Bn)[NM]) {
#pragma unroll
      for (int m = 0; m < NM; ++m) {
        An[m] = __builtin_amdgcn_raw_buffer_load_b128(ur, ao, 512 * (NM - 1 - m), 0);
        Bn[m] = m == 0 ? __builtin_amdgcn_raw_buffer_load_b128(hr, bo, 0, 0) : __builtin_amdgcn_raw_buffer_load_b128(hr, bo1, 512 * (m - 1), 0);
      }
      __builtin_amdgcn_sched_barrier(0);
#pragma unroll
      for (int m = 0; m < NM; ++m)
#pragma unroll
        for (int n = 3; n >= m; --n) {
          acc[n] = MFMA16(__builtin_bit_cast(bf16x8, Ac[m]), __builtin_bit_cast(bf16x8, Bc[n - m]), acc[n]);
          SSM_PACE();
        }
      __builtin_amdgcn_sched_barrier(0);
    };
#pragma unroll 1
    for (int rho = 0; rho < 8; rho += 2) {
      if (rho == SSM_HI) __builtin_amdgcn_s_setprio(SSM_PRIO);
      ao -= 64; bo -= 64; bo1 -= 64; asm volatile("" : "+v"(ao), "+v"(bo), "+v"(bo1));
      round(A, B, A2, B2);
      if (rho < 6) { ao -= 64; bo -= 64; bo1 -= 64; asm volatile("" : "+v"(ao), "+v"(bo), "+v"(bo1)); }
      round(A2, B2, A, B);
    }
  }
  __builtin_amdgcn_s_setprio(3);
  const __amdgpu_buffer_rsrc_t zr = __builtin_amdgcn_make_buffer_rsrc((void*)(ws + WS_Z + (size_t)c * (S_LEN * 2)), 0, S_LEN * 2, 0x00020000);
#pragma unroll
  for (int n = 0; n < 4; ++n) {
    const int tq = 256 * (N0 + n) + 16 * i + 8 * (g & 1) + 4 * (g >> 1);
    const auto s02 = __builtin_amdgcn_permlane32_swap(__float_as_uint(acc[n][0]), __float_as_uint(acc[n][2]), false, false);
    const auto s13 = __builtin_amdgcn_permlane32_swap(__float_as_uint(acc[n][1]), __float_as_uint(acc[n][3]), false, false);
    const float z0 = __uint_as_float(s02[0]) + bfhi(u[n][1]) * dd;
    const float z1 = __uint_as_float(s02[1]) + bflo(u[n][1]) * dd;
    const float z2 = __uint_as_float(s13[0]) + bfhi(u[n][0]) * dd;
    const float z3 = __uint_as_float(s13[1]) + bflo(u[n][0]) * dd;
    __builtin_amdgcn_raw_buffer_store_b64(u32x2{pack2(z0, z1), pack2(z2, z3)}, zr, tq * 2, 0, 16);
  }
  wait_vm<0>();
  if (lane == 0) cnt_add(ws, (CNT_Z + (c >> 6) * 2 + (N0 ? 1 : 0)) * 128);
}

__device__ __forceinline__ f32x16 qk(bf16x8 k1, s16x4 k2, bf16x8 q1, s16x4 q2) {
  f32x16 z;
#pragma unroll
  for (int i = 0; i < 16; ++i) z[i] = 0.f;
  f32x16 s = __builtin_amdgcn_mfma_f32_32x32x16_bf16(k1, q1, z, 0, 0, 0);
  return __builtin_amdgcn_mfma_f32_32x32x8bf16_1k(k2, q2, s, 0, 0, 0);
}
__device__ __forceinline__ float max16(f32x16 s) {
  float m = fmaxf(s[0], s[1]);
#pragma unroll
  for (int i = 2; i < 16; ++i) m = fmaxf(m, s[i]);
  return m;
}
__device__ __forceinline__ s16x4 mk2(unsigned a, unsigned b) { return __builtin_bit_cast(s16x4, u32x2{a, b}); }
__device__ __forceinline__ float swap32_max(float m) {
  const auto s = __builtin_amdgcn_permlane32_swap(__float_as_uint(m), __float_as_uint(m), false, false);
  return fmaxf(__uint_as_float(s[0]), __uint_as_float(s[1]));
}

#define ATTN_CLOB "v96","v97","v98","v99","v100","v101","v102","v103","v104","v105","v106","v107","v108","v109","v110","v111", \
  "v112","v113","v114","v115","v116","v117","v118","v119","v120","v121","v122","v123","v124","v125","v126","v127", \
  "v128","v129","v130","v131","v132","v133","v134","v135","v136","v137","v138","v139","v140","v141","v142","v143"
#define EX1(a) "v_exp_f32 v" #a ", v" #a "\n"
#define EX2(a, b) EX1(a) EX1(b)
#define EX3(a, b, c) EX1(a) EX1(b) EX1(c)
#define EX4(a, b, c, d) EX1(a) EX1(b) EX1(c) EX1(d)
#define ATILE(OVHI, OK1N, K2C, QB2, K2N, QA2, OVLON, X3, X4, X6, X7) \
  EX2(96, 97) \
  "v_smfmac_f32_32x32x32_bf16 %[ob], v[140:143], v[120:127], %[idx]\n" \
  "ds_read_b128 v[140:143], %[av] offset:" OVHI "\n" \
  EX3(98, 99, 100) \
  "v_mfma_f32_32x32x8_bf16 v[112:127], " K2C ", %[" QB2 "], 0\n" \
  "s_waitcnt lgkmcnt(1)\n" \
  EX4(101, 102, 103, 104) \
  "v_mfma_f32_32x32x16_bf16 v[112:127], v[128:131], %[qb1], v[112:127]\n" \
  "ds_read_b128 v[128:131], %[ak1] offset:" OK1N "\n" \
  X3 \
  EX4(105, 106, 107, 108) \
  "v_smfmac_f32_32x32x32_bf16 %[oa], v[136:139], v[96:103], %[idx]\n" \
  X4 \
  EX3(109, 110, 111) \
  EX2(112, 113) \
  "v_smfmac_f32_32x32x32_bf16 %[oa], v[140:143], v[104:111], %[idx]\n" \
  "s_waitcnt lgkmcnt(0)\n" \
  EX3(114, 115, 116) \
  "v_mfma_f32_32x32x8_bf16 v[96:111], " K2N ", %[" QA2 "], 0\n" \
  X6 \
  EX4(117, 118, 119, 120) \
  "v_mfma_f32_32x32x16_bf16 v[96:111], v[128:131], %[qa1], v[96:111]\n" \
  X7 \
  EX4(121, 122, 123, 124) \
  "v_smfmac_f32_32x32x32_bf16 %[ob], v[136:139], v[112:119], %[idx]\n" \
  "ds_read_b128 v[136:139], %[av] offset:" OVLON "\n" \
  EX3(125, 126, 127)
#define ITSTAMP
#define ITCLOB
#define WL2 "s_waitcnt lgkmcnt(2)\n"
#define WL1 "s_waitcnt lgkmcnt(1)\n"
#define SPIN(T) "s_cbranch_scc0 L_attn_go" T "_%=\n L_attn_spin" T "_%=:\n ds_read_b32 %[tmp], %[syn]\n s_add_i32 %[chk], %[chk], 16\n s_waitcnt lgkmcnt(0)\n" \
  " v_readfirstlane_b32 %[stmp], %[tmp]\n s_cmp_gt_u32 %[chk], 0x10000000\n s_cbranch_scc1 L_attn_go" T "_%=\n s_cmp_lt_u32 %[stmp], 4\n" \
  " s_cbranch_scc1 L_attn_spin" T "_%=\n L_attn_go" T "_%=:\n"
#define ASG(O, A6, A7, B6, B7) \
  ATILE(O "+640", O "+1024", "v[132:133]", "qb2e", "v[132:133]", "qa2o", O "+1280", "", WL1, "", "") \
  ATILE(O "+1920", O "+2048", "v[132:133]", "qb2o", "v[134:135]", "qa2e", O "+2560", "", WL1, "", "") \
  ATILE(O "+3200", O "+3072", "v[134:135]", "qb2e", "v[134:135]", "qa2o", O "+3840", "", WL1, A6, A7) \
  ATILE(O "+4480", O "+9744", "v[134:135]", "qb2o", "v[132:133]", "qa2e", O "+9744", \
        "ds_read_b128 v[132:135], %[ak2] offset:" O "+9744\n", WL2, B6, B7)
#define ATTN_LOOP_ASM \
  "ds_read_b128 v[128:131], %[ak1]\n" \
  "ds_read_b128 v[132:135], %[ak2]\n" \
  "ds_read_b128 v[136:139], %[av]\n" \
  "v_mov_b32 v140, 0\n v_mov_b32 v141, 0\n v_mov_b32 v142, 0\n v_mov_b32 v143, 0\n" \
  "v_mov_b32 v120, 0\n v_mov_b32 v121, 0\n v_mov_b32 v122, 0\n v_mov_b32 v123, 0\n" \
  "v_mov_b32 v124, 0\n v_mov_b32 v125, 0\n v_mov_b32 v126, 0\n v_mov_b32 v127, 0\n" \
  "s_waitcnt lgkmcnt(1)\n" \
  "v_mfma_f32_32x32x16_bf16 v[96:111], v[128:131], %[qa1], 0\n" \
  "v_mfma_f32_32x32x8_bf16 v[96:111], v[132:133], %[qa2e], v[96:111]\n" \
  "s_nop 11\n" \
  "L_attn_loop_%=:\n" ITSTAMP \
  ASG("0", "s_cmp_eq_u32 %[it], %[chk]\n", SPIN("a"), "s_add_i32 %[it], %[it], 1\n", "") \
  ASG("9744", "s_cmp_eq_u32 %[it], %[chk]\n", SPIN("b"), "s_add_i32 %[it], %[it], 1\n", "s_cmp_lt_i32 %[it], %[nit]\n") \
  "v_add_u32 %[ak1], 19488, %[ak1]\n" \
  "v_add_u32 %[ak2], 19488, %[ak2]\n" \
  "v_add_u32 %[av], 19488, %[av]\n" \
  "s_cbranch_scc1 L_attn_loop_%=\n" \
  "v_smfmac_f32_32x32x32_bf16 %[ob], v[140:143], v[120:127], %[idx]\n" \
  "s_waitcnt lgkmcnt(0)\n" \
  "s_nop 15\n"

__device__ __forceinline__ f32x16 attn_exact(__amdgpu_buffer_rsrc_t ir, int offK1, int offK2, int offV, int hh, bf16x8 q1, unsigned q2) {
  const s16x4 x2 = mk2(hh ? 0u : q2, 0u);
  float m = -INFINITY;
#pragma unroll 1
  for (int tile = 0; tile < 64; ++tile) {
    const int base = (tile >> 2) * SG_BYTES;
    const s16x4 kk = mk2(__builtin_amdgcn_raw_buffer_load_b32(ir, base + offK2 + (hh ? 0 : (tile & 3) * 4), 0, 16), 0u);
    const bf16x8 k1 = __builtin_bit_cast(bf16x8, __builtin_amdgcn_raw_buffer_load_b128(ir, base + offK1 + (tile & 3) * 1024, 0, 16));
    m = fmaxf(m, max16(qk(k1, kk, q1, x2)));
  }
  m = swap32_max(m);
  f32x16 O;
#pragma unroll
  for (int i = 0; i < 16; ++i) O[i] = 0.f;
#pragma unroll 1
  for (int tile = 0; tile < 64; ++tile) {
    const int base = (tile >> 2) * SG_BYTES;
    const s16x4 kk = mk2(__builtin_amdgcn_raw_buffer_load_b32(ir, base + offK2 + (hh ? 0 : (tile & 3) * 4), 0, 16), 0u);
    const bf16x8 k1 = __builtin_bit_cast(bf16x8, __builtin_amdgcn_raw_buffer_load_b128(ir, base + offK1 + (tile & 3) * 1024, 0, 16));
    const bf16x8 vlo = __builtin_bit_cast(bf16x8, __builtin_amdgcn_raw_buffer_load_b128(ir, base + offV + (tile & 3) * 1280, 0, 16));
    const bf16x8 vhi = __builtin_bit_cast(bf16x8, __builtin_amdgcn_raw_buffer_load_b128(ir, base + offV + (tile & 3) * 1280 + 640, 0, 16));
    f32x16 S = qk(k1, kk, q1, x2);
#pragma unroll
    for (int i = 0; i < 16; ++i) S[i] = __builtin_amdgcn_exp2f(S[i] - m);
    O = __builtin_amdgcn_smfmac_f32_32x32x32_bf16(vlo, __builtin_bit_cast(bf16x16, __builtin_shufflevector(S, S, 0, 1, 2, 3, 4, 5, 6, 7)), O, 0xDDDDDDDD, 0, 0);
    O = __builtin_amdgcn_smfmac_f32_32x32x32_bf16(vhi, __builtin_bit_cast(bf16x16, __builtin_shufflevector(S, S, 8, 9, 10, 11, 12, 13, 14, 15)), O, 0xDDDDDDDD, 0, 0);
  }
  return O;
}

#define EARLY_POST_PRIO 2
#define OUT_AUX 16
#define Q_SLEEP 4
#define KV_DELAY 0
#define O_SLEEP 4
#define I_SLEEP 6
#define NLATE 4
constexpr int T_QX = 16384;
constexpr int T_WO = 58624;
constexpr int T_XCH = T_WO + 73728;
constexpr int T_Z = 0;
constexpr int T_PART = 10240;
constexpr int T_EPI = 30720;
static_assert(NLATE % 2 == 0 && T_WO >= NLATE * SG_BYTES && T_XCH + 24576 <= T_SYNC && T_EPI + 12800 <= T_WO, "lds");
constexpr int P_EARLY = NLATE * SG_BYTES / 1024;
constexpr int NG_HI = (153 - (P_EARLY + 20) + 3) / 4;
static_assert(P_EARLY + 20 + NG_HI * 4 <= IMG_PIECES && 12 + (32 - NG_HI) * 4 >= P_EARLY && (P_EARLY + 20) * 1024 >= (NLATE + 2) * SG_BYTES, "dma plan");
static_assert(NG_HI >= 16 && T_QX >= 12288 && T_QX + 12288 <= P_EARLY * 1024, "q hand-over area");

__device__ __forceinline__ void img_group(const char* src, char* sm, int p) {
  const GLB_AS unsigned* g = (const GLB_AS unsigned*)(src + p * 1024);
  LDS_AS unsigned* l = (LDS_AS unsigned*)(sm + p * 1024);
  __builtin_amdgcn_global_load_lds(g, l, 16, 0, 16);
  __builtin_amdgcn_global_load_lds(g, l, 16, 1024, 16);
  __builtin_amdgcn_global_load_lds(g, l, 16, 2048, 16);
  __builtin_amdgcn_global_load_lds(g, l, 16, 3072, 16);
}

extern "C" __global__ void __launch_bounds__(768) k_main(const u16* __restrict__ x, const u16* __restrict__ wq,
                                               const u16* __restrict__ wk, const u16* __restrict__ wv,
                                               const u16* __restrict__ gw, const u16* __restrict__ bk,
                                               const u16* __restrict__ bv, const u16* __restrict__ shk, char* ws,
                                               const u16* __restrict__ hk, const u16* __restrict__ dk,
                                               const u16* __restrict__ wo, u16* __restrict__ out) {
  __shared__ __attribute__((aligned(256))) char sm[163840];
  const int tid = threadIdx.x, lane = tid & 63;
  const int wave = __builtin_amdgcn_readfirstlane(tid >> 6);
  const int b = blockIdx.x;
  const int grp = b & 7, loc = b >> 3;
  volatile LDS_AS unsigned* sync = (volatile LDS_AS unsigned*)(sm + T_SYNC);
  STAMP_DECL;
  STAMP(0);
  if (wave == 6 && loc < 26) code_prefetch(loc >= 18, lane);
  if (wave == 7) {
    const u64 e = __hip_atomic_load((GLB_AS u64*)(ws + WS_FLAG + FL_E), __ATOMIC_RELAXED, __HIP_MEMORY_SCOPE_AGENT) + 1u;
    if (lane < 16) sync[lane] = lane < 14 ? 0u : (lane == 14 ? (unsigned)e : (unsigned)(e >> 32));
  }
  if (loc < 18) q_tile(x, wq, ws, sm, grp, loc, wave, lane STAMP_PASS);
  else if (loc < 26) kvg_tile(x, wk, wv, gw, ws, sm, grp * 8 + loc - 18, tid, wave, lane);
  else { __builtin_amdgcn_s_sleep(KV_DELAY); u_tile(bk, bv, shk, ws, sm, grp, loc - 26, tid, wave, lane); }
  STAMP(13);

  const bool has_tile = loc < (grp < 2 ? 30 : 29);
  const int qid = has_tile ? grp * 29 + (grp < 2 ? grp : 2) + loc : 0;
  const int mt = qid / 9, nt = qid - mt * 9;
  const int t0 = mt * 80, n0 = nt * 64;
  const __amdgpu_buffer_rsrc_t orsrc = __builtin_amdgcn_make_buffer_rsrc((void*)(ws + WS_O), 0, (int)O_BYTES, 0x00020000);
  const u64 tag = lds_tag(sm);
  const int qb = ((grp + 4) & 7) * 32 + loc;
  const int cwi = wave - 2;
  const int kh = cwi >= 5 ? 1 : 0, cw = cwi - kh * 5;
  const int g = lane >> 4, tl = lane & 15;
  u32x2 xv[2]; float gt;
  const int te = min(t0 + max(cw, 0) * 16 + tl, S_LEN - 1);
  auto load_x = [&]() {
#pragma unroll
    for (int h = 0; h < 2; ++h) xv[h] = *(const u32x2*)(x + (size_t)te * DM + n0 + kh * 32 + h * 16 + g * 4);
    asm volatile("" ::: "memory");
  };
  auto load_g = [&]() {
    gt = __uint_as_float(__builtin_amdgcn_raw_buffer_load_b32(__builtin_amdgcn_make_buffer_rsrc((void*)(ws + WS_G), 0, S_LEN * 4, 0x00020000), te * 4, 0, 16));
    asm volatile("" ::: "memory");
  };

  if (wave < 4) {
    __builtin_amdgcn_s_setprio(2);
    const int W = b < 208 ? b + 208 * wave : 4 * b + wave;
    lds_wait(sync + 8, 1u);
    STAMP(14);
    const char* src = ws + WS_IMG + lane * 16;
    {
      const int pa = P_EARLY + wave * 4, pb = wave == 0 ? P_EARLY + 16 : wave * 4 - 4;
#pragma unroll 1
      for (int spin = 0; spin < (1 << 20); ++spin) {
        img_group(src, sm, pa);
        img_group(src, sm, pb);
        wait_vm<0>();
        unsigned mx = 0;
#pragma unroll
        for (int i = 0; i < 8; ++i) {
          const u32x4 v = *(const u32x4*)(sm + ((i < 4 ? pa : pb - 4) + i) * 1024 + lane * 16);
          mx = max(max(mx, max(v[0], v[1])), max(v[2], v[3]));
        }
        if (__all(mx != O_EMPTY)) break;
        __builtin_amdgcn_s_sleep(I_SLEEP);
      }
    }
    __builtin_amdgcn_s_setprio(3);
    RAW_BARRIER();
    if (wave == 0 && __builtin_amdgcn_readfirstlane(sync[7]) == 0u) { flag_wait<2>(ws, FL_S + grp * 512 + lane * 8, tag); if (lane == 0) sync[7] = 1u; }
    else lds_wait(sync + 7, 1u);
#pragma unroll
    for (int i = 0; i < 8; ++i) {
      if (i == 4) lds_wait(sync + 10, 4u);
      int p = P_EARLY + 20 + (wave + 4 * i) * 4;
      p = p > 152 ? p - (P_EARLY + 20 + NG_HI * 4) + 12 : p;
      img_group(src, sm, p);
    }
    wait_vm<0>();
    if (lane == 0) __hip_atomic_fetch_add((LDS_AS unsigned*)sync + 5, 1u, __ATOMIC_RELAXED, __HIP_MEMORY_SCOPE_WORKGROUP);
    {
      const int c1 = W < 576 ? W : W - 576, c2 = (W >= 576 && W < 576 + 128) ? W - 128 : c1;
      flag_wait<8>(ws, FL_U + (((lane & 16) ? c2 : c1) >> 6) * 128 + (lane & 15) * 8, tag);
      const __amdgpu_buffer_rsrc_t us = __builtin_amdgcn_make_buffer_rsrc((void*)(ws + WS_UR), 0, DM * S_LEN * 2, 0x00020000);
      const __amdgpu_buffer_rsrc_t up = __builtin_amdgcn_make_buffer_rsrc((void*)(ws + WS_PS), 0, (int)(WS_G - WS_PS), 0x00020000);
      u32x4 ud[4]; unsigned un[4]; int upo[4];
#pragma unroll
      for (int q = 0; q < 4; ++q) {
        const int cc = q < 2 ? c1 : c2, blk = W < 576 ? q : 2 + (q & 1);
        const int uo = cc * (S_LEN * 2) + blk * 1024 + lane * 16;
        upo[q] = (cc * 2 + (W < 576 ? 1 : 0)) * (PS_COPY * 4) + blk * 1024 + lane * 16;
        ud[q] = __builtin_amdgcn_raw_buffer_load_b128(us, uo, 0, 16);
        un[q] = __builtin_amdgcn_raw_buffer_load_b32(us, uo + 16, 0, 16);
        if (blk == 3 && lane == 63) un[q] = 0u;
      }
#pragma unroll
      for (int q = 0; q < 4; ++q) {
        const u32x4 sh = {__builtin_amdgcn_alignbit(ud[q][1], ud[q][0], 16), __builtin_amdgcn_alignbit(ud[q][2], ud[q][1], 16),
                          __builtin_amdgcn_alignbit(ud[q][3], ud[q][2], 16), __builtin_amdgcn_alignbit(un[q], ud[q][3], 16)};
        __builtin_amdgcn_raw_buffer_store_b128(ud[q], up, upo[q], 0, PS_AUX);
        __builtin_amdgcn_raw_buffer_store_b128(sh, up, upo[q] + PS_COPY * 2, 0, PS_AUX);
        if ((q & 1) && lane < 4 && (q == 3 || W >= 576))
          __builtin_amdgcn_raw_buffer_store_b128(u32x4{0u, 0u, 0u, 0u}, up, upo[q] + 1024 + (lane >> 1) * (PS_COPY * 2 - 32), 0, PS_AUX);
      }
    }
    STAMP(1);
    STAMP(9);
    if (W < 576) {
      ssm_unit<4>(W, hk, dk, ws, lane);
    } else {
      for (int c = W - 576; c < 576; c += 448) ssm_unit<0>(c, hk, dk, ws, lane);
    }
    if (b == 0 && wave == 1) {
      cnt_wait<8>(ws, (CNT_F + min(lane, 25)) * 128, lane >= 25 ? 6u : 10u);
      if (lane == 0) flag_set(ws, FL_E, tag + 1u);
    }
    load_x(); load_g();
    wait_vm<0>();
    STAMP(2);
  } else {
    const int pair = wave & 3;
    const bool early = wave < 8;
    __builtin_amdgcn_s_setprio(2);
    const int r = lane & 31, hh = lane >> 5;
    const int tq = qb * 8 + pair * 2;
    const int offK1 = SG_K1 + lane * 16;
    const int offK2 = SG_K2 + (hh ? 512 : r * 16);
    const int offV = SG_V + hh * 320 + vpos(r) * 16;
    const int idx = 0xDDDDDDDD;
    const unsigned ldsBase = (unsigned)(uintptr_t)(LDS_AS char*)sm + (early ? NLATE * SG_BYTES : 0);

    const __amdgpu_buffer_rsrc_t irsrc = __builtin_amdgcn_make_buffer_rsrc((void*)(ws + WS_IMG), 0, IMG_BYTES, 0x00020000);
    bf16x8 qa1, qb1; unsigned qa2, qb2;
    f32x16 Oa, Ob;
    {
#pragma unroll
      for (int i = 0; i < 16; ++i) { Oa[i] = 0.f; Ob[i] = 0.f; }
      load_x();
      unsigned aK1 = ldsBase + offK1, aK2 = ldsBase + offK2, aV = ldsBase + offV;
      asm volatile("" : "+v"(Oa), "+v"(Ob), "+v"(aK1), "+v"(aK2), "+v"(aV) :: "memory");
      const __amdgpu_buffer_rsrc_t qr = __builtin_amdgcn_make_buffer_rsrc((void*)(ws + WS_Q + (size_t)tq * (DM * 2)), 0, DM * 4, 0x00020000);
      bf16x8 k1; unsigned k2;
      auto load_q = [&]() {
#pragma unroll 1
        for (int spin = 0; spin < (1 << 22); ++spin) {
          asm volatile("" ::: "memory");
          const u32x4 ta = __builtin_amdgcn_raw_buffer_load_b128(qr, r * 36 + hh * 16, 0, 16);
          const u32x4 tb = __builtin_amdgcn_raw_buffer_load_b128(qr, r * 36 + hh * 16 + DM * 2, 0, 16);
          qa2 = __builtin_amdgcn_raw_buffer_load_b32(qr, r * 36 + 32, 0, 16);
          qb2 = __builtin_amdgcn_raw_buffer_load_b32(qr, r * 36 + 32 + DM * 2, 0, 16);
          const u32x4 tk = __builtin_amdgcn_raw_buffer_load_b128(irsrc, offK1, 0, 16);
          k2 = __builtin_amdgcn_raw_buffer_load_b32(irsrc, offK2, 0, 16);
          qa1 = __builtin_bit_cast(bf16x8, ta); qb1 = __builtin_bit_cast(bf16x8, tb); k1 = __builtin_bit_cast(bf16x8, tk);
          const unsigned mq = max(max(max(max(tk[0], tk[1]), max(tk[2], tk[3])), k2), max(max(max(max(ta[0], ta[1]), max(ta[2], ta[3])), max(max(tb[0], tb[1]), max(tb[2], tb[3]))), max(qa2, qb2)));
          if (__all(mq != O_EMPTY)) break;
          __builtin_amdgcn_s_sleep(Q_SLEEP);
        }
      };
      u32x4* qx = (u32x4*)(sm + T_QX + pair * 3072) + lane;
      unsigned pm;
      if (early) {
        if (wave == 5) {
#pragma unroll
          for (int i = 0; i < 9; ++i)
            __builtin_amdgcn_raw_buffer_store_b128(u32x4{O_EMPTY, O_EMPTY, O_EMPTY, O_EMPTY}, orsrc,
                                                   ((qb >> 1) * 72 + i * 8 + (lane >> 3)) * 256 + (qb & 1) * 128 + (lane & 7) * 16, 0, 16);
        }
        if (wave >= 6) {
          flag_wait<4>(ws, wave == 6 ? FL_M + grp * 512 + lane * 8 : FL_Q + (qb >> 4) * 128 + min(lane, 8) * 8, tag);
          if (lane == 0) sync[wave == 6 ? 8 : 11] = 1u;
        }
        lds_wait(sync + 8, 1u);
        lds_wait(sync + 11, 1u);
        STAMP(14);
        load_q();
        if (lane == 0) __hip_atomic_fetch_add((LDS_AS unsigned*)sync + 12, 1u, __ATOMIC_RELAXED, __HIP_MEMORY_SCOPE_WORKGROUP);
        if (wave == 5) { wait_vm<0>(); if (lane == 0) cnt_add(ws, (CNT_F + qb / 10) * 128); }
        const float ma = swap32_max(max16(qk(k1, mk2(k2, 0u), qa1, mk2(hh ? 0u : qa2, 0u))));
        const float mb = swap32_max(max16(qk(k1, mk2(k2, 0u), qb1, mk2(hh ? 0u : qb2, 0u))));
        pm = pack2(-ma, -mb);
        qx[0] = __builtin_bit_cast(u32x4, qa1); qx[64] = __builtin_bit_cast(u32x4, qb1); qx[128] = u32x4{qa2, qb2, pm, 0u};
        STAMP(9);
        RAW_BARRIER();
      } else {
        __builtin_amdgcn_s_setprio(3);
        if (loc >= 26) { u_store(ws, sm, grp, loc - 26, wave, lane); u_publish(ws, sm, grp, loc - 26, lane); }
        if (wave == 8) {
          GLB_AS u64* fp = (GLB_AS u64*)(ws + WS_FLAG + FL_S + grp * 512 + lane * 8);
#pragma unroll 1
          for (int spin = 0; spin < (1 << 22) && __builtin_amdgcn_readfirstlane(sync[12]) == 0u; ++spin) {
            if (__all(__hip_atomic_load(fp, __ATOMIC_RELAXED, __HIP_MEMORY_SCOPE_AGENT) == tag)) { if (lane == 0) sync[7] = 1u; break; }
            __builtin_amdgcn_s_sleep(4);
          }
        }
        RAW_BARRIER();
        qa1 = __builtin_bit_cast(bf16x8, qx[0]); qb1 = __builtin_bit_cast(bf16x8, qx[64]);
        const u32x4 e = qx[128];
        qa2 = e[0]; qb2 = e[1]; pm = e[2];
        WAIT_LGKM0();
        if (lane == 0) __hip_atomic_fetch_add((LDS_AS unsigned*)sync + 10, 1u, __ATOMIC_RELAXED, __HIP_MEMORY_SCOPE_WORKGROUP);
        __builtin_amdgcn_s_setprio(LATE_PRIO);
      }
      STAMP(1); CSTAMP(10);
      const unsigned xa = hh ? 0u : qa2, xb = hh ? 0u : qb2;
      const unsigned nma = pm & 0xffffu, nmb = pm >> 16;
      const s16x4 qa2e = mk2(hh ? nma : qa2, 0u), qa2o = mk2(hh ? nma : 0u, xa);
      const s16x4 qb2e = mk2(hh ? nmb : qb2, 0u), qb2o = mk2(hh ? nmb : 0u, xb);
      int it = 0;
      const int nit = early ? 16 - NLATE : NLATE;
      int chk = 2 - (wave >> 2), stmp;
      unsigned tmp;
      const unsigned syn = (unsigned)(uintptr_t)(sync + 5);
      asm volatile(ATTN_LOOP_ASM
                   : [oa] "+v"(Oa), [ob] "+v"(Ob), [ak1] "+v"(aK1), [ak2] "+v"(aK2), [av] "+v"(aV), [it] "+s"(it),
                     [chk] "+s"(chk), [stmp] "=&s"(stmp), [tmp] "=&v"(tmp)
                   : [qa1] "v"(qa1), [qb1] "v"(qb1), [qa2e] "v"(qa2e), [qa2o] "v"(qa2o), [qb2e] "v"(qb2e), [qb2o] "v"(qb2o),
                     [idx] "v"(idx), [nit] "s"(nit), [syn] "v"(syn)
                   : "memory", "scc", ATTN_CLOB ITCLOB);
    }
    STAMP(2); CSTAMP(11);
    load_g();
    f32x4* xch = (f32x4*)(sm + T_XCH + pair * 6144) + lane;
    if (early) {
      __builtin_amdgcn_s_setprio(EARLY_POST_PRIO);
      if (lane == 0) __hip_atomic_fetch_add((LDS_AS unsigned*)sync, 1u, __ATOMIC_RELAXED, __HIP_MEMORY_SCOPE_WORKGROUP);
      lds_wait(sync, 4u);
#pragma unroll
      for (int i = 0; i < 3; ++i) {
        xch[i * 64] = f32x4{Oa[4 * i], Oa[4 * i + 1], Oa[4 * i + 2], Oa[4 * i + 3]};
        xch[(3 + i) * 64] = f32x4{Ob[4 * i], Ob[4 * i + 1], Ob[4 * i + 2], Ob[4 * i + 3]};
      }
      WAIT_LGKM0();
      if (lane == 0) sync[1 + pair] = 1u;
      const char* wsrc = (const char*)(wo + n0) + (size_t)(pair * 144 + (lane >> 3)) * (DM * 2);
      const int c0 = ((lane & 7) ^ (wsw(lane >> 3) << 1)) << 4;
#pragma unroll
      for (int i = 0; i < 18; ++i) glds16(wsrc + i * 8 * DM * 2 + (c0 ^ ((i & 1) << 6)), sm + T_WO + pair * 18432 + i * 1024);
      if (wave == 4) cnt_wait<8>(ws, (CNT_F + mt) * 128, mt == 25 ? 6u : 10u);
      wait_vm<0>();
    } else {
      __builtin_amdgcn_s_setprio(3);
      lds_wait(sync + 1 + pair, 1u);
      float big = 0.f;
#pragma unroll
      for (int i = 0; i < 3; ++i) {
        const f32x4 pa = xch[i * 64], pb = xch[(3 + i) * 64];
#pragma unroll
        for (int e = 0; e < 4; ++e) if (4 * i + e < 11) {
          Oa[4 * i + e] += pa[e]; Ob[4 * i + e] += pb[e];
          big += fabsf(Oa[4 * i + e]) + fabsf(Ob[4 * i + e]);
        }
      }
      if (__any(!(big < 1e30f) || (!hh && !(fminf(Oa[10], Ob[10]) > 1e-12f)))) {
        Oa = attn_exact(irsrc, offK1, offK2, offV, hh, qa1, qa2);
        Ob = attn_exact(irsrc, offK1, offK2, offV, hh, qb1, qb2);
      }
      STAMP(3);
      const auto la = __builtin_amdgcn_permlane32_swap(__float_as_uint(Oa[10]), __float_as_uint(Oa[10]), false, false);
      const auto lb = __builtin_amdgcn_permlane32_swap(__float_as_uint(Ob[10]), __float_as_uint(Ob[10]), false, false);
      const float ia = __builtin_amdgcn_rcpf(__uint_as_float(la[0])), ib = __builtin_amdgcn_rcpf(__uint_as_float(lb[0]));
      char* ost = sm + T_XCH + pair * 6144;
      char* od = ost + r * 36 + hh * 8;
      *(unsigned*)(od) = pack2(Oa[0] * ia, Oa[1] * ia);        *(unsigned*)(od + 1168) = pack2(Ob[0] * ib, Ob[1] * ib);
      *(unsigned*)(od + 4) = pack2(Oa[2] * ia, Oa[3] * ia);    *(unsigned*)(od + 1172) = pack2(Ob[2] * ib, Ob[3] * ib);
      *(unsigned*)(od + 16) = pack2(Oa[4] * ia, Oa[5] * ia);   *(unsigned*)(od + 1184) = pack2(Ob[4] * ib, Ob[5] * ib);
      *(unsigned*)(od + 20) = pack2(Oa[6] * ia, Oa[7] * ia);   *(unsigned*)(od + 1188) = pack2(Ob[6] * ib, Ob[7] * ib);
      if (!hh) { *(unsigned*)(od + 32) = pack2(Oa[8] * ia, Oa[9] * ia); *(unsigned*)(od + 1200) = pack2(Ob[8] * ib, Ob[9] * ib); }
      u32x4 ov[3];
#pragma unroll
      for (int i = 0; i < 3; ++i) ov[i] = *(const u32x4*)(ost + (lane & 1) * 1168 + (i * 32 + (lane >> 1)) * 16);
#pragma unroll
      for (int i = 0; i < 3; ++i) {
#pragma unroll
        for (int e = 0; e < 4; ++e) ov[i][e] = min(ov[i][e], O_EMPTY - 1u);
        if (i < 2 || lane < 16)
          __builtin_amdgcn_raw_buffer_store_b128(ov[i], orsrc, (((tq >> 4) * 72 + i * 32 + (lane >> 1)) * 16 + (tq & 15) + (lane & 1)) * 16, 0, 16);
      }
    }
  }
  STAMP(4);
  if (!has_tile) { STAMP_FLUSH(1); return; }

  RAW_BARRIER();
  STAMP(5);
  if (wave < 2) {
    cnt_wait<2>(ws, (CNT_Z + nt * 2 + (((lane & 1) ? t0 : t0 + 79) < 1024 ? 0 : 1)) * 128, 64u);
    const __amdgpu_buffer_rsrc_t zrsrc = __builtin_amdgcn_make_buffer_rsrc((void*)(ws + WS_Z), 0, (int)Z_BYTES, 0x00020000);
    u32x4 zv[5];
#pragma unroll
    for (int i = 0; i < 5; ++i) {
      const int ci = (wave * 5 + i) * 64 + lane, c = ci / 10, k = ci - c * 10;
      zv[i] = __builtin_amdgcn_raw_buffer_load_b128(zrsrc, ((n0 + c) * S_LEN + t0 + k * 8) * 2, 0, 16);
    }
#pragma unroll
    for (int i = 0; i < 5; ++i) *(u32x4*)(sm + T_Z + ((wave * 5 + i) * 64 + lane) * 16) = zv[i];
    WAIT_LGKM0();
  } else {
    float bx[2][4];
    const float omg = 1.0f - gt;
#pragma unroll
    for (int h = 0; h < 2; ++h) {
      bx[h][0] = omg * bflo(xv[h][0]); bx[h][1] = omg * bfhi(xv[h][0]);
      bx[h][2] = omg * bflo(xv[h][1]); bx[h][3] = omg * bfhi(xv[h][1]);
    }
    const int rgp = mt * 5 + cw;
    const int obase = ((rgp * 18 + kh * 9) * 64 + lane) * 16;
    const char* wl = sm + T_WO + kh * 9 * 4096;
    constexpr int NPRE = 5;
    bf16x8 wpre[NPRE][4];
    int so[9];
#pragma unroll
    for (int s = 0; s < 9; ++s) so[s] = s + nt >= 9 ? s + nt - 9 : s + nt;
#pragma unroll
    for (int s = 0; s < NPRE; ++s)
#pragma unroll
      for (int nb = 0; nb < 4; ++nb) wpre[s][nb] = w_frag(wl + so[9 - NPRE + s] * 4096, lane, 0, nb);
    unsigned wq4[4];
#pragma unroll
    for (int nb = 0; nb < 4; ++nb) {
      const int k = g * 8 + (tl >> 2);
      wq4[nb] = (unsigned)(uintptr_t)(LDS_AS char*)sm + T_WO + kh * 9 * 4096 + k * 128 + ((nb ^ wsw(k)) << 5) + (lane & 3) * 8;
    }
    STAMP(6);
    f32x4 acc[4];
#pragma unroll 1
    for (int spin = 0; spin < (1 << 20); ++spin) {
      asm volatile("" : "+v"(wq4[0]), "+v"(wq4[1]), "+v"(wq4[2]), "+v"(wq4[3]) :: "memory");
      u32x4 of[9];
#pragma unroll
      for (int s = 0; s < 9; ++s) of[s] = __builtin_amdgcn_raw_buffer_load_b128(orsrc, obase, so[s] * 1024, 16);
      __builtin_amdgcn_sched_barrier(0);
#pragma unroll
      for (int nb = 0; nb < 4; ++nb) acc[nb] = f32x4{0.f, 0.f, 0.f, 0.f};
      unsigned mx = 0;
#pragma unroll
      for (int s = 0; s < 9; ++s) {
#pragma unroll
        for (int nb = 0; nb < 4; ++nb) {
          bf16x8 wf;
          if (s >= 9 - NPRE) wf = wpre[s - (9 - NPRE)][nb];
          else {
            LDS_AS bf16x4* wp = (LDS_AS bf16x4*)(uintptr_t)(wq4[nb] + so[s] * 4096);
            wf = __builtin_shufflevector(__builtin_amdgcn_ds_read_tr16_b64_v4bf16(wp), __builtin_amdgcn_ds_read_tr16_b64_v4bf16(wp + 64), 0, 1, 2, 3, 4, 5, 6, 7);
          }
          acc[nb] = MFMA16(wf, __builtin_bit_cast(bf16x8, of[s]), acc[nb]);
        }
        mx = max(max(mx, max(of[s][0], of[s][1])), max(of[s][2], of[s][3]));
      }
      if (__all(mx != O_EMPTY)) break;
      __builtin_amdgcn_s_sleep(O_SLEEP);
    }
    f32x4* pd = (f32x4*)(sm + T_PART + cwi * 2048) + lane;
    pd[0] = kh ? acc[0] : acc[2];
    pd[64] = kh ? acc[1] : acc[3];
    STAMP(7);
    RAW_BARRIER();
    const f32x4* ps = (const f32x4*)(sm + T_PART + (kh ? cw : cw + 5) * 2048) + lane;
    char* sE = sm + T_EPI + cwi * 1280;
    f32x4 pa[2]; u32x2 z[2];
#pragma unroll
    for (int h = 0; h < 2; ++h) {
      pa[h] = ps[h * 64];
      z[h] = __builtin_bit_cast(u32x2, tr_read(sm + T_Z + ((kh * 2 + h) * 16 + g * 4 + (tl >> 2)) * 160 + (cw * 16 + (tl & 3) * 4) * 2));
    }
    asm volatile("" ::: "memory");
#pragma unroll
    for (int h = 0; h < 2; ++h) {
      const f32x4 a = (kh ? acc[2 + h] : acc[h]) + pa[h];
      *(u32x2*)(sE + tl * 80 + h * 32 + g * 8) =
          u32x2{pack2(gt * a[0] + bx[h][0] * bflo(z[h][0]), gt * a[1] + bx[h][1] * bfhi(z[h][0])),
                pack2(gt * a[2] + bx[h][2] * bflo(z[h][1]), gt * a[3] + bx[h][3] * bfhi(z[h][1]))};
    }
    const u32x4 o = *(const u32x4*)(sE + (lane >> 2) * 80 + (lane & 3) * 16);
    const __amdgpu_buffer_rsrc_t outr = __builtin_amdgcn_make_buffer_rsrc((void*)out, 0, S_LEN * DM * 2, 0x00020000);
    __builtin_amdgcn_raw_buffer_store_b128(o, outr, ((t0 + cw * 16 + (lane >> 2)) * DM + n0 + kh * 32 + (lane & 3) * 8) * 2, 0, OUT_AUX);
  }
  STAMP(8);
  STAMP_FLUSH(1);
}

extern "C" __global__ void k9_end() {}

extern "C" void kernel_launch(void* const* d_in, const int* in_sizes, int n_in, void* d_out, int out_size,
                              void* d_ws, size_t ws_size, hipStream_t stream) {
  const u16* x = (const u16*)d_in[0];
  const u16* wq = (const u16*)d_in[1];
  const u16* wk = (const u16*)d_in[2];
  const u16* wv = (const u16*)d_in[3];
  const u16* wo = (const u16*)d_in[4];
  const u16* bk = (const u16*)d_in[6];
  const u16* bv = (const u16*)d_in[7];
  const u16* shk = (const u16*)d_in[8];
  const u16* hk = (const u16*)d_in[9];
  const u16* dk = (const u16*)d_in[10];
  const u16* gw = (const u16*)d_in[11];
  char* ws = (char*)d_ws;
  k_main<<<256, 768, 0, stream>>>(x, wq, wk, wv, gw, bk, bv, shk, ws, hk, dk, wo, (u16*)d_out);
}
```

```cpp
#include <hip/hip_runtime.h>
#include <hip/hip_bf16.h>
#include <stdint.h>

typedef __attribute__((ext_vector_type(4))) __bf16 bf16x4;
typedef __attribute__((ext_vector_type(8))) __bf16 bf16x8;
typedef __attribute__((ext_vector_type(16))) __bf16 bf16x16;
typedef __attribute__((ext_vector_type(4))) short s16x4;
typedef __attribute__((ext_vector_type(4))) float f32x4;
typedef __attribute__((ext_vector_type(16))) float f32x16;
typedef __attribute__((ext_vector_type(2))) unsigned u32x2;
typedef __attribute__((ext_vector_type(4))) unsigned u32x4;
typedef unsigned short u16;
typedef __attribute__((ext_vector_type(4), aligned(4))) unsigned u32x4_a4;

#define LDS_AS __attribute__((address_space(3)))
#define GLB_AS __attribute__((address_space(1)))

constexpr int S_LEN = 2048, DM = 576, DH = 18;
constexpr int PS_COPY = 2112;
constexpr int SG_K1 = 0, SG_K2 = 4096, SG_V = 4624, SG_BYTES = 9744;
constexpr int IMG_PIECES = 156;
constexpr int IMG_BYTES = IMG_PIECES * 1024;

constexpr size_t WS_CNT = 0;
constexpr size_t WS_FLAG = 8192;
constexpr int FL_E = 0, FL_Q = 128, FL_S = FL_Q + 16 * 128, FL_M = FL_S + 8 * 512, FL_U = FL_M + 8 * 512;
constexpr size_t WS_Q = 24576;
constexpr size_t WS_IMG = WS_Q + (size_t)S_LEN * DM * 2;
constexpr size_t WS_UR = WS_IMG + IMG_BYTES;
constexpr size_t WS_PS = WS_UR + (size_t)DM * S_LEN * 2;
constexpr size_t WS_G = WS_PS + (size_t)DM * 4 * PS_COPY * 2;
constexpr size_t WS_O = WS_G + S_LEN * 4;
constexpr size_t O_BYTES = (size_t)S_LEN * DM * 2;
constexpr size_t WS_Z = WS_O + O_BYTES;
constexpr size_t Z_BYTES = (size_t)S_LEN * DM * 2;
constexpr int CNT_Z = 0, CNT_F = 18;
constexpr unsigned O_EMPTY = 0xffffffffu;

#define STAMP_DECL do {} while (0)
#define STAMP_ARG
#define STAMP_PASS
#define STAMP(i) do {} while (0)
#define CSTAMP(i) do {} while (0)
#define STAMP_FLUSH(kern) do {} while (0)
#define WAIT_LGKM0() do { asm volatile("" ::: "memory"); __builtin_amdgcn_s_waitcnt(0xC07F); asm volatile("" ::: "memory"); } while (0)
#define RAW_BARRIER() do { WAIT_LGKM0(); __builtin_amdgcn_s_barrier(); asm volatile("" ::: "memory"); } while (0)
template <int N> __device__ __forceinline__ void wait_vm() {
  asm volatile("s_waitcnt vmcnt(%0)" :: "i"(N) : "memory");
  if (N == 0) __builtin_amdgcn_s_waitcnt(0x0070);
}

__device__ __forceinline__ float bflo(unsigned w) { return __uint_as_float(w << 16); }
__device__ __forceinline__ float bfhi(unsigned w) { return __uint_as_float(w & 0xffff0000u); }
__device__ __forceinline__ unsigned pack2(float lo, float hi) {
  typedef __attribute__((ext_vector_type(2))) __bf16 bf16x2;
  bf16x2 v; v[0] = (__bf16)lo; v[1] = (__bf16)hi;
  return __builtin_bit_cast(unsigned, v);
}
__device__ __forceinline__ float rbf(float x) { return (float)(__bf16)x; }
__device__ __forceinline__ void glds16(const void* g, void* lds) {
  __builtin_amdgcn_global_load_lds((const GLB_AS unsigned*)g, (LDS_AS unsigned*)lds, 16, 0, 0);
}
__device__ __forceinline__ bf16x4 tr_read(const char* p) {
  return __builtin_amdgcn_ds_read_tr16_b64_v4bf16((LDS_AS bf16x4*)p);
}
__device__ __forceinline__ int vpos(int d) {
  const int a = d - (d > 11 && d < 16);
  return d > 18 ? 15 : (d == 11 ? 19 : a);
}
#define MFMA16(a, b, c) __builtin_amdgcn_mfma_f32_16x16x32_bf16(a, b, c, 0, 0, 0)

__device__ __forceinline__ int wsw(int k) { return ((k >> 1) & 1) | ((k >> 2) & 2); }
__device__ __forceinline__ bf16x8 a_frag(const char* sA, int lane, int rg, int s) {
  const int row = rg * 16 + (lane & 15);
  return *(const bf16x8*)(sA + row * 128 + (((s * 4 + (lane >> 4)) ^ ((row >> 1) & 7)) << 4));
}
__device__ __forceinline__ bf16x8 w_frag(const char* sW, int lane, int s, int nb) {
  const int k = s * 32 + (lane >> 4) * 8 + ((lane & 15) >> 2);
  const char* b = sW + k * 128 + ((nb ^ wsw(k)) << 5) + (lane & 3) * 8;
  const bf16x4 lo = tr_read(b), hi = tr_read(b + 512);
  return __builtin_shufflevector(lo, hi, 0, 1, 2, 3, 4, 5, 6, 7);
}

template <int NP, int NSLOT, int SLOT, int DEPTH>
__device__ __forceinline__ void ring_loader(const char* const (&base)[NP], const int (&step)[NP], char* const (&dst)[NP], int rot) {
  auto issue = [&](int j) {
    int ch = j + rot; ch = ch >= 9 ? ch - 9 : ch;
#pragma unroll
    for (int i = 0; i < NP; ++i) glds16(base[i] + ch * step[i], dst[i] + ((j + 3) % NSLOT) * SLOT);
  };
#pragma unroll
  for (int j = 0; j < DEPTH; ++j) issue(j);
#define RL_STEP(j) do { wait_vm<NP * ((8 - (j)) < (DEPTH - 1) ? (8 - (j)) : (DEPTH - 1))>(); __builtin_amdgcn_s_barrier(); \
    asm volatile("" ::: "memory"); if ((j) + DEPTH < 9) issue((j) + DEPTH); } while (0)
  RL_STEP(0); RL_STEP(1); RL_STEP(2); RL_STEP(3); RL_STEP(4); RL_STEP(5); RL_STEP(6); RL_STEP(7); RL_STEP(8);
#undef RL_STEP
}

#define K1_DEPTH 3
__device__ __forceinline__ void lds_wait(volatile LDS_AS unsigned* p, unsigned need) {
#pragma unroll 1
  for (int spin = 0; spin < (1 << 24); ++spin) {
    if (__builtin_amdgcn_readfirstlane(*p) >= need) break;
    __builtin_amdgcn_s_sleep(2);
  }
  asm volatile("" ::: "memory");
}

typedef unsigned long long u64;
__device__ __forceinline__ void flag_set(char* ws, int off, u64 tag) {
  __hip_atomic_store((GLB_AS u64*)(ws + WS_FLAG + off), tag, __ATOMIC_RELAXED, __HIP_MEMORY_SCOPE_AGENT);
}
template <int SLP>
__device__ __forceinline__ void flag_wait(char* ws, int off, u64 tag) {
  GLB_AS u64* fp = (GLB_AS u64*)(ws + WS_FLAG + off);
#pragma unroll 1
  for (int spin = 0; spin < (1 << 22); ++spin) {
    const u64 v = __hip_atomic_load(fp, __ATOMIC_RELAXED, __HIP_MEMORY_SCOPE_AGENT);
    if (__all(v == tag)) break;
    __builtin_amdgcn_s_sleep(SLP);
  }
  asm volatile("" ::: "memory");
}
__device__ __forceinline__ void code_prefetch(bool all, int lane) {
  u64 pc; unsigned d;
  asm volatile("s_getpc_b64 %0\nL_pf_%=:\n s_mov_b32 %1, k9_end-L_pf_%=" : "=s"(pc), "=s"(d));
  unsigned o[4], t[4];
#pragma unroll
  for (int i = 0; i < 4; ++i) o[i] = min((unsigned)(lane * 128 + i * 8192), all ? d - 4u : 12288u);
  asm volatile("global_load_dword %0, %4, %8\n global_load_dword %1, %5, %8\n global_load_dword %2, %6, %8\n"
               " global_load_dword %3, %7, %8\n s_waitcnt vmcnt(0)"
               : "=&v"(t[0]), "=&v"(t[1]), "=&v"(t[2]), "=&v"(t[3])
               : "v"(o[0]), "v"(o[1]), "v"(o[2]), "v"(o[3]), "s"(pc) : "memory");
}
constexpr int T_SYNC = 163840 - 64;
__device__ __forceinline__ u64 lds_tag(char* sm) { return *(volatile LDS_AS u64*)(sm + T_SYNC + 56); }

__device__ __forceinline__ void q_tile(const u16* __restrict__ x, const u16* __restrict__ wq, char* ws, char* sm,
                                       int grp, int loc, int wave, int lane STAMP_ARG) {
  const int g = lane >> 4, tl = lane & 15;
  const int mh = loc >= 9 ? 1 : 0, nt = loc - mh * 9;
  const int t0 = (grp * 2 + mh) * 128, n0 = nt * 64;
  constexpr int SLOT = 24576, NSLOT = 6;
  const __amdgpu_buffer_rsrc_t qr = __builtin_amdgcn_make_buffer_rsrc((void*)(ws + WS_Q), 0, S_LEN * DM * 2, 0x00020000);
  if (wave < 6) {
    int rot = nt + 4 * mh + grp; rot = rot >= 18 ? rot - 18 : (rot >= 9 ? rot - 9 : rot);
    const char* base[4]; int step[4]; char* dst[4];
#pragma unroll
    for (int i = 0; i < 4; ++i) {
      const int pc = wave + 6 * i;
      const bool isW = pc < 8;
      const int row = (isW ? pc : pc - 8) * 8 + (lane >> 3);
      const int sw = isW ? wsw(row) << 1 : (row >> 1) & 7;
      base[i] = (isW ? (const char*)(wq + n0) : (const char*)(x + (size_t)t0 * DM)) + (unsigned)row * (DM * 2) + (((lane & 7) ^ sw) << 4);
      step[i] = isW ? 64 * DM * 2 : 128;
      dst[i] = sm + pc * 1024;
    }
    ring_loader<4, NSLOT, SLOT, K1_DEPTH>(base, step, dst, rot);
  } else if (wave < 8) {
    RAW_BARRIER();
    if (wave == 7) {
#pragma unroll
      for (int i = 0; i < 4; ++i) {
#pragma unroll
        for (int k = 0; k < 4; ++k)
          __builtin_amdgcn_raw_buffer_store_b128(u32x4{O_EMPTY, O_EMPTY, O_EMPTY, O_EMPTY}, qr, ((t0 + (i * 4 + k) * 8 + (lane >> 3)) * DM + n0 + (lane & 7) * 8) * 2, 0, 16);
        RAW_BARRIER();
      }
      RAW_BARRIER(); RAW_BARRIER(); RAW_BARRIER();
      wait_vm<0>();
      if (lane == 0) flag_set(ws, FL_Q + (grp * 2 + mh) * 128 + nt * 8, lds_tag(sm));
      RAW_BARRIER();
    } else {
#pragma unroll
      for (int j = 1; j < 9; ++j) RAW_BARRIER();
    }
  } else {
    const int cwv = wave - 8;
    __builtin_amdgcn_s_setprio(3);
    f32x4 acc[2][4];
#pragma unroll
    for (int i = 0; i < 2; ++i)
#pragma unroll
      for (int nb = 0; nb < 4; ++nb) acc[i][nb] = f32x4{0.f, 0.f, 0.f, 0.f};
    bf16x8 af[2][4], wf[2][8];
    auto rd = [&](int j, bf16x8 (&a)[4], bf16x8 (&w)[8]) {
      const char* sl = sm + (j >= 3 ? j - 3 : j + 3) * SLOT;
#pragma unroll
      for (int s = 0; s < 2; ++s) {
#pragma unroll
        for (int i = 0; i < 2; ++i) a[s * 2 + i] = a_frag(sl + 8192, lane, cwv * 2 + i, s);
#pragma unroll
        for (int nb = 0; nb < 4; ++nb) w[s * 4 + nb] = w_frag(sl, lane, s, nb);
      }
    };
    auto mm = [&](bf16x8 (&a)[4], bf16x8 (&w)[8]) {
#pragma unroll
      for (int s = 0; s < 2; ++s)
#pragma unroll
        for (int nb = 0; nb < 4; ++nb)
#pragma unroll
          for (int i = 0; i < 2; ++i) acc[i][nb] = MFMA16(w[s * 4 + nb], a[s * 2 + i], acc[i][nb]);
    };
    auto mix = [&]() {
#pragma unroll
      for (int i = 0; i < 10; ++i) { __builtin_amdgcn_sched_group_barrier(0x8, 1, 0); __builtin_amdgcn_sched_group_barrier(0x100, 2, 0); }
      __builtin_amdgcn_sched_group_barrier(0x8, 6, 0);
      __builtin_amdgcn_sched_barrier(0);
    };
    RAW_BARRIER();
    STAMP(12);
    rd(0, af[0], wf[0]);
#pragma unroll 1
    for (int j = 1; j < 9; j += 2) {
      RAW_BARRIER(); __builtin_amdgcn_sched_barrier(0); rd(j, af[1], wf[1]); mm(af[0], wf[0]); mix();
      RAW_BARRIER(); __builtin_amdgcn_sched_barrier(0); rd(j + 1, af[0], wf[0]); mm(af[1], wf[1]); mix();
    }
    mm(af[0], wf[0]);
    const float qscale = 0.23570226039551584f * 1.4426950408889634f;
    char* sE = sm + 4 * SLOT + cwv * 4608;
#pragma unroll
    for (int i = 0; i < 2; ++i)
#pragma unroll
      for (int nb = 0; nb < 4; ++nb) {
        const f32x4 a = acc[i][nb] * qscale;
        *(u32x2*)(sE + (i * 16 + tl) * 144 + nb * 32 + g * 8) =
            u32x2{min(pack2(a[0], a[1]), O_EMPTY - 1u), min(pack2(a[2], a[3]), O_EMPTY - 1u)};
      }
#pragma unroll
    for (int i = 0; i < 4; ++i) {
      const int row = i * 8 + (lane >> 3);
      const u32x4 v = *(const u32x4*)(sE + row * 144 + (lane & 7) * 16);
      __builtin_amdgcn_raw_buffer_store_b128(v, qr, ((t0 + cwv * 32 + row) * DM + n0 + (lane & 7) * 8) * 2, 0, 16);
    }
  }
}

__device__ __forceinline__ void kvg_tile(const u16* __restrict__ x, const u16* __restrict__ wk, const u16* __restrict__ wv,
                                         const u16* __restrict__ gw, char* ws, char* sm, int si, int tid, int wave, int lane) {
  const int g = lane >> 4, tl = lane & 15;
  const int t0 = si * 32;
  const __amdgpu_buffer_rsrc_t ir = __builtin_amdgcn_make_buffer_rsrc((void*)(ws + WS_IMG), 0, IMG_BYTES, 0x00020000);
  char* sW = sm + 36864;
  if (wave != 6 && wave != 7) {
    const int k = min(wave < 6 ? tid : tid - 128, 575);
    const u32x4 k0 = *(const u32x4_a4*)(wk + k * DH), k1 = *(const u32x4_a4*)(wk + k * DH + 8);
    const u32x4 v0 = *(const u32x4_a4*)(wv + k * DH), v1 = *(const u32x4_a4*)(wv + k * DH + 8);
    const unsigned k2 = *(const unsigned*)(wk + k * DH + 16), v2 = *(const unsigned*)(wv + k * DH + 16);
    const unsigned gk = gw[k];
    if (wave < 4) {
      const int row = wave * 8 + (lane >> 3);
      const char* xs = (const char*)x + (unsigned)(t0 + row) * (DM * 2) + (((lane & 7) ^ ((row >> 1) & 7)) << 4);
#pragma unroll
      for (int ch = 0; ch < 9; ++ch) glds16(xs + ch * 128, sm + ch * 4096 + wave * 1024);
    }
    char* d = sW + k * 128;
    const int sw = wsw(k);
    *(u32x4*)(d + (sw << 5)) = k0; *(u32x4*)(d + (sw << 5) + 16) = k1;
    *(u32x4*)(d + ((1 ^ sw) << 5)) = v0; *(u32x4*)(d + ((1 ^ sw) << 5) + 16) = v1;
    *(u32x4*)(d + ((2 ^ sw) << 5)) = u32x4{k2, gk, v2, 0u}; *(u32x4*)(d + ((2 ^ sw) << 5) + 16) = u32x4{0u, 0u, 0u, 0u};
  } else if (wave == 7) {
    const int img = (si >> 2) * SG_BYTES, tile = si & 3;
    __builtin_amdgcn_raw_buffer_store_b128(u32x4{O_EMPTY, O_EMPTY, O_EMPTY, O_EMPTY}, ir, img + SG_K1 + tile * 1024 + lane * 16, 0, 16);
#pragma unroll
    for (int i = 0; i < 2; ++i) {
      const int ch = i * 64 + lane, pos = ch % 20;
      const unsigned val = pos == 15 ? 0u : (pos == 18 ? 0x3f803f80u : O_EMPTY);
      if (ch < 80) __builtin_amdgcn_raw_buffer_store_b128(u32x4{val, val, val, val}, ir, img + SG_V + tile * 1280 + ch * 16, 0, 16);
    }
    if (lane < 33) __builtin_amdgcn_raw_buffer_store_b32(lane < 32 ? O_EMPTY : ((tile & 1) ? 0u : 0x3f80u), ir, img + SG_K2 + lane * 16 + tile * 4, 0, 16);
    if (si == 0) __hip_atomic_store((GLB_AS unsigned*)(ws + WS_CNT + lane * 128), 0u, __ATOMIC_RELAXED, __HIP_MEMORY_SCOPE_AGENT);
    wait_vm<0>();
    if (lane < 8) flag_set(ws, FL_M + lane * 512 + si * 8, lds_tag(sm));
  }
  if (wave != 7) wait_vm<0>();
  RAW_BARRIER();
  const int rg = wave & 1, kh = (wave >> 1) & 1, sub = wave >> 2;
  f32x4 acc = {0.f, 0.f, 0.f, 0.f};
  {
    bf16x8 fa[9], fw[9];
#pragma unroll
    for (int q = 0; q < 9; ++q) { fa[q] = a_frag(sm + q * 4096, lane, rg, kh); fw[q] = w_frag(sW + q * 8192, lane, kh, sub); }
    __builtin_amdgcn_sched_barrier(0);
    if (sub == 1) {
#pragma unroll
      for (int q = 0; q < 9; ++q) acc = MFMA16(fa[q], fw[q], acc);
    } else {
#pragma unroll
      for (int q = 0; q < 9; ++q) acc = MFMA16(fw[q], fa[q], acc);
    }
  }
  f32x4* ex = (f32x4*)(sm + 114688 + (sub * 2 + rg) * 1024) + lane;
  if (kh) *ex = acc;
  RAW_BARRIER();
  if (!kh) {
    acc += *ex;
    const int tw = t0 + rg * 16, t = tw + tl;
    const int img = (tw >> 7) * SG_BYTES, vimg = img + SG_V + ((tw >> 4) & 7) * 640;
    const int tile = (tw >> 5) & 3, r = t & 31;
    const unsigned p01 = min(pack2(acc[0], acc[1]), O_EMPTY - 1u), p23 = min(pack2(acc[2], acc[3]), O_EMPTY - 1u);
    if (sub == 0) {
      __builtin_amdgcn_raw_buffer_store_b64(u32x2{p01, p23}, ir, img + SG_K1 + tile * 1024 + (r + 32 * (g >> 1)) * 16 + (g & 1) * 8, 0, 16);
    } else if (sub == 1) {
      __builtin_amdgcn_raw_buffer_store_b64(u32x2{p01, p23}, ir, vimg + (g >> 1) * 320 + (g & 1) * 8 + vpos(tl) * 16, 0, 16);
    } else {
      const unsigned nb = __shfl_xor(p01, 1);
      const unsigned pv = min((tl & 1) ? (nb >> 16) | (p01 & 0xffff0000u) : (p01 & 0xffffu) | (nb << 16), O_EMPTY - 1u);
      if (g < 2)
        __builtin_amdgcn_raw_buffer_store_b32(g ? pv : p01, ir,
            g ? vimg + (tl >> 3) * 320 + (16 + (tl & 1)) * 16 + (tl & 6) * 2 : img + SG_K2 + r * 16 + tile * 4, 0, 16);
      if (g == 0) {
        const float lg = rbf(acc[2]);
        __hip_atomic_store((GLB_AS float*)(ws + WS_G) + t, rbf(__builtin_amdgcn_rcpf(1.0f + __expf(-lg))), __ATOMIC_RELAXED, __HIP_MEMORY_SCOPE_AGENT);
      }
    }
    wait_vm<0>();
  }
  RAW_BARRIER();
  if (tid < 8) flag_set(ws, FL_S + tid * 512 + si * 8, lds_tag(sm));
}

__device__ __forceinline__ void u_tile(const u16* __restrict__ bk, const u16* __restrict__ bv, const u16* __restrict__ shk,
                                       char* ws, char* sm, int grp, int j, int tid, int wave, int lane) {
  constexpr int UP = 776, UB = 98304;
  const int tA = grp * 256, tB = tA + (j & 1) * 128, cB = 6 + (j >> 1);
  {
    const bool sec = wave >= 8;
    const int cg = tid & 7, st = (tid >> 3) & 63;
    const int c0 = (sec ? cB : j) * 64 + cg * 8, ts = (sec ? tB : tA) + st * 4;
    u32x4 kr[7], vr[4], sk[4];
#pragma unroll
    for (int i = 0; i < 7; ++i) kr[i] = *(const u32x4*)(bk + (size_t)max(ts - 3 + i, 0) * DM + c0);
#pragma unroll
    for (int i = 0; i < 4; ++i) vr[i] = *(const u32x4*)(bv + (size_t)(ts + i) * DM + c0);
#pragma unroll
    for (int i = 0; i < 4; ++i) sk[i] = *(const u32x4*)(shk + c0 * 4 + i * 8);
    if (ts == 0) { kr[0] = u32x4{0u, 0u, 0u, 0u}; kr[1] = kr[0]; kr[2] = kr[0]; }
#pragma unroll
    for (int c = 0; c < 8; ++c) {
      const unsigned w01 = sk[c >> 1][(c & 1) * 2], w23 = sk[c >> 1][(c & 1) * 2 + 1];
      const float w0 = bflo(w01), w1 = bfhi(w01), w2 = bflo(w23), w3 = bfhi(w23);
      float kk[7], o[4];
#pragma unroll
      for (int i = 0; i < 7; ++i) kk[i] = (c & 1) ? bfhi(kr[i][c >> 1]) : bflo(kr[i][c >> 1]);
#pragma unroll
      for (int i = 0; i < 4; ++i) {
        const float a = kk[i] * w3 + kk[i + 1] * w2 + kk[i + 2] * w1 + kk[i + 3] * w0;
        o[i] = a * ((c & 1) ? bfhi(vr[i][c >> 1]) : bflo(vr[i][c >> 1]));
      }
      *(u32x2*)(sm + UB + (cg * 8 + c) * UP + ((sec ? 380 : 252) - st * 4) * 2) = u32x2{pack2(o[3], o[2]), pack2(o[1], o[0])};
    }
  }
  RAW_BARRIER();
}
__device__ __forceinline__ void u_store(char* ws, char* sm, int grp, int j, int wave, int lane) {
  constexpr int UP = 776, UB = 98304;
  const int tA = grp * 256, tB = tA + (j & 1) * 128, cB = 6 + (j >> 1);
  const __amdgpu_buffer_rsrc_t ur = __builtin_amdgcn_make_buffer_rsrc((void*)(ws + WS_UR), 0, DM * S_LEN * 2, 0x00020000);
  u32x4 v[12];
#pragma unroll
  for (int i = 0; i < 12; ++i) {
    const int row = (wave - 8) * 16 + (i < 8 ? i * 2 + (lane >> 5) : (i - 8) * 4 + (lane >> 4));
    const char* sp = sm + UB + row * UP + (i < 8 ? (lane & 31) * 16 : 512 + (lane & 15) * 16);
    const u32x2 lo = *(const u32x2*)sp, hi = *(const u32x2*)(sp + 8);
    v[i] = u32x4{lo[0], lo[1], hi[0], hi[1]};
  }
#pragma unroll
  for (int i = 0; i < 12; ++i) {
    const int row = (wave - 8) * 16 + (i < 8 ? i * 2 + (lane >> 5) : (i - 8) * 4 + (lane >> 4));
    const int off = i < 8 ? (j * 64 + row) * S_LEN + (S_LEN - 256 - tA) + (lane & 31) * 8 : (cB * 64 + row) * S_LEN + (S_LEN - 128 - tB) + (lane & 15) * 8;
    __builtin_amdgcn_raw_buffer_store_b128(v[i], ur, off * 2, 0, 16);
  }
}
__device__ __forceinline__ void u_publish(char* ws, char* sm, int grp, int j, int lane) {
  wait_vm<0>();
  unsigned n = 0;
  if (lane == 0) n = __hip_atomic_fetch_add((LDS_AS unsigned*)(sm + T_SYNC) + 9, 1u, __ATOMIC_RELAXED, __HIP_MEMORY_SCOPE_WORKGROUP);
  if (__builtin_amdgcn_readfirstlane(n) == 3u && lane < 3)
    flag_set(ws, FL_U + (lane < 2 ? j * 128 + (grp * 2 + lane) * 8 : (6 + (j >> 1)) * 128 + (grp * 2 + (j & 1)) * 8), lds_tag(sm));
}

__device__ __forceinline__ void cnt_add(char* ws, int byte_off) {
  __hip_atomic_fetch_add((GLB_AS unsigned*)(ws + WS_CNT + byte_off), 1u, __ATOMIC_RELAXED, __HIP_MEMORY_SCOPE_AGENT);
}
template <int SLP>
__device__ __forceinline__ void cnt_wait(char* ws, int byte_off, unsigned need) {
  GLB_AS unsigned* cp = (GLB_AS unsigned*)(ws + WS_CNT + byte_off);
#pragma unroll 1
  for (int spin = 0; spin < (1 << 22); ++spin) {
    const unsigned v = __hip_atomic_load(cp, __ATOMIC_RELAXED, __HIP_MEMORY_SCOPE_AGENT);
    if (__all(v >= need)) break;
    __builtin_amdgcn_s_sleep(SLP);
  }
  asm volatile("" ::: "memory");
}

#define SSM_HI 0
#define SSM_PRIO 1
#define LATE_PRIO 0
#define PS_AUX 16
#define SSM_PACE() do {} while (0)
template <int N0>
__device__ __forceinline__ void ssm_unit(int c, const u16* __restrict__ hk, const u16* __restrict__ dk,
                                         char* ws, int lane) {
  constexpr int NM = N0 + 4;
  const int i = lane & 15, g = lane >> 4;
  const __amdgpu_buffer_rsrc_t ur = __builtin_amdgcn_make_buffer_rsrc((void*)(ws + WS_PS + (size_t)(c * 2 + (N0 ? 1 : 0)) * (PS_COPY * 4)), 0,
                                                                      PS_COPY * 4, 0x00020000);
  int ao = (((i & 8) ? 0 : PS_COPY) + 2030 - 256 * (NM - 1) - 2 * (i & 7) + 8 * g) * 2;
  const __amdgpu_buffer_rsrc_t hr = __builtin_amdgcn_make_buffer_rsrc((void*)(hk + (size_t)c * S_LEN), 0, S_LEN * 2, 0x00020000);
  int bo = (16 * i - 16 + 8 * g) * 2, bo1 = bo + 512;
  asm volatile("" : "+v"(bo1), "+v"(ao));
  f32x4 acc[4];
#pragma unroll
  for (int n = 0; n < 4; ++n) acc[n] = f32x4{0.f, 0.f, 0.f, 0.f};
  u32x4 A[NM], B[NM];
  auto ldA = [&](int m) { A[m] = __builtin_amdgcn_raw_buffer_load_b128(ur, ao, 512 * (NM - 1 - m), 0); };
  auto ldB = [&](int j) {
    if (j == 0) B[0] = __builtin_amdgcn_raw_buffer_load_b128(hr, bo, 0, 0);
    else B[j] = __builtin_amdgcn_raw_buffer_load_b128(hr, bo1, 512 * (j - 1), 0);
  };
#pragma unroll
  for (int j = 0; j < NM; ++j) ldB(j);
  const float dd = bflo(dk[c]);
  wait_vm<0>();
  const unsigned tch = __builtin_amdgcn_raw_buffer_load_b32(ur, (lane >> 5) * (PS_COPY * 2) + (N0 ? (lane & 31) * 128 : 2048 + (lane & 31) * 64), 0, 0);
#pragma unroll
  for (int m = 0; m < NM; ++m) ldA(m);
  u32x2 u[4];
#pragma unroll
  for (int n = 0; n < 4; ++n) {
    int uo = (2044 - (256 * (N0 + n) + 16 * i + 8 * (g & 1) + 4 * (g >> 1))) * 2;
    asm volatile("" : "+v"(uo));
    u[n] = __builtin_amdgcn_raw_buffer_load_b64(ur, uo, 0, 0);
  }
  asm volatile("" :: "v"(tch));
  if (N0) {
#pragma unroll 1
    for (int rho = 0; rho < 8; ++rho) {
      if (rho == SSM_HI) __builtin_amdgcn_s_setprio(SSM_PRIO);
      if (rho < 7) { ao -= 64; bo -= 64; bo1 -= 64; asm volatile("" : "+v"(ao), "+v"(bo), "+v"(bo1)); }
#pragma unroll
      for (int m = 0; m < NM; ++m) {
#pragma unroll
        for (int n = 3; n >= 0; --n)
          if (N0 + n - m >= 0) {
            acc[n] = MFMA16(__builtin_bit_cast(bf16x8, A[m]), __builtin_bit_cast(bf16x8, B[N0 + n - m]), acc[n]);
            SSM_PACE();
          }
        __builtin_amdgcn_sched_barrier(0);
        ldA(m); ldB(NM - 1 - m);
        __builtin_amdgcn_sched_barrier(0);
      }
    }
  } else {
    u32x4 A2[NM], B2[NM];
    auto round = [&](u32x4 (&Ac)[NM], u32x4 (&Bc)[NM], u32x4 (&An)[NM], u32x4 (&Bn)[NM]) {
#pragma unroll
      for (int m = 0; m < NM; ++m) {
        An[m] = __builtin_amdgcn_raw_buffer_load_b128(ur, ao, 512 * (NM - 1 - m), 0);
        Bn[m] = m == 0 ? __builtin_amdgcn_raw_buffer_load_b128(hr, bo, 0, 0) : __builtin_amdgcn_raw_buffer_load_b128(hr, bo1, 512 * (m - 1), 0);
      }
      __builtin_amdgcn_sched_barrier(0);
#pragma unroll
      for (int m = 0; m < NM; ++m)
#pragma unroll
        for (int n = 3; n >= m; --n) {
          acc[n] = MFMA16(__builtin_bit_cast(bf16x8, Ac[m]), __builtin_bit_cast(bf16x8, Bc[n - m]), acc[n]);
          SSM_PACE();
        }
      __builtin_amdgcn_sched_barrier(0);
    };
#pragma unroll 1
    for (int rho = 0; rho < 8; rho += 2) {
      if (rho == SSM_HI) __builtin_amdgcn_s_setprio(SSM_PRIO);
      ao -= 64; bo -= 64; bo1 -= 64; asm volatile("" : "+v"(ao), "+v"(bo), "+v"(bo1));
      round(A, B, A2, B2);
      if (rho < 6) { ao -= 64; bo -= 64; bo1 -= 64; asm volatile("" : "+v"(ao), "+v"(bo), "+v"(bo1)); }
      round(A2, B2, A, B);
    }
  }
  __builtin_amdgcn_s_setprio(3);
  const __amdgpu_buffer_rsrc_t zr = __builtin_amdgcn_make_buffer_rsrc((void*)(ws + WS_Z + (size_t)c * (S_LEN * 2)), 0, S_LEN * 2, 0x00020000);
#pragma unroll
  for (int n = 0; n < 4; ++n) {
    const int tq = 256 * (N0 + n) + 16 * i + 8 * (g & 1) + 4 * (g >> 1);
    const auto s02 = __builtin_amdgcn_permlane32_swap(__float_as_uint(acc[n][0]), __float_as_uint(acc[n][2]), false, false);
    const auto s13 = __builtin_amdgcn_permlane32_swap(__float_as_uint(acc[n][1]), __float_as_uint(acc[n][3]), false, false);
    const float z0 = __uint_as_float(s02[0]) + bfhi(u[n][1]) * dd;
    const float z1 = __uint_as_float(s02[1]) + bflo(u[n][1]) * dd;
    const float z2 = __uint_as_float(s13[0]) + bfhi(u[n][0]) * dd;
    const float z3 = __uint_as_float(s13[1]) + bflo(u[n][0]) * dd;
    __builtin_amdgcn_raw_buffer_store_b64(u32x2{pack2(z0, z1), pack2(z2, z3)}, zr, tq * 2, 0, 16);
  }
  wait_vm<0>();
  if (lane == 0) cnt_add(ws, (CNT_Z + (c >> 6) * 2 + (N0 ? 1 : 0)) * 128);
}

__device__ __forceinline__ f32x16 qk(bf16x8 k1, s16x4 k2, bf16x8 q1, s16x4 q2) {
  f32x16 z;
#pragma unroll
  for (int i = 0; i < 16; ++i) z[i] = 0.f;
  f32x16 s = __builtin_amdgcn_mfma_f32_32x32x16_bf16(k1, q1, z, 0, 0, 0);
  return __builtin_amdgcn_mfma_f32_32x32x8bf16_1k(k2, q2, s, 0, 0, 0);
}
__device__ __forceinline__ float max16(f32x16 s) {
  float m = fmaxf(s[0], s[1]);
#pragma unroll
  for (int i = 2; i < 16; ++i) m = fmaxf(m, s[i]);
  return m;
}
__device__ __forceinline__ s16x4 mk2(unsigned a, unsigned b) { return __builtin_bit_cast(s16x4, u32x2{a, b}); }
__device__ __forceinline__ float swap32_max(float m) {
  const auto s = __builtin_amdgcn_permlane32_swap(__float_as_uint(m), __float_as_uint(m), false, false);
  return fmaxf(__uint_as_float(s[0]), __uint_as_float(s[1]));
}

#define ATTN_CLOB "v96","v97","v98","v99","v100","v101","v102","v103","v104","v105","v106","v107","v108","v109","v110","v111", \
  "v112","v113","v114","v115","v116","v117","v118","v119","v120","v121","v122","v123","v124","v125","v126","v127", \
  "v128","v129","v130","v131","v132","v133","v134","v135","v136","v137","v138","v139","v140","v141","v142","v143"
#define EX1(a) "v_exp_f32 v" #a ", v" #a "\n"
#define EX2(a, b) EX1(a) EX1(b)
#define EX3(a, b, c) EX1(a) EX1(b) EX1(c)
#define EX4(a, b, c, d) EX1(a) EX1(b) EX1(c) EX1(d)
#define ATILE(OVHI, OK1N, K2C, QB2, K2N, QA2, OVLON, X3, X4, X6, X7) \
  EX2(96, 97) \
  "v_smfmac_f32_32x32x32_bf16 %[ob], v[140:143], v[120:127], %[idx]\n" \
  "ds_read_b128 v[140:143], %[av] offset:" OVHI "\n" \
  EX3(98, 99, 100) \
  "v_mfma_f32_32x32x8_bf16 v[112:127], " K2C ", %[" QB2 "], 0\n" \
  "s_waitcnt lgkmcnt(1)\n" \
  EX4(101, 102, 103, 104) \
  "v_mfma_f32_32x32x16_bf16 v[112:127], v[128:131], %[qb1], v[112:127]\n" \
  "ds_read_b128 v[128:131], %[ak1] offset:" OK1N "\n" \
  X3 \
  EX4(105, 106, 107, 108) \
  "v_smfmac_f32_32x32x32_bf16 %[oa], v[136:139], v[96:103], %[idx]\n" \
  X4 \
  EX3(109, 110, 111) \
  EX2(112, 113) \
  "v_smfmac_f32_32x32x32_bf16 %[oa], v[140:143], v[104:111], %[idx]\n" \
  "s_waitcnt lgkmcnt(0)\n" \
  EX3(114, 115, 116) \
  "v_mfma_f32_32x32x8_bf16 v[96:111], " K2N ", %[" QA2 "], 0\n" \
  X6 \
  EX4(117, 118, 119, 120) \
  "v_mfma_f32_32x32x16_bf16 v[96:111], v[128:131], %[qa1], v[96:111]\n" \
  X7 \
  EX4(121, 122, 123, 124) \
  "v_smfmac_f32_32x32x32_bf16 %[ob], v[136:139], v[112:119], %[idx]\n" \
  "ds_read_b128 v[136:139], %[av] offset:" OVLON "\n" \
  EX3(125, 126, 127)
#define ITSTAMP
#define ITCLOB
#define WL2 "s_waitcnt lgkmcnt(2)\n"
#define WL1 "s_waitcnt lgkmcnt(1)\n"
#define SPIN(T) "s_cbranch_scc0 L_attn_go" T "_%=\n L_attn_spin" T "_%=:\n ds_read_b32 %[tmp], %[syn]\n s_add_i32 %[chk], %[chk], 16\n s_waitcnt lgkmcnt(0)\n" \
  " v_readfirstlane_b32 %[stmp], %[tmp]\n s_cmp_gt_u32 %[chk], 0x10000000\n s_cbranch_scc1 L_attn_go" T "_%=\n s_cmp_lt_u32 %[stmp], 4\n" \
  " s_cbranch_scc1 L_attn_spin" T "_%=\n L_attn_go" T "_%=:\n"
#define ASG(O, A6, A7, B6, B7) \
  ATILE(O "+640", O "+1024", "v[132:133]", "qb2e", "v[132:133]", "qa2o", O "+1280", "", WL1, "", "") \
  ATILE(O "+1920", O "+2048", "v[132:133]", "qb2o", "v[134:135]", "qa2e", O "+2560", "", WL1, "", "") \
  ATILE(O "+3200", O "+3072", "v[134:135]", "qb2e", "v[134:135]", "qa2o", O "+3840", "", WL1, A6, A7) \
  ATILE(O "+4480", O "+9744", "v[134:135]", "qb2o", "v[132:133]", "qa2e", O "+9744", \
        "ds_read_b128 v[132:135], %[ak2] offset:" O "+9744\n", WL2, B6, B7)
#define ATTN_LOOP_ASM \
  "ds_read_b128 v[128:131], %[ak1]\n" \
  "ds_read_b128 v[132:135], %[ak2]\n" \
  "ds_read_b128 v[136:139], %[av]\n" \
  "v_mov_b32 v140, 0\n v_mov_b32 v141, 0\n v_mov_b32 v142, 0\n v_mov_b32 v143, 0\n" \
  "v_mov_b32 v120, 0\n v_mov_b32 v121, 0\n v_mov_b32 v122, 0\n v_mov_b32 v123, 0\n" \
  "v_mov_b32 v124, 0\n v_mov_b32 v125, 0\n v_mov_b32 v126, 0\n v_mov_b32 v127, 0\n" \
  "s_waitcnt lgkmcnt(1)\n" \
  "v_mfma_f32_32x32x16_bf16 v[96:111], v[128:131], %[qa1], 0\n" \
  "v_mfma_f32_32x32x8_bf16 v[96:111], v[132:133], %[qa2e], v[96:111]\n" \
  "s_nop 11\n" \
  "L_attn_loop_%=:\n" ITSTAMP \
  ASG("0", "s_cmp_eq_u32 %[it], %[chk]\n", SPIN("a"), "s_add_i32 %[it], %[it], 1\n", "") \
  ASG("9744", "s_cmp_eq_u32 %[it], %[chk]\n", SPIN("b"), "s_add_i32 %[it], %[it], 1\n", "s_cmp_lt_i32 %[it], %[nit]\n") \
  "v_add_u32 %[ak1], 19488, %[ak1]\n" \
  "v_add_u32 %[ak2], 19488, %[ak2]\n" \
  "v_add_u32 %[av], 19488, %[av]\n" \
  "s_cbranch_scc1 L_attn_loop_%=\n" \
  "v_smfmac_f32_32x32x32_bf16 %[ob], v[140:143], v[120:127], %[idx]\n" \
  "s_waitcnt lgkmcnt(0)\n" \
  "s_nop 15\n"

__device__ __forceinline__ f32x16 attn_exact(__amdgpu_buffer_rsrc_t ir, int offK1, int offK2, int offV, int hh, bf16x8 q1, unsigned q2) {
  const s16x4 x2 = mk2(hh ? 0u : q2, 0u);
  float m = -INFINITY;
#pragma unroll 1
  for (int tile = 0; tile < 64; ++tile) {
    const int base = (tile >> 2) * SG_BYTES;
    const s16x4 kk = mk2(__builtin_amdgcn_raw_buffer_load_b32(ir, base + offK2 + (hh ? 0 : (tile & 3) * 4), 0, 16), 0u);
    const bf16x8 k1 = __builtin_bit_cast(bf16x8, __builtin_amdgcn_raw_buffer_load_b128(ir, base + offK1 + (tile & 3) * 1024, 0, 16));
    m = fmaxf(m, max16(qk(k1, kk, q1, x2)));
  }
  m = swap32_max(m);
  f32x16 O;
#pragma unroll
  for (int i = 0; i < 16; ++i) O[i] = 0.f;
#pragma unroll 1
  for (int tile = 0; tile < 64; ++tile) {
    const int base = (tile >> 2) * SG_BYTES;
    const s16x4 kk = mk2(__builtin_amdgcn_raw_buffer_load_b32(ir, base + offK2 + (hh ? 0 : (tile & 3) * 4), 0, 16), 0u);
    const bf16x8 k1 = __builtin_bit_cast(bf16x8, __builtin_amdgcn_raw_buffer_load_b128(ir, base + offK1 + (tile & 3) * 1024, 0, 16));
    const bf16x8 vlo = __builtin_bit_cast(bf16x8, __builtin_amdgcn_raw_buffer_load_b128(ir, base + offV + (tile & 3) * 1280, 0, 16));
    const bf16x8 vhi = __builtin_bit_cast(bf16x8, __builtin_amdgcn_raw_buffer_load_b128(ir, base + offV + (tile & 3) * 1280 + 640, 0, 16));
    f32x16 S = qk(k1, kk, q1, x2);
#pragma unroll
    for (int i = 0; i < 16; ++i) S[i] = __builtin_amdgcn_exp2f(S[i] - m);
    O = __builtin_amdgcn_smfmac_f32_32x32x32_bf16(vlo, __builtin_bit_cast(bf16x16, __builtin_shufflevector(S, S, 0, 1, 2, 3, 4, 5, 6, 7)), O, 0xDDDDDDDD, 0, 0);
    O = __builtin_amdgcn_smfmac_f32_32x32x32_bf16(vhi, __builtin_bit_cast(bf16x16, __builtin_shufflevector(S, S, 8, 9, 10, 11, 12, 13, 14, 15)), O, 0xDDDDDDDD, 0, 0);
  }
  return O;
}

#define EARLY_POST_PRIO 2
#define OUT_AUX 18
#define Q_SLEEP 4
#define KV_DELAY 0
#define O_SLEEP 4
#define I_SLEEP 6
#define NLATE 4
constexpr int T_QX = 16384;
constexpr int T_WO = 58624;
constexpr int T_XCH = T_WO + 73728;
constexpr int T_Z = 0;
constexpr int T_PART = 10240;
constexpr int T_EPI = 30720;
static_assert(NLATE % 2 == 0 && T_WO >= NLATE * SG_BYTES && T_XCH + 24576 <= T_SYNC && T_EPI + 12800 <= T_WO, "lds");
constexpr int P_EARLY = NLATE * SG_BYTES / 1024;
constexpr int NG_HI = (153 - (P_EARLY + 20) + 3) / 4;
static_assert(P_EARLY + 20 + NG_HI * 4 <= IMG_PIECES && 12 + (32 - NG_HI) * 4 >= P_EARLY && (P_EARLY + 20) * 1024 >= (NLATE + 2) * SG_BYTES, "dma plan");
static_assert(NG_HI >= 16 && T_QX >= 12288 && T_QX + 12288 <= P_EARLY * 1024, "q hand-over area");

__device__ __forceinline__ void img_group(const char* src, char* sm, int p) {
  const GLB_AS unsigned* g = (const GLB_AS unsigned*)(src + p * 1024);
  LDS_AS unsigned* l = (LDS_AS unsigned*)(sm + p * 1024);
  __builtin_amdgcn_global_load_lds(g, l, 16, 0, 16);
  __builtin_amdgcn_global_load_lds(g, l, 16, 1024, 16);
  __builtin_amdgcn_global_load_lds(g, l, 16, 2048, 16);
  __builtin_amdgcn_global_load_lds(g, l, 16, 3072, 16);
}

extern "C" __global__ void __launch_bounds__(768) k_main(const u16* __restrict__ x, const u16* __restrict__ wq,
                                               const u16* __restrict__ wk, const u16* __restrict__ wv,
                                               const u16* __restrict__ gw, const u16* __restrict__ bk,
                                               const u16* __restrict__ bv, const u16* __restrict__ shk, char* ws,
                                               const u16* __restrict__ hk, const u16* __restrict__ dk,
                                               const u16* __restrict__ wo, u16* __restrict__ out) {
  __shared__ __attribute__((aligned(256))) char sm[163840];
  const int tid = threadIdx.x, lane = tid & 63;
  const int wave = __builtin_amdgcn_readfirstlane(tid >> 6);
  const int b = blockIdx.x;
  const int grp = b & 7, loc = b >> 3;
  volatile LDS_AS unsigned* sync = (volatile LDS_AS unsigned*)(sm + T_SYNC);
  STAMP_DECL;
  STAMP(0);
  if (wave == 6 && loc < 26) code_prefetch(loc >= 18, lane);
  if (wave == 7) {
    const u64 e = __hip_atomic_load((GLB_AS u64*)(ws + WS_FLAG + FL_E), __ATOMIC_RELAXED, __HIP_MEMORY_SCOPE_AGENT) + 1u;
    if (lane < 16) sync[lane] = lane < 14 ? 0u : (lane == 14 ? (unsigned)e : (unsigned)(e >> 32));
  }
  if (loc < 18) q_tile(x, wq, ws, sm, grp, loc, wave, lane STAMP_PASS);
  else if (loc < 26) kvg_tile(x, wk, wv, gw, ws, sm, grp * 8 + loc - 18, tid, wave, lane);
  else { __builtin_amdgcn_s_sleep(KV_DELAY); u_tile(bk, bv, shk, ws, sm, grp, loc - 26, tid, wave, lane); }
  STAMP(13);

  const bool has_tile = loc < (grp < 2 ? 30 : 29);
  const int qid = has_tile ? grp * 29 + (grp < 2 ? grp : 2) + loc : 0;
  const int mt = qid / 9, nt = qid - mt * 9;
  const int t0 = mt * 80, n0 = nt * 64;
  const __amdgpu_buffer_rsrc_t orsrc = __builtin_amdgcn_make_buffer_rsrc((void*)(ws + WS_O), 0, (int)O_BYTES, 0x00020000);
  const u64 tag = lds_tag(sm);
  const int qb = ((grp + 4) & 7) * 32 + loc;
  const int cwi = wave - 2;
  const int kh = cwi >= 5 ? 1 : 0, cw = cwi - kh * 5;
  const int g = lane >> 4, tl = lane & 15;
  u32x2 xv[2]; float gt;
  auto load_xg = [&]() {
    const int te = min(t0 + max(cw, 0) * 16 + tl, S_LEN - 1);
#pragma unroll
    for (int h = 0; h < 2; ++h) xv[h] = *(const u32x2*)(x + (size_t)te * DM + n0 + kh * 32 + h * 16 + g * 4);
    gt = __uint_as_float(__builtin_amdgcn_raw_buffer_load_b32(__builtin_amdgcn_make_buffer_rsrc((void*)(ws + WS_G), 0, S_LEN * 4, 0x00020000), te * 4, 0, 16));
    asm volatile("" ::: "memory");
  };

  if (wave < 4) {
    __builtin_amdgcn_s_setprio(2);
    const int W = b < 208 ? b + 208 * wave : 4 * b + wave;
    lds_wait(sync + 8, 1u);
    STAMP(14);
    const char* src = ws + WS_IMG + lane * 16;
    {
      const int pa = P_EARLY + wave * 4, pb = wave == 0 ? P_EARLY + 16 : wave * 4 - 4;
#pragma unroll 1
      for (int spin = 0; spin < (1 << 20); ++spin) {
        img_group(src, sm, pa);
        img_group(src, sm, pb);
        wait_vm<0>();
        unsigned mx = 0;
#pragma unroll
        for (int i = 0; i < 8; ++i) {
          const u32x4 v = *(const u32x4*)(sm + ((i < 4 ? pa : pb - 4) + i) * 1024 + lane * 16);
          mx = max(max(mx, max(v[0], v[1])), max(v[2], v[3]));
        }
        if (__all(mx != O_EMPTY)) break;
        __builtin_amdgcn_s_sleep(I_SLEEP);
      }
    }
    __builtin_amdgcn_s_setprio(3);
    RAW_BARRIER();
    if (wave == 0) { flag_wait<2>(ws, FL_S + grp * 512 + lane * 8, tag); if (lane == 0) sync[7] = 1u; }
    else lds_wait(sync + 7, 1u);
#pragma unroll
    for (int i = 0; i < 8; ++i) {
      if (i == 4) lds_wait(sync + 10, 4u);
      int p = P_EARLY + 20 + (wave + 4 * i) * 4;
      p = p > 152 ? p - (P_EARLY + 20 + NG_HI * 4) + 12 : p;
      img_group(src, sm, p);
    }
    wait_vm<0>();
    if (lane == 0) __hip_atomic_fetch_add((LDS_AS unsigned*)sync + 5, 1u, __ATOMIC_RELAXED, __HIP_MEMORY_SCOPE_WORKGROUP);
    {
      const int c1 = W < 576 ? W : W - 576, c2 = (W >= 576 && W < 576 + 128) ? W - 128 : c1;
      flag_wait<8>(ws, FL_U + (((lane & 16) ? c2 : c1) >> 6) * 128 + (lane & 15) * 8, tag);
      const __amdgpu_buffer_rsrc_t us = __builtin_amdgcn_make_buffer_rsrc((void*)(ws + WS_UR), 0, DM * S_LEN * 2, 0x00020000);
      const __amdgpu_buffer_rsrc_t up = __builtin_amdgcn_make_buffer_rsrc((void*)(ws + WS_PS), 0, (int)(WS_G - WS_PS), 0x00020000);
      u32x4 ud[4]; unsigned un[4]; int upo[4];
#pragma unroll
      for (int q = 0; q < 4; ++q) {
        const int cc = q < 2 ? c1 : c2, blk = W < 576 ? q : 2 + (q & 1);
        const int uo = cc * (S_LEN * 2) + blk * 1024 + lane * 16;
        upo[q] = (cc * 2 + (W < 576 ? 1 : 0)) * (PS_COPY * 4) + blk * 1024 + lane * 16;
        ud[q] = __builtin_amdgcn_raw_buffer_load_b128(us, uo, 0, 16);
        un[q] = __builtin_amdgcn_raw_buffer_load_b32(us, uo + 16, 0, 16);
        if (blk == 3 && lane == 63) un[q] = 0u;
      }
#pragma unroll
      for (int q = 0; q < 4; ++q) {
        const u32x4 sh = {__builtin_amdgcn_alignbit(ud[q][1], ud[q][0], 16), __builtin_amdgcn_alignbit(ud[q][2], ud[q][1], 16),
                          __builtin_amdgcn_alignbit(ud[q][3], ud[q][2], 16), __builtin_amdgcn_alignbit(un[q], ud[q][3], 16)};
        __builtin_amdgcn_raw_buffer_store_b128(ud[q], up, upo[q], 0, PS_AUX);
        __builtin_amdgcn_raw_buffer_store_b128(sh, up, upo[q] + PS_COPY * 2, 0, PS_AUX);
        if ((q & 1) && lane < 4 && (q == 3 || W >= 576))
          __builtin_amdgcn_raw_buffer_store_b128(u32x4{0u, 0u, 0u, 0u}, up, upo[q] + 1024 + (lane >> 1) * (PS_COPY * 2 - 32), 0, PS_AUX);
      }
    }
    STAMP(1);
    if (wave == 3) {
#pragma unroll
      for (int i = 0; i < 9; ++i)
        __builtin_amdgcn_raw_buffer_store_b128(u32x4{O_EMPTY, O_EMPTY, O_EMPTY, O_EMPTY}, orsrc,
                                               ((qb >> 1) * 72 + i * 8 + (lane >> 3)) * 256 + (qb & 1) * 128 + (lane & 7) * 16, 0, 16);
      wait_vm<0>();
      if (lane == 0) { cnt_add(ws, (CNT_F + qb / 10) * 128); sync[6] = 1u; }
    }
    STAMP(9);
    if (W < 576) {
      ssm_unit<4>(W, hk, dk, ws, lane);
    } else {
      for (int c = W - 576; c < 576; c += 448) ssm_unit<0>(c, hk, dk, ws, lane);
    }
    if (b == 0 && wave == 1) {
      cnt_wait<8>(ws, (CNT_F + min(lane, 25)) * 128, lane >= 25 ? 6u : 10u);
      if (lane == 0) flag_set(ws, FL_E, tag + 1u);
    }
    load_xg();
    wait_vm<0>();
    STAMP(2);
  } else {
    const int pair = wave & 3;
    const bool early = wave < 8;
    __builtin_amdgcn_s_setprio(2);
    const int r = lane & 31, hh = lane >> 5;
    const int tq = qb * 8 + pair * 2;
    const int offK1 = SG_K1 + lane * 16;
    const int offK2 = SG_K2 + (hh ? 512 : r * 16);
    const int offV = SG_V + hh * 320 + vpos(r) * 16;
    const int idx = 0xDDDDDDDD;
    const unsigned ldsBase = (unsigned)(uintptr_t)(LDS_AS char*)sm + (early ? NLATE * SG_BYTES : 0);

    const __amdgpu_buffer_rsrc_t irsrc = __builtin_amdgcn_make_buffer_rsrc((void*)(ws + WS_IMG), 0, IMG_BYTES, 0x00020000);
    bf16x8 qa1, qb1; unsigned qa2, qb2;
    f32x16 Oa, Ob;
    {
#pragma unroll
      for (int i = 0; i < 16; ++i) { Oa[i] = 0.f; Ob[i] = 0.f; }
      unsigned aK1 = ldsBase + offK1, aK2 = ldsBase + offK2, aV = ldsBase + offV;
      asm volatile("" : "+v"(Oa), "+v"(Ob), "+v"(aK1), "+v"(aK2), "+v"(aV) :: "memory");
      const __amdgpu_buffer_rsrc_t qr = __builtin_amdgcn_make_buffer_rsrc((void*)(ws + WS_Q + (size_t)tq * (DM * 2)), 0, DM * 4, 0x00020000);
      bf16x8 k1; unsigned k2;
      auto load_q = [&]() {
#pragma unroll 1
        for (int spin = 0; spin < (1 << 22); ++spin) {
          asm volatile("" ::: "memory");
          const u32x4 ta = __builtin_amdgcn_raw_buffer_load_b128(qr, r * 36 + hh * 16, 0, 16);
          const u32x4 tb = __builtin_amdgcn_raw_buffer_load_b128(qr, r * 36 + hh * 16 + DM * 2, 0, 16);
          qa2 = __builtin_amdgcn_raw_buffer_load_b32(qr, r * 36 + 32, 0, 16);
          qb2 = __builtin_amdgcn_raw_buffer_load_b32(qr, r * 36 + 32 + DM * 2, 0, 16);
          const u32x4 tk = __builtin_amdgcn_raw_buffer_load_b128(irsrc, offK1, 0, 16);
          k2 = __builtin_amdgcn_raw_buffer_load_b32(irsrc, offK2, 0, 16);
          qa1 = __builtin_bit_cast(bf16x8, ta); qb1 = __builtin_bit_cast(bf16x8, tb); k1 = __builtin_bit_cast(bf16x8, tk);
          const unsigned mq = max(max(max(max(tk[0], tk[1]), max(tk[2], tk[3])), k2), max(max(max(max(ta[0], ta[1]), max(ta[2], ta[3])), max(max(tb[0], tb[1]), max(tb[2], tb[3]))), max(qa2, qb2)));
          if (__all(mq != O_EMPTY)) break;
          __builtin_amdgcn_s_sleep(Q_SLEEP);
        }
      };
      u32x4* qx = (u32x4*)(sm + T_QX + pair * 3072) + lane;
      unsigned pm;
      if (early) {
        if (wave >= 6) {
          flag_wait<4>(ws, wave == 6 ? FL_M + grp * 512 + lane * 8 : FL_Q + (qb >> 4) * 128 + min(lane, 8) * 8, tag);
          if (lane == 0) sync[wave == 6 ? 8 : 11] = 1u;
        }
        lds_wait(sync + 8, 1u);
        lds_wait(sync + 11, 1u);
        STAMP(14);
        load_q();
        const float ma = swap32_max(max16(qk(k1, mk2(k2, 0u), qa1, mk2(hh ? 0u : qa2, 0u))));
        const float mb = swap32_max(max16(qk(k1, mk2(k2, 0u), qb1, mk2(hh ? 0u : qb2, 0u))));
        pm = pack2(-ma, -mb);
        qx[0] = __builtin_bit_cast(u32x4, qa1); qx[64] = __builtin_bit_cast(u32x4, qb1); qx[128] = u32x4{qa2, qb2, pm, 0u};
        STAMP(9);
        RAW_BARRIER();
      } else {
        __builtin_amdgcn_s_setprio(3);
        if (loc >= 26) { u_store(ws, sm, grp, loc - 26, wave, lane); u_publish(ws, sm, grp, loc - 26, lane); }
        RAW_BARRIER();
        qa1 = __builtin_bit_cast(bf16x8, qx[0]); qb1 = __builtin_bit_cast(bf16x8, qx[64]);
        const u32x4 e = qx[128];
        qa2 = e[0]; qb2 = e[1]; pm = e[2];
        WAIT_LGKM0();
        if (lane == 0) __hip_atomic_fetch_add((LDS_AS unsigned*)sync + 10, 1u, __ATOMIC_RELAXED, __HIP_MEMORY_SCOPE_WORKGROUP);
        __builtin_amdgcn_s_setprio(LATE_PRIO);
      }
      STAMP(1); CSTAMP(10);
      const unsigned xa = hh ? 0u : qa2, xb = hh ? 0u : qb2;
      const unsigned nma = pm & 0xffffu, nmb = pm >> 16;
      const s16x4 qa2e = mk2(hh ? nma : qa2, 0u), qa2o = mk2(hh ? nma : 0u, xa);
      const s16x4 qb2e = mk2(hh ? nmb : qb2, 0u), qb2o = mk2(hh ? nmb : 0u, xb);
      int it = 0;
      const int nit = early ? 16 - NLATE : NLATE;
      int chk = 2 - (wave >> 2), stmp;
      unsigned tmp;
      const unsigned syn = (unsigned)(uintptr_t)(sync + 5);
      asm volatile(ATTN_LOOP_ASM
                   : [oa] "+v"(Oa), [ob] "+v"(Ob), [ak1] "+v"(aK1), [ak2] "+v"(aK2), [av] "+v"(aV), [it] "+s"(it),
                     [chk] "+s"(chk), [stmp] "=&s"(stmp), [tmp] "=&v"(tmp)
                   : [qa1] "v"(qa1), [qb1] "v"(qb1), [qa2e] "v"(qa2e), [qa2o] "v"(qa2o), [qb2e] "v"(qb2e), [qb2o] "v"(qb2o),
                     [idx] "v"(idx), [nit] "s"(nit), [syn] "v"(syn)
                   : "memory", "scc", ATTN_CLOB ITCLOB);
    }
    STAMP(2); CSTAMP(11);
    load_xg();
    f32x4* xch = (f32x4*)(sm + T_XCH + pair * 6144) + lane;
    if (early) {
      __builtin_amdgcn_s_setprio(EARLY_POST_PRIO);
      if (lane == 0) __hip_atomic_fetch_add((LDS_AS unsigned*)sync, 1u, __ATOMIC_RELAXED, __HIP_MEMORY_SCOPE_WORKGROUP);
      lds_wait(sync, 4u);
      lds_wait(sync + 6, 1u);
#pragma unroll
      for (int i = 0; i < 3; ++i) {
        xch[i * 64] = f32x4{Oa[4 * i], Oa[4 * i + 1], Oa[4 * i + 2], Oa[4 * i + 3]};
        xch[(3 + i) * 64] = f32x4{Ob[4 * i], Ob[4 * i + 1], Ob[4 * i + 2], Ob[4 * i + 3]};
      }
      WAIT_LGKM0();
      if (lane == 0) sync[1 + pair] = 1u;
      const char* wsrc = (const char*)(wo + n0) + (size_t)(pair * 144 + (lane >> 3)) * (DM * 2);
      const int c0 = ((lane & 7) ^ (wsw(lane >> 3) << 1)) << 4;
#pragma unroll
      for (int i = 0; i < 18; ++i) glds16(wsrc + i * 8 * DM * 2 + (c0 ^ ((i & 1) << 6)), sm + T_WO + pair * 18432 + i * 1024);
      if (wave == 4) cnt_wait<8>(ws, (CNT_F + mt) * 128, mt == 25 ? 6u : 10u);
      wait_vm<0>();
    } else {
      __builtin_amdgcn_s_setprio(3);
      lds_wait(sync + 1 + pair, 1u);
      float big = 0.f;
#pragma unroll
      for (int i = 0; i < 3; ++i) {
        const f32x4 pa = xch[i * 64], pb = xch[(3 + i) * 64];
#pragma unroll
        for (int e = 0; e < 4; ++e) if (4 * i + e < 11) {
          Oa[4 * i + e] += pa[e]; Ob[4 * i + e] += pb[e];
          big += fabsf(Oa[4 * i + e]) + fabsf(Ob[4 * i + e]);
        }
      }
      if (__any(!(big < 1e30f) || (!hh && !(fminf(Oa[10], Ob[10]) > 1e-12f)))) {
        Oa = attn_exact(irsrc, offK1, offK2, offV, hh, qa1, qa2);
        Ob = attn_exact(irsrc, offK1, offK2, offV, hh, qb1, qb2);
      }
      STAMP(3);
      const auto la = __builtin_amdgcn_permlane32_swap(__float_as_uint(Oa[10]), __float_as_uint(Oa[10]), false, false);
      const auto lb = __builtin_amdgcn_permlane32_swap(__float_as_uint(Ob[10]), __float_as_uint(Ob[10]), false, false);
      const float ia = __builtin_amdgcn_rcpf(__uint_as_float(la[0])), ib = __builtin_amdgcn_rcpf(__uint_as_float(lb[0]));
      char* ost = sm + T_XCH + pair * 6144;
      char* od = ost + r * 36 + hh * 8;
      *(unsigned*)(od) = pack2(Oa[0] * ia, Oa[1] * ia);        *(unsigned*)(od + 1168) = pack2(Ob[0] * ib, Ob[1] * ib);
      *(unsigned*)(od + 4) = pack2(Oa[2] * ia, Oa[3] * ia);    *(unsigned*)(od + 1172) = pack2(Ob[2] * ib, Ob[3] * ib);
      *(unsigned*)(od + 16) = pack2(Oa[4] * ia, Oa[5] * ia);   *(unsigned*)(od + 1184) = pack2(Ob[4] * ib, Ob[5] * ib);
      *(unsigned*)(od + 20) = pack2(Oa[6] * ia, Oa[7] * ia);   *(unsigned*)(od + 1188) = pack2(Ob[6] * ib, Ob[7] * ib);
      if (!hh) { *(unsigned*)(od + 32) = pack2(Oa[8] * ia, Oa[9] * ia); *(unsigned*)(od + 1200) = pack2(Ob[8] * ib, Ob[9] * ib); }
      u32x4 ov[3];
#pragma unroll
      for (int i = 0; i < 3; ++i) ov[i] = *(const u32x4*)(ost + (lane & 1) * 1168 + (i * 32 + (lane >> 1)) * 16);
#pragma unroll
      for (int i = 0; i < 3; ++i) {
#pragma unroll
        for (int e = 0; e < 4; ++e) ov[i][e] = min(ov[i][e], O_EMPTY - 1u);
        if (i < 2 || lane < 16)
          __builtin_amdgcn_raw_buffer_store_b128(ov[i], orsrc, (((tq >> 4) * 72 + i * 32 + (lane >> 1)) * 16 + (tq & 15) + (lane & 1)) * 16, 0, 16);
      }
    }
  }
  STAMP(4);
  if (!has_tile) { STAMP_FLUSH(1); return; }

  RAW_BARRIER();
  STAMP(5);
  if (wave < 2) {
    cnt_wait<2>(ws, (CNT_Z + nt * 2 + (((lane & 1) ? t0 : t0 + 79) < 1024 ? 0 : 1)) * 128, 64u);
    const __amdgpu_buffer_rsrc_t zrsrc = __builtin_amdgcn_make_buffer_rsrc((void*)(ws + WS_Z), 0, (int)Z_BYTES, 0x00020000);
    u32x4 zv[5];
#pragma unroll
    for (int i = 0; i < 5; ++i) {
      const int ci = (wave * 5 + i) * 64 + lane, c = ci / 10, k = ci - c * 10;
      zv[i] = __builtin_amdgcn_raw_buffer_load_b128(zrsrc, ((n0 + c) * S_LEN + t0 + k * 8) * 2, 0, 16);
    }
#pragma unroll
    for (int i = 0; i < 5; ++i) *(u32x4*)(sm + T_Z + ((wave * 5 + i) * 64 + lane) * 16) = zv[i];
    WAIT_LGKM0();
  } else {
    float bx[2][4];
    const float omg = 1.0f - gt;
#pragma unroll
    for (int h = 0; h < 2; ++h) {
      bx[h][0] = omg * bflo(xv[h][0]); bx[h][1] = omg * bfhi(xv[h][0]);
      bx[h][2] = omg * bflo(xv[h][1]); bx[h][3] = omg * bfhi(xv[h][1]);
    }
    const int rgp = mt * 5 + cw;
    const int obase = ((rgp * 18 + kh * 9) * 64 + lane) * 16;
    const char* wl = sm + T_WO + kh * 9 * 4096;
    constexpr int NPRE = 5;
    bf16x8 wpre[NPRE][4];
    int so[9];
#pragma unroll
    for (int s = 0; s < 9; ++s) so[s] = s + nt >= 9 ? s + nt - 9 : s + nt;
#pragma unroll
    for (int s = 0; s < NPRE; ++s)
#pragma unroll
      for (int nb = 0; nb < 4; ++nb) wpre[s][nb] = w_frag(wl + so[9 - NPRE + s] * 4096, lane, 0, nb);
    unsigned wq4[4];
#pragma unroll
    for (int nb = 0; nb < 4; ++nb) {
      const int k = g * 8 + (tl >> 2);
      wq4[nb] = (unsigned)(uintptr_t)(LDS_AS char*)sm + T_WO + kh * 9 * 4096 + k * 128 + ((nb ^ wsw(k)) << 5) + (lane & 3) * 8;
    }
    STAMP(6);
    f32x4 acc[4];
#pragma unroll 1
    for (int spin = 0; spin < (1 << 20); ++spin) {
      asm volatile("" : "+v"(wq4[0]), "+v"(wq4[1]), "+v"(wq4[2]), "+v"(wq4[3]) :: "memory");
      u32x4 of[9];
#pragma unroll
      for (int s = 0; s < 9; ++s) of[s] = __builtin_amdgcn_raw_buffer_load_b128(orsrc, obase, so[s] * 1024, 16);
      __builtin_amdgcn_sched_barrier(0);
#pragma unroll
      for (int nb = 0; nb < 4; ++nb) acc[nb] = f32x4{0.f, 0.f, 0.f, 0.f};
      unsigned mx = 0;
#pragma unroll
      for (int s = 0; s < 9; ++s) {
#pragma unroll
        for (int nb = 0; nb < 4; ++nb) {
          bf16x8 wf;
          if (s >= 9 - NPRE) wf = wpre[s - (9 - NPRE)][nb];
          else {
            LDS_AS bf16x4* wp = (LDS_AS bf16x4*)(uintptr_t)(wq4[nb] + so[s] * 4096);
            wf = __builtin_shufflevector(__builtin_amdgcn_ds_read_tr16_b64_v4bf16(wp), __builtin_amdgcn_ds_read_tr16_b64_v4bf16(wp + 64), 0, 1, 2, 3, 4, 5, 6, 7);
          }
          acc[nb] = MFMA16(wf, __builtin_bit_cast(bf16x8, of[s]), acc[nb]);
        }
        mx = max(max(mx, max(of[s][0], of[s][1])), max(of[s][2], of[s][3]));
      }
      if (__all(mx != O_EMPTY)) break;
      __builtin_amdgcn_s_sleep(O_SLEEP);
    }
    f32x4* pd = (f32x4*)(sm + T_PART + cwi * 2048) + lane;
    pd[0] = kh ? acc[0] : acc[2];
    pd[64] = kh ? acc[1] : acc[3];
    STAMP(7);
    RAW_BARRIER();
    const f32x4* ps = (const f32x4*)(sm + T_PART + (kh ? cw : cw + 5) * 2048) + lane;
    char* sE = sm + T_EPI + cwi * 1280;
    f32x4 pa[2]; u32x2 z[2];
#pragma unroll
    for (int h = 0; h < 2; ++h) {
      pa[h] = ps[h * 64];
      z[h] = __builtin_bit_cast(u32x2, tr_read(sm + T_Z + ((kh * 2 + h) * 16 + g * 4 + (tl >> 2)) * 160 + (cw * 16 + (tl & 3) * 4) * 2));
    }
    asm volatile("" ::: "memory");
#pragma unroll
    for (int h = 0; h < 2; ++h) {
      const f32x4 a = (kh ? acc[2 + h] : acc[h]) + pa[h];
      *(u32x2*)(sE + tl * 80 + h * 32 + g * 8) =
          u32x2{pack2(gt * a[0] + bx[h][0] * bflo(z[h][0]), gt * a[1] + bx[h][1] * bfhi(z[h][0])),
                pack2(gt * a[2] + bx[h][2] * bflo(z[h][1]), gt * a[3] + bx[h][3] * bfhi(z[h][1]))};
    }
    const u32x4 o = *(const u32x4*)(sE + (lane >> 2) * 80 + (lane & 3) * 16);
    const __amdgpu_buffer_rsrc_t outr = __builtin_amdgcn_make_buffer_rsrc((void*)out, 0, S_LEN * DM * 2, 0x00020000);
    __builtin_amdgcn_raw_buffer_store_b128(o, outr, ((t0 + cw * 16 + (lane >> 2)) * DM + n0 + kh * 32 + (lane & 3) * 8) * 2, 0, OUT_AUX);
  }
  STAMP(8);
  STAMP_FLUSH(1);
}

extern "C" __global__ void k9_end() {}

extern "C" void kernel_launch(void* const* d_in, const int* in_sizes, int n_in, void* d_out, int out_size,
                              void* d_ws, size_t ws_size, hipStream_t stream) {
  const u16* x = (const u16*)d_in[0];
  const u16* wq = (const u16*)d_in[1];
  const u16* wk = (const u16*)d_in[2];
  const u16* wv = (const u16*)d_in[3];
  const u16* wo = (const u16*)d_in[4];
  const u16* bk = (const u16*)d_in[6];
  const u16* bv = (const u16*)d_in[7];
  const u16* shk = (const u16*)d_in[8];
  const u16* hk = (const u16*)d_in[9];
  const u16* dk = (const u16*)d_in[10];
  const u16* gw = (const u16*)d_in[11];
  char* ws = (char*)d_ws;
  k_main<<<256, 768, 0, stream>>>(x, wq, wk, wv, gw, bk, bv, shk, ws, hk, dk, wo, (u16*)d_out);
}
```

```cpp
#include <hip/hip_runtime.h>
#include <hip/hip_bf16.h>
#include <stdint.h>

typedef __attribute__((ext_vector_type(4))) __bf16 bf16x4;
typedef __attribute__((ext_vector_type(8))) __bf16 bf16x8;
typedef __attribute__((ext_vector_type(16))) __bf16 bf16x16;
typedef __attribute__((ext_vector_type(4))) short s16x4;
typedef __attribute__((ext_vector_type(4))) float f32x4;
typedef __attribute__((ext_vector_type(16))) float f32x16;
typedef __attribute__((ext_vector_type(2))) unsigned u32x2;
typedef __attribute__((ext_vector_type(4))) unsigned u32x4;
typedef unsigned short u16;
typedef __attribute__((ext_vector_type(4), aligned(4))) unsigned u32x4_a4;

#define LDS_AS __attribute__((address_space(3)))
#define GLB_AS __attribute__((address_space(1)))

constexpr int S_LEN = 2048, DM = 576, DH = 18;
constexpr int PS_COPY = 2112;
constexpr int SG_K1 = 0, SG_K2 = 4096, SG_V = 4624, SG_BYTES = 9744;
constexpr int IMG_PIECES = 156;
constexpr int IMG_BYTES = IMG_PIECES * 1024;

constexpr size_t WS_CNT = 0;
constexpr size_t WS_FLAG = 8192;
constexpr int FL_E = 0, FL_Q = 128, FL_S = FL_Q + 16 * 128, FL_M = FL_S + 8 * 512, FL_U = FL_M + 8 * 512;
constexpr size_t WS_Q = 24576;
constexpr size_t WS_IMG = WS_Q + (size_t)S_LEN * DM * 2;
constexpr size_t WS_UR = WS_IMG + IMG_BYTES;
constexpr size_t WS_PS = WS_UR + (size_t)DM * S_LEN * 2;
constexpr size_t WS_G = WS_PS + (size_t)DM * 4 * PS_COPY * 2;
constexpr size_t WS_O = WS_G + S_LEN * 4;
constexpr size_t O_BYTES = (size_t)S_LEN * DM * 2;
constexpr size_t WS_Z = WS_O + O_BYTES;
constexpr size_t Z_BYTES = (size_t)S_LEN * DM * 2;
constexpr int CNT_Z = 0, CNT_F = 18;
constexpr unsigned O_EMPTY = 0xffffffffu;

#define WAIT_LGKM0() do { asm volatile("" ::: "memory"); __builtin_amdgcn_s_waitcnt(0xC07F); asm volatile("" ::: "memory"); } while (0)
#define RAW_BARRIER() do { WAIT_LGKM0(); __builtin_amdgcn_s_barrier(); asm volatile("" ::: "memory"); } while (0)
template <int N> __device__ __forceinline__ void wait_vm() {
  asm volatile("s_waitcnt vmcnt(%0)" :: "i"(N) : "memory");
  if (N == 0) __builtin_amdgcn_s_waitcnt(0x0070);
}

__device__ __forceinline__ float bflo(unsigned w) { return __uint_as_float(w << 16); }
__device__ __forceinline__ float bfhi(unsigned w) { return __uint_as_float(w & 0xffff0000u); }
__device__ __forceinline__ unsigned pack2(float lo, float hi) {
  typedef __attribute__((ext_vector_type(2))) __bf16 bf16x2;
  bf16x2 v; v[0] = (__bf16)lo; v[1] = (__bf16)hi;
  return __builtin_bit_cast(unsigned, v);
}
__device__ __forceinline__ float rbf(float x) { return (float)(__bf16)x; }
__device__ __forceinline__ void glds16(const void* g, void* lds) {
  __builtin_amdgcn_global_load_lds((const GLB_AS unsigned*)g, (LDS_AS unsigned*)lds, 16, 0, 0);
}
__device__ __forceinline__ bf16x4 tr_read(const char* p) {
  return __builtin_amdgcn_ds_read_tr16_b64_v4bf16((LDS_AS bf16x4*)p);
}
__device__ __forceinline__ int vpos(int d) {
  const int a = d - (d > 11 && d < 16);
  return d > 18 ? 15 : (d == 11 ? 19 : a);
}
#define MFMA16(a, b, c) __builtin_amdgcn_mfma_f32_16x16x32_bf16(a, b, c, 0, 0, 0)

__device__ __forceinline__ int wsw(int k) { return ((k >> 1) & 1) | ((k >> 2) & 2); }
__device__ __forceinline__ bf16x8 a_frag(const char* sA, int lane, int rg, int s) {
  const int row = rg * 16 + (lane & 15);
  return *(const bf16x8*)(sA + row * 128 + (((s * 4 + (lane >> 4)) ^ ((row >> 1) & 7)) << 4));
}
__device__ __forceinline__ bf16x8 w_frag(const char* sW, int lane, int s, int nb) {
  const int k = s * 32 + (lane >> 4) * 8 + ((lane & 15) >> 2);
  const char* b = sW + k * 128 + ((nb ^ wsw(k)) << 5) + (lane & 3) * 8;
  const bf16x4 lo = tr_read(b), hi = tr_read(b + 512);
  return __builtin_shufflevector(lo, hi, 0, 1, 2, 3, 4, 5, 6, 7);
}

template <int NP, int NSLOT, int SLOT, int DEPTH>
__device__ __forceinline__ void ring_loader(const char* const (&base)[NP], const int (&step)[NP], char* const (&dst)[NP], int rot) {
  auto issue = [&](int j) {
    int ch = j + rot; ch = ch >= 9 ? ch - 9 : ch;
#pragma unroll
    for (int i = 0; i < NP; ++i) glds16(base[i] + ch * step[i], dst[i] + ((j + 3) % NSLOT) * SLOT);
  };
#pragma unroll
  for (int j = 0; j < DEPTH; ++j) issue(j);
#define RL_STEP(j) do { wait_vm<NP * ((8 - (j)) < (DEPTH - 1) ? (8 - (j)) : (DEPTH - 1))>(); __builtin_amdgcn_s_barrier(); \
    asm volatile("" ::: "memory"); if ((j) + DEPTH < 9) issue((j) + DEPTH); } while (0)
  RL_STEP(0); RL_STEP(1); RL_STEP(2); RL_STEP(3); RL_STEP(4); RL_STEP(5); RL_STEP(6); RL_STEP(7); RL_STEP(8);
#undef RL_STEP
}

__device__ __forceinline__ void lds_wait(volatile LDS_AS unsigned* p, unsigned need) {
#pragma unroll 1
  for (int spin = 0; spin < (1 << 24); ++spin) {
    if (__builtin_amdgcn_readfirstlane(*p) >= need) break;
    __builtin_amdgcn_s_sleep(2);
  }
  asm volatile("" ::: "memory");
}

typedef unsigned long long u64;
__device__ __forceinline__ void flag_set(char* ws, int off, u64 tag) {
  __hip_atomic_store((GLB_AS u64*)(ws + WS_FLAG + off), tag, __ATOMIC_RELAXED, __HIP_MEMORY_SCOPE_AGENT);
}
template <int SLP>
__device__ __forceinline__ void flag_wait(char* ws, int off, u64 tag) {
  GLB_AS u64* fp = (GLB_AS u64*)(ws + WS_FLAG + off);
#pragma unroll 1
  for (int spin = 0; spin < (1 << 22); ++spin) {
    const u64 v = __hip_atomic_load(fp, __ATOMIC_RELAXED, __HIP_MEMORY_SCOPE_AGENT);
    if (__all(v == tag)) break;
    __builtin_amdgcn_s_sleep(SLP);
  }
  asm volatile("" ::: "memory");
}
__device__ __forceinline__ void code_prefetch(bool all, int lane) {
  u64 pc; unsigned d;
  asm volatile("s_getpc_b64 %0\nL_pf_%=:\n s_mov_b32 %1, k9_end-L_pf_%=" : "=s"(pc), "=s"(d));
  unsigned o[5], t[5];
#pragma unroll
  for (int i = 0; i < 5; ++i) o[i] = d - 4u - min((unsigned)(lane * 128 + i * 8192), all ? d - 4u : 12288u);
  asm volatile("global_load_dword %0, %5, %10\n global_load_dword %1, %6, %10\n global_load_dword %2, %7, %10\n"
               " global_load_dword %3, %8, %10\n global_load_dword %4, %9, %10\n s_waitcnt vmcnt(0)"
               : "=&v"(t[0]), "=&v"(t[1]), "=&v"(t[2]), "=&v"(t[3]), "=&v"(t[4])
               : "v"(o[0]), "v"(o[1]), "v"(o[2]), "v"(o[3]), "v"(o[4]), "s"(pc) : "memory");
}
constexpr int T_SYNC = 163840 - 64;
__device__ __forceinline__ u64 lds_tag(char* sm) { return *(volatile LDS_AS u64*)(sm + T_SYNC + 56); }

__device__ __forceinline__ void q_load(const u16* __restrict__ x, const u16* __restrict__ wq, char* sm, int grp, int loc, int wave, int lane) {
  const int mh = loc >= 9 ? 1 : 0, nt = loc - mh * 9;
  const int t0 = (grp * 2 + mh) * 128, n0 = nt * 64;
  constexpr int SLOT = 24576, NSLOT = 6;
  {
    int rot = nt + 4 * mh + grp; rot = rot >= 18 ? rot - 18 : (rot >= 9 ? rot - 9 : rot);
    const char* base[4]; int step[4]; char* dst[4];
#pragma unroll
    for (int i = 0; i < 4; ++i) {
      const int pc = wave + 6 * i;
      const bool isW = pc < 8;
      const int row = (isW ? pc : pc - 8) * 8 + (lane >> 3);
      const int sw = isW ? wsw(row) << 1 : (row >> 1) & 7;
      base[i] = (isW ? (const char*)(wq + n0) : (const char*)(x + (size_t)t0 * DM)) + (unsigned)row * (DM * 2) + (((lane & 7) ^ sw) << 4);
      step[i] = isW ? 64 * DM * 2 : 128;
      dst[i] = sm + pc * 1024;
    }
    ring_loader<4, NSLOT, SLOT, 3>(base, step, dst, rot);
  }
}
__device__ __forceinline__ void q_tile(char* ws, char* sm,
                                       int grp, int loc, int wave, int lane) {
  const int g = lane >> 4, tl = lane & 15;
  const int mh = loc >= 9 ? 1 : 0, nt = loc - mh * 9;
  const int t0 = (grp * 2 + mh) * 128, n0 = nt * 64;
  constexpr int SLOT = 24576;
  const __amdgpu_buffer_rsrc_t qr = __builtin_amdgcn_make_buffer_rsrc((void*)(ws + WS_Q), 0, S_LEN * DM * 2, 0x00020000);
  if (wave < 8) {
    RAW_BARRIER();
    if (wave == 7) {
#pragma unroll
      for (int i = 0; i < 4; ++i) {
#pragma unroll
        for (int k = 0; k < 4; ++k)
          __builtin_amdgcn_raw_buffer_store_b128(u32x4{O_EMPTY, O_EMPTY, O_EMPTY, O_EMPTY}, qr, ((t0 + (i * 4 + k) * 8 + (lane >> 3)) * DM + n0 + (lane & 7) * 8) * 2, 0, 16);
        RAW_BARRIER();
      }
      RAW_BARRIER(); RAW_BARRIER(); RAW_BARRIER();
      wait_vm<0>();
      if (lane == 0) flag_set(ws, FL_Q + (grp * 2 + mh) * 128 + nt * 8, lds_tag(sm));
      RAW_BARRIER();
    } else {
#pragma unroll
      for (int j = 1; j < 9; ++j) RAW_BARRIER();
    }
  } else {
    const int cwv = wave - 8;
    __builtin_amdgcn_s_setprio(3);
    f32x4 acc[2][4];
#pragma unroll
    for (int i = 0; i < 2; ++i)
#pragma unroll
      for (int nb = 0; nb < 4; ++nb) acc[i][nb] = f32x4{0.f, 0.f, 0.f, 0.f};
    bf16x8 af[2][4], wf[2][8];
    auto rd = [&](int j, bf16x8 (&a)[4], bf16x8 (&w)[8]) {
      const char* sl = sm + (j >= 3 ? j - 3 : j + 3) * SLOT;
#pragma unroll
      for (int s = 0; s < 2; ++s) {
#pragma unroll
        for (int i = 0; i < 2; ++i) a[s * 2 + i] = a_frag(sl + 8192, lane, cwv * 2 + i, s);
#pragma unroll
        for (int nb = 0; nb < 4; ++nb) w[s * 4 + nb] = w_frag(sl, lane, s, nb);
      }
    };
    auto mm = [&](bf16x8 (&a)[4], bf16x8 (&w)[8]) {
#pragma unroll
      for (int s = 0; s < 2; ++s)
#pragma unroll
        for (int nb = 0; nb < 4; ++nb)
#pragma unroll
          for (int i = 0; i < 2; ++i) acc[i][nb] = MFMA16(w[s * 4 + nb], a[s * 2 + i], acc[i][nb]);
    };
    auto mix = [&]() {
#pragma unroll
      for (int i = 0; i < 10; ++i) { __builtin_amdgcn_sched_group_barrier(0x8, 1, 0); __builtin_amdgcn_sched_group_barrier(0x100, 2, 0); }
      __builtin_amdgcn_sched_group_barrier(0x8, 6, 0);
      __builtin_amdgcn_sched_barrier(0);
    };
    RAW_BARRIER();
    rd(0, af[0], wf[0]);
#pragma unroll 1
    for (int j = 1; j < 9; j += 2) {
      RAW_BARRIER(); __builtin_amdgcn_sched_barrier(0); rd(j, af[1], wf[1]); mm(af[0], wf[0]); mix();
      RAW_BARRIER(); __builtin_amdgcn_sched_barrier(0); rd(j + 1, af[0], wf[0]); mm(af[1], wf[1]); mix();
    }
    mm(af[0], wf[0]);
    const float qscale = 0.23570226039551584f * 1.4426950408889634f;
    char* sE = sm + 4 * SLOT + cwv * 4608;
#pragma unroll
    for (int i = 0; i < 2; ++i)
#pragma unroll
      for (int nb = 0; nb < 4; ++nb) {
        const f32x4 a = acc[i][nb] * qscale;
        *(u32x2*)(sE + (i * 16 + tl) * 144 + nb * 32 + g * 8) =
            u32x2{min(pack2(a[0], a[1]), O_EMPTY - 1u), min(pack2(a[2], a[3]), O_EMPTY - 1u)};
      }
#pragma unroll
    for (int i = 0; i < 4; ++i) {
      const int row = i * 8 + (lane >> 3);
      const u32x4 v = *(const u32x4*)(sE + row * 144 + (lane & 7) * 16);
      __builtin_amdgcn_raw_buffer_store_b128(v, qr, ((t0 + cwv * 32 + row) * DM + n0 + (lane & 7) * 8) * 2, 0, 16);
    }
  }
}

__device__ __forceinline__ void kvg_tile(const u16* __restrict__ x, const u16* __restrict__ wk, const u16* __restrict__ wv,
                                         const u16* __restrict__ gw, char* ws, char* sm, int si, int tid, int wave, int lane) {
  const int g = lane >> 4, tl = lane & 15;
  const int t0 = si * 32;
  const __amdgpu_buffer_rsrc_t ir = __builtin_amdgcn_make_buffer_rsrc((void*)(ws + WS_IMG), 0, IMG_BYTES, 0x00020000);
  char* sW = sm + 36864;
  if (wave != 6 && wave != 7) {
    const int k = min(wave < 6 ? tid : tid - 128, 575);
    const u32x4 k0 = *(const u32x4_a4*)(wk + k * DH), k1 = *(const u32x4_a4*)(wk + k * DH + 8);
    const u32x4 v0 = *(const u32x4_a4*)(wv + k * DH), v1 = *(const u32x4_a4*)(wv + k * DH + 8);
    const unsigned k2 = *(const unsigned*)(wk + k * DH + 16), v2 = *(const unsigned*)(wv + k * DH + 16);
    const unsigned gk = gw[k];
    if (wave < 4) {
      const int row = wave * 8 + (lane >> 3);
      const char* xs = (const char*)x + (unsigned)(t0 + row) * (DM * 2) + (((lane & 7) ^ ((row >> 1) & 7)) << 4);
#pragma unroll
      for (int ch = 0; ch < 9; ++ch) glds16(xs + ch * 128, sm + ch * 4096 + wave * 1024);
    }
    char* d = sW + k * 128;
    const int sw = wsw(k);
    *(u32x4*)(d + (sw << 5)) = k0; *(u32x4*)(d + (sw << 5) + 16) = k1;
    *(u32x4*)(d + ((1 ^ sw) << 5)) = v0; *(u32x4*)(d + ((1 ^ sw) << 5) + 16) = v1;
    *(u32x4*)(d + ((2 ^ sw) << 5)) = u32x4{k2, gk, v2, 0u}; *(u32x4*)(d + ((2 ^ sw) << 5) + 16) = u32x4{0u, 0u, 0u, 0u};
  } else if (wave == 7) {
    const int img = (si >> 2) * SG_BYTES, tile = si & 3;
    __builtin_amdgcn_raw_buffer_store_b128(u32x4{O_EMPTY, O_EMPTY, O_EMPTY, O_EMPTY}, ir, img + SG_K1 + tile * 1024 + lane * 16, 0, 16);
#pragma unroll
    for (int i = 0; i < 2; ++i) {
      const int ch = i * 64 + lane, pos = ch % 20;
      const unsigned val = pos == 15 ? 0u : (pos == 18 ? 0x3f803f80u : O_EMPTY);
      if (ch < 80) __builtin_amdgcn_raw_buffer_store_b128(u32x4{val, val, val, val}, ir, img + SG_V + tile * 1280 + ch * 16, 0, 16);
    }
    if (lane < 33) __builtin_amdgcn_raw_buffer_store_b32(lane < 32 ? O_EMPTY : ((tile & 1) ? 0u : 0x3f80u), ir, img + SG_K2 + lane * 16 + tile * 4, 0, 16);
    if (si == 0) __hip_atomic_store((GLB_AS unsigned*)(ws + WS_CNT + lane * 128), 0u, __ATOMIC_RELAXED, __HIP_MEMORY_SCOPE_AGENT);
    wait_vm<0>();
    if (lane < 8) flag_set(ws, FL_M + lane * 512 + si * 8, lds_tag(sm));
  }
  if (wave != 7) wait_vm<0>();
  RAW_BARRIER();
  const int rg = wave & 1, kh = (wave >> 1) & 1, sub = wave >> 2;
  f32x4 acc = {0.f, 0.f, 0.f, 0.f};
  {
    bf16x8 fa[9], fw[9];
#pragma unroll
    for (int q = 0; q < 9; ++q) { fa[q] = a_frag(sm + q * 4096, lane, rg, kh); fw[q] = w_frag(sW + q * 8192, lane, kh, sub); }
    __builtin_amdgcn_sched_barrier(0);
    if (sub == 1) {
#pragma unroll
      for (int q = 0; q < 9; ++q) acc = MFMA16(fa[q], fw[q], acc);
    } else {
#pragma unroll
      for (int q = 0; q < 9; ++q) acc = MFMA16(fw[q], fa[q], acc);
    }
  }
  f32x4* ex = (f32x4*)(sm + 114688 + (sub * 2 + rg) * 1024) + lane;
  if (kh) *ex = acc;
  RAW_BARRIER();
  if (!kh) {
    acc += *ex;
    const int tw = t0 + rg * 16, t = tw + tl;
    const int img = (tw >> 7) * SG_BYTES, vimg = img + SG_V + ((tw >> 4) & 7) * 640;
    const int tile = (tw >> 5) & 3, r = t & 31;
    const unsigned p01 = min(pack2(acc[0], acc[1]), O_EMPTY - 1u), p23 = min(pack2(acc[2], acc[3]), O_EMPTY - 1u);
    if (sub == 0) {
      __builtin_amdgcn_raw_buffer_store_b64(u32x2{p01, p23}, ir, img + SG_K1 + tile * 1024 + (r + 32 * (g >> 1)) * 16 + (g & 1) * 8, 0, 16);
    } else if (sub == 1) {
      __builtin_amdgcn_raw_buffer_store_b64(u32x2{p01, p23}, ir, vimg + (g >> 1) * 320 + (g & 1) * 8 + vpos(tl) * 16, 0, 16);
    } else {
      const unsigned nb = __shfl_xor(p01, 1);
      const unsigned pv = min((tl & 1) ? (nb >> 16) | (p01 & 0xffff0000u) : (p01 & 0xffffu) | (nb << 16), O_EMPTY - 1u);
      if (g < 2)
        __builtin_amdgcn_raw_buffer_store_b32(g ? pv : p01, ir,
            g ? vimg + (tl >> 3) * 320 + (16 + (tl & 1)) * 16 + (tl & 6) * 2 : img + SG_K2 + r * 16 + tile * 4, 0, 16);
      if (g == 0) {
        const float lg = rbf(acc[2]);
        __hip_atomic_store((GLB_AS float*)(ws + WS_G) + t, rbf(__builtin_amdgcn_rcpf(1.0f + __expf(-lg))), __ATOMIC_RELAXED, __HIP_MEMORY_SCOPE_AGENT);
      }
    }
    wait_vm<0>();
  }
  RAW_BARRIER();
  if (tid < 8) flag_set(ws, FL_S + tid * 512 + si * 8, lds_tag(sm));
}

__device__ __forceinline__ void u_tile(const u16* __restrict__ bk, const u16* __restrict__ bv, const u16* __restrict__ shk,
                                       char* ws, char* sm, int grp, int j, int tid, int wave, int lane) {
  constexpr int UP = 776, UB = 98304;
  const int tA = grp * 256, tB = tA + (j & 1) * 128, cB = 6 + (j >> 1);
  {
    const bool sec = wave >= 8;
    const int cg = tid & 7, st = (tid >> 3) & 63;
    const int c0 = (sec ? cB : j) * 64 + cg * 8, ts = (sec ? tB : tA) + st * 4;
    u32x4 kr[7], vr[4], sk[4];
#pragma unroll
    for (int i = 0; i < 7; ++i) kr[i] = *(const u32x4*)(bk + (size_t)max(ts - 3 + i, 0) * DM + c0);
#pragma unroll
    for (int i = 0; i < 4; ++i) vr[i] = *(const u32x4*)(bv + (size_t)(ts + i) * DM + c0);
#pragma unroll
    for (int i = 0; i < 4; ++i) sk[i] = *(const u32x4*)(shk + c0 * 4 + i * 8);
    if (ts == 0) { kr[0] = u32x4{0u, 0u, 0u, 0u}; kr[1] = kr[0]; kr[2] = kr[0]; }
#pragma unroll
    for (int c = 0; c < 8; ++c) {
      const unsigned w01 = sk[c >> 1][(c & 1) * 2], w23 = sk[c >> 1][(c & 1) * 2 + 1];
      const float w0 = bflo(w01), w1 = bfhi(w01), w2 = bflo(w23), w3 = bfhi(w23);
      float kk[7], o[4];
#pragma unroll
      for (int i = 0; i < 7; ++i) kk[i] = (c & 1) ? bfhi(kr[i][c >> 1]) : bflo(kr[i][c >> 1]);
#pragma unroll
      for (int i = 0; i < 4; ++i) {
        const float a = kk[i] * w3 + kk[i + 1] * w2 + kk[i + 2] * w1 + kk[i + 3] * w0;
        o[i] = a * ((c & 1) ? bfhi(vr[i][c >> 1]) : bflo(vr[i][c >> 1]));
      }
      *(u32x2*)(sm + UB + (cg * 8 + c) * UP + ((sec ? 380 : 252) - st * 4) * 2) = u32x2{pack2(o[3], o[2]), pack2(o[1], o[0])};
    }
  }
  RAW_BARRIER();
}
__device__ __forceinline__ void u_store(char* ws, char* sm, int grp, int j, int wave, int lane) {
  constexpr int UP = 776, UB = 98304;
  const int tA = grp * 256, tB = tA + (j & 1) * 128, cB = 6 + (j >> 1);
  const __amdgpu_buffer_rsrc_t ur = __builtin_amdgcn_make_buffer_rsrc((void*)(ws + WS_UR), 0, DM * S_LEN * 2, 0x00020000);
  u32x4 v[12];
#pragma unroll
  for (int i = 0; i < 12; ++i) {
    const int row = (wave - 8) * 16 + (i < 8 ? i * 2 + (lane >> 5) : (i - 8) * 4 + (lane >> 4));
    const char* sp = sm + UB + row * UP + (i < 8 ? (lane & 31) * 16 : 512 + (lane & 15) * 16);
    const u32x2 lo = *(const u32x2*)sp, hi = *(const u32x2*)(sp + 8);
    v[i] = u32x4{lo[0], lo[1], hi[0], hi[1]};
  }
#pragma unroll
  for (int i = 0; i < 12; ++i) {
    const int row = (wave - 8) * 16 + (i < 8 ? i * 2 + (lane >> 5) : (i - 8) * 4 + (lane >> 4));
    const int off = i < 8 ? (j * 64 + row) * S_LEN + (S_LEN - 256 - tA) + (lane & 31) * 8 : (cB * 64 + row) * S_LEN + (S_LEN - 128 - tB) + (lane & 15) * 8;
    __builtin_amdgcn_raw_buffer_store_b128(v[i], ur, off * 2, 0, 16);
  }
}
__device__ __forceinline__ void u_publish(char* ws, char* sm, int grp, int j, int lane) {
  wait_vm<0>();
  unsigned n = 0;
  if (lane == 0) n = __hip_atomic_fetch_add((LDS_AS unsigned*)(sm + T_SYNC) + 9, 1u, __ATOMIC_RELAXED, __HIP_MEMORY_SCOPE_WORKGROUP);
  if (__builtin_amdgcn_readfirstlane(n) == 3u && lane < 3)
    flag_set(ws, FL_U + (lane < 2 ? j * 128 + (grp * 2 + lane) * 8 : (6 + (j >> 1)) * 128 + (grp * 2 + (j & 1)) * 8), lds_tag(sm));
}

__device__ __forceinline__ void cnt_add(char* ws, int byte_off) {
  __hip_atomic_fetch_add((GLB_AS unsigned*)(ws + WS_CNT + byte_off), 1u, __ATOMIC_RELAXED, __HIP_MEMORY_SCOPE_AGENT);
}
template <int SLP>
__device__ __forceinline__ void cnt_wait(char* ws, int byte_off, unsigned need) {
  GLB_AS unsigned* cp = (GLB_AS unsigned*)(ws + WS_CNT + byte_off);
#pragma unroll 1
  for (int spin = 0; spin < (1 << 22); ++spin) {
    const unsigned v = __hip_atomic_load(cp, __ATOMIC_RELAXED, __HIP_MEMORY_SCOPE_AGENT);
    if (__all(v >= need)) break;
    __builtin_amdgcn_s_sleep(SLP);
  }
  asm volatile("" ::: "memory");
}

template <int N0>
__device__ __forceinline__ void ssm_unit(int c, const u16* __restrict__ hk, const u16* __restrict__ dk,
                                         char* ws, int lane) {
  constexpr int NM = N0 + 4;
  const int i = lane & 15, g = lane >> 4;
  const __amdgpu_buffer_rsrc_t ur = __builtin_amdgcn_make_buffer_rsrc((void*)(ws + WS_PS + (size_t)(c * 2 + (N0 ? 1 : 0)) * (PS_COPY * 4)), 0,
                                                                      PS_COPY * 4, 0x00020000);
  int ao = (((i & 8) ? 0 : PS_COPY) + 2030 - 256 * (NM - 1) - 2 * (i & 7) + 8 * g) * 2;
  const __amdgpu_buffer_rsrc_t hr = __builtin_amdgcn_make_buffer_rsrc((void*)(hk + (size_t)c * S_LEN), 0, S_LEN * 2, 0x00020000);
  int bo = (16 * i - 16 + 8 * g) * 2, bo1 = bo + 512;
  asm volatile("" : "+v"(bo1), "+v"(ao));
  f32x4 acc[4];
#pragma unroll
  for (int n = 0; n < 4; ++n) acc[n] = f32x4{0.f, 0.f, 0.f, 0.f};
  u32x4 A[NM], B[NM];
  auto ldA = [&](int m) { A[m] = __builtin_amdgcn_raw_buffer_load_b128(ur, ao, 512 * (NM - 1 - m), 0); };
  auto ldB = [&](int j) {
    if (j == 0) B[0] = __builtin_amdgcn_raw_buffer_load_b128(hr, bo, 0, 0);
    else B[j] = __builtin_amdgcn_raw_buffer_load_b128(hr, bo1, 512 * (j - 1), 0);
  };
#pragma unroll
  for (int j = 0; j < NM; ++j) ldB(j);
  const float dd = bflo(dk[c]);
  wait_vm<0>();
  const unsigned tch = __builtin_amdgcn_raw_buffer_load_b32(ur, (lane >> 5) * (PS_COPY * 2) + (N0 ? (lane & 31) * 128 : 2048 + (lane & 31) * 64), 0, 0);
#pragma unroll
  for (int m = 0; m < NM; ++m) ldA(m);
  u32x2 u[4];
#pragma unroll
  for (int n = 0; n < 4; ++n) {
    int uo = (2044 - (256 * (N0 + n) + 16 * i + 8 * (g & 1) + 4 * (g >> 1))) * 2;
    asm volatile("" : "+v"(uo));
    u[n] = __builtin_amdgcn_raw_buffer_load_b64(ur, uo, 0, 0);
  }
  asm volatile("" :: "v"(tch));
  if (N0) {
#pragma unroll 1
    for (int rho = 0; rho < 8; ++rho) {
      if (rho == 2) __builtin_amdgcn_s_setprio(1);
      if (rho < 7) { ao -= 64; bo -= 64; bo1 -= 64; asm volatile("" : "+v"(ao), "+v"(bo), "+v"(bo1)); }
#pragma unroll
      for (int m = 0; m < NM; ++m) {
#pragma unroll
        for (int n = 3; n >= 0; --n)
          if (N0 + n - m >= 0) {
            acc[n] = MFMA16(__builtin_bit_cast(bf16x8, A[m]), __builtin_bit_cast(bf16x8, B[N0 + n - m]), acc[n]);
          }
        __builtin_amdgcn_sched_barrier(0);
        ldA(m); ldB(NM - 1 - m);
        __builtin_amdgcn_sched_barrier(0);
      }
    }
  } else {
    u32x4 A2[NM], B2[NM];
    auto round = [&](u32x4 (&Ac)[NM], u32x4 (&Bc)[NM], u32x4 (&An)[NM], u32x4 (&Bn)[NM]) {
#pragma unroll
      for (int m = 0; m < NM; ++m) {
        An[m] = __builtin_amdgcn_raw_buffer_load_b128(ur, ao, 512 * (NM - 1 - m), 0);
        Bn[m] = m == 0 ? __builtin_amdgcn_raw_buffer_load_b128(hr, bo, 0, 0) : __builtin_amdgcn_raw_buffer_load_b128(hr, bo1, 512 * (m - 1), 0);
      }
      __builtin_amdgcn_sched_barrier(0);
#pragma unroll
      for (int m = 0; m < NM; ++m)
#pragma unroll
        for (int n = 3; n >= m; --n) {
          acc[n] = MFMA16(__builtin_bit_cast(bf16x8, Ac[m]), __builtin_bit_cast(bf16x8, Bc[n - m]), acc[n]);
        }
      __builtin_amdgcn_sched_barrier(0);
    };
#pragma unroll 1
    for (int rho = 0; rho < 8; rho += 2) {
      if (rho == 2) __builtin_amdgcn_s_setprio(1);
      ao -= 64; bo -= 64; bo1 -= 64; asm volatile("" : "+v"(ao), "+v"(bo), "+v"(bo1));
      round(A, B, A2, B2);
      if (rho < 6) { ao -= 64; bo -= 64; bo1 -= 64; asm volatile("" : "+v"(ao), "+v"(bo), "+v"(bo1)); }
      round(A2, B2, A, B);
    }
  }
  __builtin_amdgcn_s_setprio(3);
  const __amdgpu_buffer_rsrc_t zr = __builtin_amdgcn_make_buffer_rsrc((void*)(ws + WS_Z + (size_t)c * (S_LEN * 2)), 0, S_LEN * 2, 0x00020000);
#pragma unroll
  for (int n = 0; n < 4; ++n) {
    const int tq = 256 * (N0 + n) + 16 * i + 8 * (g & 1) + 4 * (g >> 1);
    const auto s02 = __builtin_amdgcn_permlane32_swap(__float_as_uint(acc[n][0]), __float_as_uint(acc[n][2]), false, false);
    const auto s13 = __builtin_amdgcn_permlane32_swap(__float_as_uint(acc[n][1]), __float_as_uint(acc[n][3]), false, false);
    const float z0 = __uint_as_float(s02[0]) + bfhi(u[n][1]) * dd;
    const float z1 = __uint_as_float(s02[1]) + bflo(u[n][1]) * dd;
    const float z2 = __uint_as_float(s13[0]) + bfhi(u[n][0]) * dd;
    const float z3 = __uint_as_float(s13[1]) + bflo(u[n][0]) * dd;
    __builtin_amdgcn_raw_buffer_store_b64(u32x2{pack2(z0, z1), pack2(z2, z3)}, zr, tq * 2, 0, 16);
  }
  wait_vm<0>();
  if (lane == 0) cnt_add(ws, (CNT_Z + (c >> 6) * 2 + (N0 ? 1 : 0)) * 128);
}

__device__ __forceinline__ f32x16 qk(bf16x8 k1, s16x4 k2, bf16x8 q1, s16x4 q2) {
  f32x16 z;
#pragma unroll
  for (int i = 0; i < 16; ++i) z[i] = 0.f;
  f32x16 s = __builtin_amdgcn_mfma_f32_32x32x16_bf16(k1, q1, z, 0, 0, 0);
  return __builtin_amdgcn_mfma_f32_32x32x8bf16_1k(k2, q2, s, 0, 0, 0);
}
__device__ __forceinline__ float max16(f32x16 s) {
  float m = fmaxf(s[0], s[1]);
#pragma unroll
  for (int i = 2; i < 16; ++i) m = fmaxf(m, s[i]);
  return m;
}
__device__ __forceinline__ s16x4 mk2(unsigned a, unsigned b) { return __builtin_bit_cast(s16x4, u32x2{a, b}); }
__device__ __forceinline__ float swap32_max(float m) {
  const auto s = __builtin_amdgcn_permlane32_swap(__float_as_uint(m), __float_as_uint(m), false, false);
  return fmaxf(__uint_as_float(s[0]), __uint_as_float(s[1]));
}

#define ATTN_CLOB "v96","v97","v98","v99","v100","v101","v102","v103","v104","v105","v106","v107","v108","v109","v110","v111", \
  "v112","v113","v114","v115","v116","v117","v118","v119","v120","v121","v122","v123","v124","v125","v126","v127", \
  "v128","v129","v130","v131","v132","v133","v134","v135","v136","v137","v138","v139","v140","v141","v142","v143"
#define EX1(a) "v_exp_f32 v" #a ", v" #a "\n"
#define EX2(a, b) EX1(a) EX1(b)
#define EX3(a, b, c) EX1(a) EX1(b) EX1(c)
#define EX4(a, b, c, d) EX1(a) EX1(b) EX1(c) EX1(d)
#define ATILE(OVHI, OK1N, K2C, QB2, K2N, QA2, OVLON, X3, X4, X6, X7) \
  EX2(96, 97) \
  "v_smfmac_f32_32x32x32_bf16 %[ob], v[140:143], v[120:127], %[idx]\n" \
  "ds_read_b128 v[140:143], %[av] offset:" OVHI "\n" \
  EX3(98, 99, 100) \
  "v_mfma_f32_32x32x8_bf16 v[112:127], " K2C ", %[" QB2 "], 0\n" \
  "s_waitcnt lgkmcnt(1)\n" \
  EX4(101, 102, 103, 104) \
  "v_mfma_f32_32x32x16_bf16 v[112:127], v[128:131], %[qb1], v[112:127]\n" \
  "ds_read_b128 v[128:131], %[ak1] offset:" OK1N "\n" \
  X3 \
  EX4(105, 106, 107, 108) \
  "v_smfmac_f32_32x32x32_bf16 %[oa], v[136:139], v[96:103], %[idx]\n" \
  X4 \
  EX3(109, 110, 111) \
  EX2(112, 113) \
  "v_smfmac_f32_32x32x32_bf16 %[oa], v[140:143], v[104:111], %[idx]\n" \
  "s_waitcnt lgkmcnt(0)\n" \
  EX3(114, 115, 116) \
  "v_mfma_f32_32x32x8_bf16 v[96:111], " K2N ", %[" QA2 "], 0\n" \
  X6 \
  EX4(117, 118, 119, 120) \
  "v_mfma_f32_32x32x16_bf16 v[96:111], v[128:131], %[qa1], v[96:111]\n" \
  X7 \
  EX4(121, 122, 123, 124) \
  "v_smfmac_f32_32x32x32_bf16 %[ob], v[136:139], v[112:119], %[idx]\n" \
  "ds_read_b128 v[136:139], %[av] offset:" OVLON "\n" \
  EX3(125, 126, 127)
#define WL2 "s_waitcnt lgkmcnt(2)\n"
#define WL1 "s_waitcnt lgkmcnt(1)\n"
#define SPIN(T) "s_cbranch_scc0 L_attn_go" T "_%=\n L_attn_spin" T "_%=:\n s_sleep 2\n ds_read_b32 %[tmp], %[syn]\n s_add_i32 %[chk], %[chk], 16\n s_waitcnt lgkmcnt(0)\n" \
  " v_readfirstlane_b32 %[stmp], %[tmp]\n s_cmp_gt_u32 %[chk], 0x10000000\n s_cbranch_scc1 L_attn_go" T "_%=\n s_cmp_lt_u32 %[stmp], 4\n" \
  " s_cbranch_scc1 L_attn_spin" T "_%=\n L_attn_go" T "_%=:\n"
#define ASG(O, A6, A7, B6, B7) \
  ATILE(O "+640", O "+1024", "v[132:133]", "qb2e", "v[132:133]", "qa2o", O "+1280", "", WL1, "", "") \
  ATILE(O "+1920", O "+2048", "v[132:133]", "qb2o", "v[134:135]", "qa2e", O "+2560", "", WL1, "", "") \
  ATILE(O "+3200", O "+3072", "v[134:135]", "qb2e", "v[134:135]", "qa2o", O "+3840", "", WL1, A6, A7) \
  ATILE(O "+4480", O "+9744", "v[134:135]", "qb2o", "v[132:133]", "qa2e", O "+9744", \
        "ds_read_b128 v[132:135], %[ak2] offset:" O "+9744\n", WL2, B6, B7)
#define ATTN_LOOP_ASM \
  "ds_read_b128 v[128:131], %[ak1]\n" \
  "ds_read_b128 v[132:135], %[ak2]\n" \
  "ds_read_b128 v[136:139], %[av]\n" \
  "v_mov_b32 v140, 0\n v_mov_b32 v141, 0\n v_mov_b32 v142, 0\n v_mov_b32 v143, 0\n" \
  "v_mov_b32 v120, 0\n v_mov_b32 v121, 0\n v_mov_b32 v122, 0\n v_mov_b32 v123, 0\n" \
  "v_mov_b32 v124, 0\n v_mov_b32 v125, 0\n v_mov_b32 v126, 0\n v_mov_b32 v127, 0\n" \
  "s_waitcnt lgkmcnt(1)\n" \
  "v_mfma_f32_32x32x16_bf16 v[96:111], v[128:131], %[qa1], 0\n" \
  "v_mfma_f32_32x32x8_bf16 v[96:111], v[132:133], %[qa2e], v[96:111]\n" \
  "s_nop 11\n" \
  "L_attn_loop_%=:\n" \
  ASG("0", "s_cmp_eq_u32 %[it], %[chk]\n", SPIN("a"), "s_add_i32 %[it], %[it], 1\n", "") \
  ASG("9744", "s_cmp_eq_u32 %[it], %[chk]\n", SPIN("b"), "s_add_i32 %[it], %[it], 1\n", "s_cmp_lt_i32 %[it], %[nit]\n") \
  "v_add_u32 %[ak1], 19488, %[ak1]\n" \
  "v_add_u32 %[ak2], 19488, %[ak2]\n" \
  "v_add_u32 %[av], 19488, %[av]\n" \
  "s_cbranch_scc1 L_attn_loop_%=\n" \
  "v_smfmac_f32_32x32x32_bf16 %[ob], v[140:143], v[120:127], %[idx]\n" \
  "s_waitcnt lgkmcnt(0)\n" \
  "s_nop 15\n"

__device__ __forceinline__ f32x16 attn_exact(__amdgpu_buffer_rsrc_t ir, int offK1, int offK2, int offV, int hh, bf16x8 q1, unsigned q2) {
  const s16x4 x2 = mk2(hh ? 0u : q2, 0u);
  float m = -INFINITY;
#pragma unroll 1
  for (int tile = 0; tile < 64; ++tile) {
    const int base = (tile >> 2) * SG_BYTES;
    const s16x4 kk = mk2(__builtin_amdgcn_raw_buffer_load_b32(ir, base + offK2 + (hh ? 0 : (tile & 3) * 4), 0, 16), 0u);
    const bf16x8 k1 = __builtin_bit_cast(bf16x8, __builtin_amdgcn_raw_buffer_load_b128(ir, base + offK1 + (tile & 3) * 1024, 0, 16));
    m = fmaxf(m, max16(qk(k1, kk, q1, x2)));
  }
  m = swap32_max(m);
  f32x16 O;
#pragma unroll
  for (int i = 0; i < 16; ++i) O[i] = 0.f;
#pragma unroll 1
  for (int tile = 0; tile < 64; ++tile) {
    const int base = (tile >> 2) * SG_BYTES;
    const s16x4 kk = mk2(__builtin_amdgcn_raw_buffer_load_b32(ir, base + offK2 + (hh ? 0 : (tile & 3) * 4), 0, 16), 0u);
    const bf16x8 k1 = __builtin_bit_cast(bf16x8, __builtin_amdgcn_raw_buffer_load_b128(ir, base + offK1 + (tile & 3) * 1024, 0, 16));
    const bf16x8 vlo = __builtin_bit_cast(bf16x8, __builtin_amdgcn_raw_buffer_load_b128(ir, base + offV + (tile & 3) * 1280, 0, 16));
    const bf16x8 vhi = __builtin_bit_cast(bf16x8, __builtin_amdgcn_raw_buffer_load_b128(ir, base + offV + (tile & 3) * 1280 + 640, 0, 16));
    f32x16 S = qk(k1, kk, q1, x2);
#pragma unroll
    for (int i = 0; i < 16; ++i) S[i] = __builtin_amdgcn_exp2f(S[i] - m);
    O = __builtin_amdgcn_smfmac_f32_32x32x32_bf16(vlo, __builtin_bit_cast(bf16x16, __builtin_shufflevector(S, S, 0, 1, 2, 3, 4, 5, 6, 7)), O, 0xDDDDDDDD, 0, 0);
    O = __builtin_amdgcn_smfmac_f32_32x32x32_bf16(vhi, __builtin_bit_cast(bf16x16, __builtin_shufflevector(S, S, 8, 9, 10, 11, 12, 13, 14, 15)), O, 0xDDDDDDDD, 0, 0);
  }
  return O;
}

constexpr int NLATE = 4;
constexpr int T_QX = 16384;
constexpr int T_WO = 58624;
constexpr int T_XCH = T_WO + 73728;
constexpr int T_Z = 0;
constexpr int T_PART = 10240;
constexpr int T_EPI = 30720;
static_assert(NLATE % 2 == 0 && T_WO >= NLATE * SG_BYTES && T_XCH + 24576 <= T_SYNC && T_EPI + 12800 <= T_WO, "lds");
constexpr int P_EARLY = NLATE * SG_BYTES / 1024;
constexpr int NG_HI = (153 - (P_EARLY + 20) + 3) / 4;
static_assert(P_EARLY + 20 + NG_HI * 4 <= IMG_PIECES && 12 + (32 - NG_HI) * 4 >= P_EARLY && (P_EARLY + 20) * 1024 >= (NLATE + 2) * SG_BYTES, "dma plan");
static_assert(NG_HI >= 16 && T_QX >= 12288 && T_QX + 12288 <= P_EARLY * 1024, "q hand-over area");

__device__ __forceinline__ void img_group(const char* src, char* sm, int p) {
  const GLB_AS unsigned* g = (const GLB_AS unsigned*)(src + p * 1024);
  LDS_AS unsigned* l = (LDS_AS unsigned*)(sm + p * 1024);
  __builtin_amdgcn_global_load_lds(g, l, 16, 0, 16);
  __builtin_amdgcn_global_load_lds(g, l, 16, 1024, 16);
  __builtin_amdgcn_global_load_lds(g, l, 16, 2048, 16);
  __builtin_amdgcn_global_load_lds(g, l, 16, 3072, 16);
}

extern "C" __global__ void __launch_bounds__(768, 3) k_main(const u16* __restrict__ x, const u16* __restrict__ wq,
                                               const u16* __restrict__ wk, const u16* __restrict__ wv,
                                               const u16* __restrict__ gw, const u16* __restrict__ bk,
                                               const u16* __restrict__ bv, const u16* __restrict__ shk, char* ws,
                                               const u16* __restrict__ hk, const u16* __restrict__ dk,
                                               const u16* __restrict__ wo, u16* __restrict__ out) {
  __shared__ __attribute__((aligned(256))) char sm[163840];
  const int tid = threadIdx.x, lane = tid & 63;
  const int wave = __builtin_amdgcn_readfirstlane(tid >> 6);
  const int b = blockIdx.x;
  const int grp = b & 7, loc = b >> 3;
  volatile LDS_AS unsigned* sync = (volatile LDS_AS unsigned*)(sm + T_SYNC);
  if (wave == 6 && loc < 26) code_prefetch(loc >= 18, lane);
  if (__builtin_expect(loc < 18 && wave < 6, 1)) {
    q_load(x, wq, sm, grp, loc, wave, lane);
  } else {
    if (wave == 7) {
      const u64 e = __hip_atomic_load((GLB_AS u64*)(ws + WS_FLAG + FL_E), __ATOMIC_RELAXED, __HIP_MEMORY_SCOPE_AGENT) + 1u;
      if (lane < 16) sync[lane] = lane < 14 ? 0u : (lane == 14 ? (unsigned)e : (unsigned)(e >> 32));
    }
    if (loc < 18) q_tile(ws, sm, grp, loc, wave, lane);
    else if (loc < 26) kvg_tile(x, wk, wv, gw, ws, sm, grp * 8 + loc - 18, tid, wave, lane);
    else u_tile(bk, bv, shk, ws, sm, grp, loc - 26, tid, wave, lane);
  }

  const bool has_tile = loc < (grp < 2 ? 30 : 29);
  const int qid = has_tile ? grp * 29 + (grp < 2 ? grp : 2) + loc : 0;
  const int mt = qid / 9, nt = qid - mt * 9;
  const int t0 = mt * 80, n0 = nt * 64;
  const __amdgpu_buffer_rsrc_t orsrc = __builtin_amdgcn_make_buffer_rsrc((void*)(ws + WS_O), 0, (int)O_BYTES, 0x00020000);
  const u64 tag = lds_tag(sm);
  const int qb = ((grp + 4) & 7) * 32 + loc;
  const int cwi = wave - 2;
  const int kh = cwi >= 5 ? 1 : 0, cw = cwi - kh * 5;
  const int g = lane >> 4, tl = lane & 15;
  u32x2 xv[2]; float gt;
  auto load_xg = [&]() {
    const int te = min(t0 + max(cw, 0) * 16 + tl, S_LEN - 1);
#pragma unroll
    for (int h = 0; h < 2; ++h) xv[h] = *(const u32x2*)(x + (size_t)te * DM + n0 + kh * 32 + h * 16 + g * 4);
    gt = __uint_as_float(__builtin_amdgcn_raw_buffer_load_b32(__builtin_amdgcn_make_buffer_rsrc((void*)(ws + WS_G), 0, S_LEN * 4, 0x00020000), te * 4, 0, 16));
    asm volatile("" ::: "memory");
  };

  if (wave < 4) {
    __builtin_amdgcn_s_setprio(2);
    const int W = b < 208 ? b + 208 * wave : 4 * b + wave;
    lds_wait(sync + 8, 1u);
    const char* src = ws + WS_IMG + lane * 16;
    {
      const int pa = P_EARLY + wave * 4, pb = wave == 0 ? P_EARLY + 16 : wave * 4 - 4;
#pragma unroll 1
      for (int spin = 0; spin < (1 << 20); ++spin) {
        img_group(src, sm, pa);
        img_group(src, sm, pb);
        wait_vm<0>();
        unsigned mx = 0;
#pragma unroll
        for (int i = 0; i < 8; ++i) {
          const u32x4 v = *(const u32x4*)(sm + ((i < 4 ? pa : pb - 4) + i) * 1024 + lane * 16);
          mx = max(max(mx, max(v[0], v[1])), max(v[2], v[3]));
        }
        if (__all(mx != O_EMPTY)) break;
        __builtin_amdgcn_s_sleep(6);
      }
    }
    __builtin_amdgcn_s_setprio(3);
    RAW_BARRIER();
    if (wave == 0) { flag_wait<2>(ws, FL_S + grp * 512 + lane * 8, tag); if (lane == 0) sync[7] = 1u; }
    else lds_wait(sync + 7, 1u);
#pragma unroll
    for (int i = 0; i < 8; ++i) {
      if (i == 4) lds_wait(sync + 10, 4u);
      int p = P_EARLY + 20 + (wave + 4 * i) * 4;
      p = p > 152 ? p - (P_EARLY + 20 + NG_HI * 4) + 12 : p;
      img_group(src, sm, p);
    }
    wait_vm<0>();
    if (lane == 0) __hip_atomic_fetch_add((LDS_AS unsigned*)sync + 5, 1u, __ATOMIC_RELAXED, __HIP_MEMORY_SCOPE_WORKGROUP);
    {
      const int c1 = W < 576 ? W : W - 576, c2 = (W >= 576 && W < 576 + 128) ? W - 128 : c1;
      flag_wait<8>(ws, FL_U + (((lane & 16) ? c2 : c1) >> 6) * 128 + (lane & 15) * 8, tag);
      const __amdgpu_buffer_rsrc_t us = __builtin_amdgcn_make_buffer_rsrc((void*)(ws + WS_UR), 0, DM * S_LEN * 2, 0x00020000);
      const __amdgpu_buffer_rsrc_t up = __builtin_amdgcn_make_buffer_rsrc((void*)(ws + WS_PS), 0, (int)(WS_G - WS_PS), 0x00020000);
      u32x4 ud[4]; unsigned un[4]; int upo[4];
#pragma unroll
      for (int q = 0; q < 4; ++q) {
        const int cc = q < 2 ? c1 : c2, blk = W < 576 ? q : 2 + (q & 1);
        const int uo = cc * (S_LEN * 2) + blk * 1024 + lane * 16;
        upo[q] = (cc * 2 + (W < 576 ? 1 : 0)) * (PS_COPY * 4) + blk * 1024 + lane * 16;
        ud[q] = __builtin_amdgcn_raw_buffer_load_b128(us, uo, 0, 16);
        un[q] = __builtin_amdgcn_raw_buffer_load_b32(us, uo + 16, 0, 16);
        if (blk == 3 && lane == 63) un[q] = 0u;
      }
#pragma unroll
      for (int q = 0; q < 4; ++q) {
        const u32x4 sh = {__builtin_amdgcn_alignbit(ud[q][1], ud[q][0], 16), __builtin_amdgcn_alignbit(ud[q][2], ud[q][1], 16),
                          __builtin_amdgcn_alignbit(ud[q][3], ud[q][2], 16), __builtin_amdgcn_alignbit(un[q], ud[q][3], 16)};
        __builtin_amdgcn_raw_buffer_store_b128(ud[q], up, upo[q], 0, 16);
        __builtin_amdgcn_raw_buffer_store_b128(sh, up, upo[q] + PS_COPY * 2, 0, 16);
        if ((q & 1) && lane < 4 && (q == 3 || W >= 576))
          __builtin_amdgcn_raw_buffer_store_b128(u32x4{0u, 0u, 0u, 0u}, up, upo[q] + 1024 + (lane >> 1) * (PS_COPY * 2 - 32), 0, 16);
      }
    }
    if (wave == 3) {
#pragma unroll
      for (int i = 0; i < 9; ++i)
        __builtin_amdgcn_raw_buffer_store_b128(u32x4{O_EMPTY, O_EMPTY, O_EMPTY, O_EMPTY}, orsrc,
                                               ((qb >> 1) * 72 + i * 8 + (lane >> 3)) * 256 + (qb & 1) * 128 + (lane & 7) * 16, 0, 16);
      wait_vm<0>();
      if (lane == 0) { cnt_add(ws, (CNT_F + qb / 10) * 128); sync[6] = 1u; }
    }
    if (W < 576) {
      ssm_unit<4>(W, hk, dk, ws, lane);
    } else {
      for (int c = W - 576; c < 576; c += 448) ssm_unit<0>(c, hk, dk, ws, lane);
    }
    if (b == 0 && wave == 1) {
      cnt_wait<8>(ws, (CNT_F + min(lane, 25)) * 128, lane >= 25 ? 6u : 10u);
      if (lane == 0) flag_set(ws, FL_E, tag + 1u);
    }
    load_xg();
    wait_vm<0>();
  } else {
    const int pair = wave & 3;
    const bool early = wave < 8;
    __builtin_amdgcn_s_setprio(2);
    const int r = lane & 31, hh = lane >> 5;
    const int tq = qb * 8 + pair * 2;
    const int offK1 = SG_K1 + lane * 16;
    const int offK2 = SG_K2 + (hh ? 512 : r * 16);
    const int offV = SG_V + hh * 320 + vpos(r) * 16;
    const int idx = 0xDDDDDDDD;
    const unsigned ldsBase = (unsigned)(uintptr_t)(LDS_AS char*)sm + (early ? NLATE * SG_BYTES : 0);

    const __amdgpu_buffer_rsrc_t irsrc = __builtin_amdgcn_make_buffer_rsrc((void*)(ws + WS_IMG), 0, IMG_BYTES, 0x00020000);
    bf16x8 qa1, qb1; unsigned qa2, qb2;
    f32x16 Oa, Ob;
    {
#pragma unroll
      for (int i = 0; i < 16; ++i) { Oa[i] = 0.f; Ob[i] = 0.f; }
      unsigned aK1 = ldsBase + offK1, aK2 = ldsBase + offK2, aV = ldsBase + offV;
      asm volatile("" : "+v"(Oa), "+v"(Ob), "+v"(aK1), "+v"(aK2), "+v"(aV) :: "memory");
      const __amdgpu_buffer_rsrc_t qr = __builtin_amdgcn_make_buffer_rsrc((void*)(ws + WS_Q + (size_t)tq * (DM * 2)), 0, DM * 4, 0x00020000);
      bf16x8 k1; unsigned k2;
      auto load_q = [&]() {
#pragma unroll 1
        for (int spin = 0; spin < (1 << 22); ++spin) {
          asm volatile("" ::: "memory");
          const u32x4 ta = __builtin_amdgcn_raw_buffer_load_b128(qr, r * 36 + hh * 16, 0, 16);
          const u32x4 tb = __builtin_amdgcn_raw_buffer_load_b128(qr, r * 36 + hh * 16 + DM * 2, 0, 16);
          qa2 = __builtin_amdgcn_raw_buffer_load_b32(qr, r * 36 + 32, 0, 16);
          qb2 = __builtin_amdgcn_raw_buffer_load_b32(qr, r * 36 + 32 + DM * 2, 0, 16);
          const u32x4 tk = __builtin_amdgcn_raw_buffer_load_b128(irsrc, offK1, 0, 16);
          k2 = __builtin_amdgcn_raw_buffer_load_b32(irsrc, offK2, 0, 16);
          qa1 = __builtin_bit_cast(bf16x8, ta); qb1 = __builtin_bit_cast(bf16x8, tb); k1 = __builtin_bit_cast(bf16x8, tk);
          const unsigned mq = max(max(max(max(tk[0], tk[1]), max(tk[2], tk[3])), k2), max(max(max(max(ta[0], ta[1]), max(ta[2], ta[3])), max(max(tb[0], tb[1]), max(tb[2], tb[3]))), max(qa2, qb2)));
          if (__all(mq != O_EMPTY)) break;
          __builtin_amdgcn_s_sleep(4);
        }
      };
      u32x4* qx = (u32x4*)(sm + T_QX + pair * 3072) + lane;
      unsigned pm;
      if (early) {
        if (wave >= 6) {
          flag_wait<4>(ws, wave == 6 ? FL_M + grp * 512 + lane * 8 : FL_Q + (qb >> 4) * 128 + min(lane, 8) * 8, tag);
          if (lane == 0) sync[wave == 6 ? 8 : 11] = 1u;
        }
        lds_wait(sync + 8, 1u);
        lds_wait(sync + 11, 1u);
        load_q();
        const float ma = swap32_max(max16(qk(k1, mk2(k2, 0u), qa1, mk2(hh ? 0u : qa2, 0u))));
        const float mb = swap32_max(max16(qk(k1, mk2(k2, 0u), qb1, mk2(hh ? 0u : qb2, 0u))));
        pm = pack2(-ma, -mb);
        qx[0] = __builtin_bit_cast(u32x4, qa1); qx[64] = __builtin_bit_cast(u32x4, qb1); qx[128] = u32x4{qa2, qb2, pm, 0u};
        RAW_BARRIER();
      } else {
        __builtin_amdgcn_s_setprio(3);
        if (loc >= 26) { u_store(ws, sm, grp, loc - 26, wave, lane); u_publish(ws, sm, grp, loc - 26, lane); }
        RAW_BARRIER();
        qa1 = __builtin_bit_cast(bf16x8, qx[0]); qb1 = __builtin_bit_cast(bf16x8, qx[64]);
        const u32x4 e = qx[128];
        qa2 = e[0]; qb2 = e[1]; pm = e[2];
        WAIT_LGKM0();
        if (lane == 0) __hip_atomic_fetch_add((LDS_AS unsigned*)sync + 10, 1u, __ATOMIC_RELAXED, __HIP_MEMORY_SCOPE_WORKGROUP);
        __builtin_amdgcn_s_setprio(0);
      }
      const unsigned xa = hh ? 0u : qa2, xb = hh ? 0u : qb2;
      const unsigned nma = pm & 0xffffu, nmb = pm >> 16;
      const s16x4 qa2e = mk2(hh ? nma : qa2, 0u), qa2o = mk2(hh ? nma : 0u, xa);
      const s16x4 qb2e = mk2(hh ? nmb : qb2, 0u), qb2o = mk2(hh ? nmb : 0u, xb);
      int it = 0;
      const int nit = early ? 16 - NLATE : NLATE;
      int chk = 2 - (wave >> 2), stmp;
      unsigned tmp;
      const unsigned syn = (unsigned)(uintptr_t)(sync + 5);
      asm volatile(ATTN_LOOP_ASM
                   : [oa] "+v"(Oa), [ob] "+v"(Ob), [ak1] "+v"(aK1), [ak2] "+v"(aK2), [av] "+v"(aV), [it] "+s"(it),
                     [chk] "+s"(chk), [stmp] "=&s"(stmp), [tmp] "=&v"(tmp)
                   : [qa1] "v"(qa1), [qb1] "v"(qb1), [qa2e] "v"(qa2e), [qa2o] "v"(qa2o), [qb2e] "v"(qb2e), [qb2o] "v"(qb2o),
                     [idx] "v"(idx), [nit] "s"(nit), [syn] "v"(syn)
                   : "memory", "scc", ATTN_CLOB);
    }
    load_xg();
    f32x4* xch = (f32x4*)(sm + T_XCH + pair * 6144) + lane;
    if (early) {
      __builtin_amdgcn_s_setprio(2);
      if (lane == 0) __hip_atomic_fetch_add((LDS_AS unsigned*)sync, 1u, __ATOMIC_RELAXED, __HIP_MEMORY_SCOPE_WORKGROUP);
      lds_wait(sync, 4u);
      lds_wait(sync + 6, 1u);
#pragma unroll
      for (int i = 0; i < 3; ++i) {
        xch[i * 64] = f32x4{Oa[4 * i], Oa[4 * i + 1], Oa[4 * i + 2], Oa[4 * i + 3]};
        xch[(3 + i) * 64] = f32x4{Ob[4 * i], Ob[4 * i + 1], Ob[4 * i + 2], Ob[4 * i + 3]};
      }
      WAIT_LGKM0();
      if (lane == 0) sync[1 + pair] = 1u;
      const char* wsrc = (const char*)(wo + n0) + (size_t)(pair * 144 + (lane >> 3)) * (DM * 2);
      const int c0 = ((lane & 7) ^ (wsw(lane >> 3) << 1)) << 4;
#pragma unroll
      for (int i = 0; i < 18; ++i) glds16(wsrc + i * 8 * DM * 2 + (c0 ^ ((i & 1) << 6)), sm + T_WO + pair * 18432 + i * 1024);
      if (wave == 4) cnt_wait<8>(ws, (CNT_F + mt) * 128, mt == 25 ? 6u : 10u);
      wait_vm<0>();
    } else {
      __builtin_amdgcn_s_setprio(3);
      lds_wait(sync + 1 + pair, 1u);
      float big = 0.f;
#pragma unroll
      for (int i = 0; i < 3; ++i) {
        const f32x4 pa = xch[i * 64], pb = xch[(3 + i) * 64];
#pragma unroll
        for (int e = 0; e < 4; ++e) if (4 * i + e < 11) {
          Oa[4 * i + e] += pa[e]; Ob[4 * i + e] += pb[e];
          big += fabsf(Oa[4 * i + e]) + fabsf(Ob[4 * i + e]);
        }
      }
      if (__any(!(big < 1e30f) || (!hh && !(fminf(Oa[10], Ob[10]) > 1e-12f)))) {
        Oa = attn_exact(irsrc, offK1, offK2, offV, hh, qa1, qa2);
        Ob = attn_exact(irsrc, offK1, offK2, offV, hh, qb1, qb2);
      }
      const auto la = __builtin_amdgcn_permlane32_swap(__float_as_uint(Oa[10]), __float_as_uint(Oa[10]), false, false);
      const auto lb = __builtin_amdgcn_permlane32_swap(__float_as_uint(Ob[10]), __float_as_uint(Ob[10]), false, false);
      const float ia = __builtin_amdgcn_rcpf(__uint_as_float(la[0])), ib = __builtin_amdgcn_rcpf(__uint_as_float(lb[0]));
      char* ost = sm + T_XCH + pair * 6144;
      char* od = ost + r * 36 + hh * 8;
      *(unsigned*)(od) = pack2(Oa[0] * ia, Oa[1] * ia);        *(unsigned*)(od + 1168) = pack2(Ob[0] * ib, Ob[1] * ib);
      *(unsigned*)(od + 4) = pack2(Oa[2] * ia, Oa[3] * ia);    *(unsigned*)(od + 1172) = pack2(Ob[2] * ib, Ob[3] * ib);
      *(unsigned*)(od + 16) = pack2(Oa[4] * ia, Oa[5] * ia);   *(unsigned*)(od + 1184) = pack2(Ob[4] * ib, Ob[5] * ib);
      *(unsigned*)(od + 20) = pack2(Oa[6] * ia, Oa[7] * ia);   *(unsigned*)(od + 1188) = pack2(Ob[6] * ib, Ob[7] * ib);
      if (!hh) { *(unsigned*)(od + 32) = pack2(Oa[8] * ia, Oa[9] * ia); *(unsigned*)(od + 1200) = pack2(Ob[8] * ib, Ob[9] * ib); }
      u32x4 ov[3];
#pragma unroll
      for (int i = 0; i < 3; ++i) ov[i] = *(const u32x4*)(ost + (lane & 1) * 1168 + (i * 32 + (lane >> 1)) * 16);
#pragma unroll
      for (int i = 0; i < 3; ++i) {
#pragma unroll
        for (int e = 0; e < 4; ++e) ov[i][e] = min(ov[i][e], O_EMPTY - 1u);
        if (i < 2 || lane < 16)
          __builtin_amdgcn_raw_buffer_store_b128(ov[i], orsrc, (((tq >> 4) * 72 + i * 32 + (lane >> 1)) * 16 + (tq & 15) + (lane & 1)) * 16, 0, 16);
      }
    }
  }
  if (!has_tile) return;

  RAW_BARRIER();
  if (wave < 2) {
    cnt_wait<2>(ws, (CNT_Z + nt * 2 + (((lane & 1) ? t0 : t0 + 79) < 1024 ? 0 : 1)) * 128, 64u);
    const __amdgpu_buffer_rsrc_t zrsrc = __builtin_amdgcn_make_buffer_rsrc((void*)(ws + WS_Z), 0, (int)Z_BYTES, 0x00020000);
    u32x4 zv[5];
#pragma unroll
    for (int i = 0; i < 5; ++i) {
      const int ci = (wave * 5 + i) * 64 + lane, c = ci / 10, k = ci - c * 10;
      zv[i] = __builtin_amdgcn_raw_buffer_load_b128(zrsrc, ((n0 + c) * S_LEN + t0 + k * 8) * 2, 0, 16);
    }
#pragma unroll
    for (int i = 0; i < 5; ++i) *(u32x4*)(sm + T_Z + ((wave * 5 + i) * 64 + lane) * 16) = zv[i];
    WAIT_LGKM0();
  } else {
    float bx[2][4];
    const float omg = 1.0f - gt;
#pragma unroll
    for (int h = 0; h < 2; ++h) {
      bx[h][0] = omg * bflo(xv[h][0]); bx[h][1] = omg * bfhi(xv[h][0]);
      bx[h][2] = omg * bflo(xv[h][1]); bx[h][3] = omg * bfhi(xv[h][1]);
    }
    const int rgp = mt * 5 + cw;
    const int obase = ((rgp * 18 + kh * 9) * 64 + lane) * 16;
    const char* wl = sm + T_WO + kh * 9 * 4096;
    constexpr int NPRE = 5;
    bf16x8 wpre[NPRE][4];
    int so[9];
#pragma unroll
    for (int s = 0; s < 9; ++s) so[s] = s + nt >= 9 ? s + nt - 9 : s + nt;
#pragma unroll
    for (int s = 0; s < NPRE; ++s)
#pragma unroll
      for (int nb = 0; nb < 4; ++nb) wpre[s][nb] = w_frag(wl + so[9 - NPRE + s] * 4096, lane, 0, nb);
    unsigned wq4[4];
#pragma unroll
    for (int nb = 0; nb < 4; ++nb) {
      const int k = g * 8 + (tl >> 2);
      wq4[nb] = (unsigned)(uintptr_t)(LDS_AS char*)sm + T_WO + kh * 9 * 4096 + k * 128 + ((nb ^ wsw(k)) << 5) + (lane & 3) * 8;
    }
    f32x4 acc[4];
#pragma unroll 1
    for (int spin = 0; spin < (1 << 20); ++spin) {
      asm volatile("" : "+v"(wq4[0]), "+v"(wq4[1]), "+v"(wq4[2]), "+v"(wq4[3]) :: "memory");
      u32x4 of[9];
#pragma unroll
      for (int s = 0; s < 9; ++s) of[s] = __builtin_amdgcn_raw_buffer_load_b128(orsrc, obase, so[s] * 1024, 16);
      __builtin_amdgcn_sched_barrier(0);
#pragma unroll
      for (int nb = 0; nb < 4; ++nb) acc[nb] = f32x4{0.f, 0.f, 0.f, 0.f};
      unsigned mx = 0;
#pragma unroll
      for (int s = 0; s < 9; ++s) {
#pragma unroll
        for (int nb = 0; nb < 4; ++nb) {
          bf16x8 wf;
          if (s >= 9 - NPRE) wf = wpre[s - (9 - NPRE)][nb];
          else {
            LDS_AS bf16x4* wp = (LDS_AS bf16x4*)(uintptr_t)(wq4[nb] + so[s] * 4096);
            wf = __builtin_shufflevector(__builtin_amdgcn_ds_read_tr16_b64_v4bf16(wp), __builtin_amdgcn_ds_read_tr16_b64_v4bf16(wp + 64), 0, 1, 2, 3, 4, 5, 6, 7);
          }
          acc[nb] = MFMA16(wf, __builtin_bit_cast(bf16x8, of[s]), acc[nb]);
        }
        mx = max(max(mx, max(of[s][0], of[s][1])), max(of[s][2], of[s][3]));
      }
      if (__all(mx != O_EMPTY)) break;
      __builtin_amdgcn_s_sleep(4);
    }
    f32x4* pd = (f32x4*)(sm + T_PART + cwi * 2048) + lane;
    pd[0] = kh ? acc[0] : acc[2];
    pd[64] = kh ? acc[1] : acc[3];
    RAW_BARRIER();
    const f32x4* ps = (const f32x4*)(sm + T_PART + (kh ? cw : cw + 5) * 2048) + lane;
    char* sE = sm + T_EPI + cwi * 1280;
    f32x4 pa[2]; u32x2 z[2];
#pragma unroll
    for (int h = 0; h < 2; ++h) {
      pa[h] = ps[h * 64];
      z[h] = __builtin_bit_cast(u32x2, tr_read(sm + T_Z + ((kh * 2 + h) * 16 + g * 4 + (tl >> 2)) * 160 + (cw * 16 + (tl & 3) * 4) * 2));
    }
    asm volatile("" ::: "memory");
#pragma unroll
    for (int h = 0; h < 2; ++h) {
      const f32x4 a = (kh ? acc[2 + h] : acc[h]) + pa[h];
      *(u32x2*)(sE + tl * 80 + h * 32 + g * 8) =
          u32x2{pack2(gt * a[0] + bx[h][0] * bflo(z[h][0]), gt * a[1] + bx[h][1] * bfhi(z[h][0])),
                pack2(gt * a[2] + bx[h][2] * bflo(z[h][1]), gt * a[3] + bx[h][3] * bfhi(z[h][1]))};
    }
    const u32x4 o = *(const u32x4*)(sE + (lane >> 2) * 80 + (lane & 3) * 16);
    const __amdgpu_buffer_rsrc_t outr = __builtin_amdgcn_make_buffer_rsrc((void*)out, 0, S_LEN * DM * 2, 0x00020000);
    __builtin_amdgcn_raw_buffer_store_b128(o, outr, ((t0 + cw * 16 + (lane >> 2)) * DM + n0 + kh * 32 + (lane & 3) * 8) * 2, 0, 16);
  }
}

extern "C" __global__ void k9_end() {}

extern "C" void kernel_launch(void* const* d_in, const int* in_sizes, int n_in, void* d_out, int out_size,
                              void* d_ws, size_t ws_size, hipStream_t stream) {
  const u16* x = (const u16*)d_in[0];
  const u16* wq = (const u16*)d_in[1];
  const u16* wk = (const u16*)d_in[2];
  const u16* wv = (const u16*)d_in[3];
  const u16* wo = (const u16*)d_in[4];
  const u16* bk = (const u16*)d_in[6];
  const u16* bv = (const u16*)d_in[7];
  const u16* shk = (const u16*)d_in[8];
  const u16* hk = (const u16*)d_in[9];
  const u16* dk = (const u16*)d_in[10];
  const u16* gw = (const u16*)d_in[11];
  char* ws = (char*)d_ws;
  k_main<<<256, 768, 0, stream>>>(x, wq, wk, wv, gw, bk, bv, shk, ws, hk, dk, wo, (u16*)d_out);
}
```
